```python
import torch

def _softmax(x, axis=-1):
    return torch.softmax(x, dim=axis)
def _gelu(x):
    return torch.nn.functional.gelu(x, approximate="tanh")
def _silu(x):
    return torch.nn.functional.silu(x)
def _sigmoid(x):
    return torch.sigmoid(x)
def _layernorm(x, axis=-1, eps=1e-5):
    m = x.mean(dim=axis, keepdim=True)
    v = x.var(dim=axis, keepdim=True, unbiased=False)
    return (x - m) / torch.sqrt(v + eps)
def _scatter_add(tgt, idx, src, axis=0):
    return tgt.clone().index_add_(axis, idx, src)
def _segment_sum(src, seg, n):
    out = torch.zeros((n,) + src.shape[1:], dtype=src.dtype)
    return out.index_add_(0, seg, src)
def _pad(x, pw):
    flat = tuple(v for pair in reversed(pw) for v in pair)
    return torch.nn.functional.pad(x, flat)

kernel_name = 'gated_mix__linear_attn_block__glu_gated_bilinear__prompt'

def setup_inputs():

    _w = (lambda a: (lambda t: t.to(torch.bfloat16) if t.is_floating_point() else t)(torch.as_tensor(a)))
    def _A_setup():
        B, S, D = 1, 2048, 512
        return {
            "x": _w((lambda *s: torch.randn(*s, dtype=torch.bfloat16) * 0.05)(B, S, D)),
            "wqkv": _w((lambda *s: torch.randn(*s, dtype=torch.bfloat16) * 0.8)(D, 3 * D)),
            "wo": _w((lambda *s: torch.randn(*s, dtype=torch.bfloat16) * 0.05)(D, D)),
        }
    _A_in = _A_setup()

    def _B_setup():
        B, S, D, F = 1, 2048, 512, 1792
        return {
            "x": _w((lambda *s: torch.randn(*s, dtype=torch.bfloat16) * 0.05)(B, S, D)),
            "w_gate": _w((lambda *s: torch.randn(*s, dtype=torch.bfloat16) * 1.6)(D, F) * (D ** -0.5)),
            "w_up": _w((lambda *s: torch.randn(*s, dtype=torch.bfloat16) * 1.6)(D, F) * (D ** -0.5)),
            "w_down": _w((lambda *s: torch.randn(*s, dtype=torch.bfloat16) * 1.6)(F, D) * (F ** -0.5)),
        }
    _B_in = _B_setup()

    return {
        "A_x": _A_in["x"],
        "A_wqkv": _A_in["wqkv"],
        "A_wo": _A_in["wo"],
        "B_x": _B_in["x"],
        "B_w_gate": _B_in["w_gate"],
        "B_w_up": _B_in["w_up"],
        "B_w_down": _B_in["w_down"],
        "gate_w": _w((lambda *s: torch.randn(*s, dtype=torch.bfloat16) * 0.05)(512) * 16.0),
    }

def reference(A_x, A_wqkv, A_wo, B_x, B_w_gate, B_w_up, B_w_down, gate_w):
    def _A_ref(x, wqkv, wo):
        B, S, D = x.shape
        H = 32; Dh = D // H
        qkv = (lambda x, *d: x.permute(*d))((x @ wqkv).reshape(B, S, 3, H, Dh), 2, 0, 3, 1, 4)
        q, k, v = qkv[0], qkv[1], qkv[2]
        phi = lambda t: torch.where(t > 0, t + 1, torch.exp(t)) + 1e-6
        pq, pk = phi(q), phi(k)
        kv = torch.einsum("bhsd,bhse->bhde", pk, v)
        z = torch.einsum("bhsd->bhd", pk)
        num = torch.einsum("bhqd,bhde->bhqe", pq, kv)
        den = torch.einsum("bhqd,bhd->bhq", pq, z)[..., None] + 1e-6
        o = num / den
        return (lambda x, *d: x.permute(*d))(o, 0, 2, 1, 3).reshape(B, S, D) @ wo

    def _B_ref(x, w_gate, w_up, w_down):
        g = torch.einsum("bsd,df->bsf", x, w_gate)
        u = torch.einsum("bsd,df->bsf", x, w_up)
        h = g * u
        return torch.einsum("bsf,fd->bsd", h, w_down)

    x = A_x
    g = _sigmoid((x * gate_w).sum(-1, keepdims=True))
    a = _A_ref(x, A_wqkv, A_wo)
    b = _B_ref(x, B_w_gate, B_w_up, B_w_down)
    return g * a + (1 - g) * b
```

```cpp
#include <hip/hip_runtime.h>
#include <hip/hip_bf16.h>
#include <stdint.h>

typedef unsigned short u16;
__device__ __forceinline__ float bf2f(u16 v){ return __uint_as_float(((uint32_t)v)<<16); }
__device__ __forceinline__ u16 f2bf(float f){ uint32_t u=__float_as_uint(f); u += 0x7fff + ((u>>16)&1); return (u16)(u>>16); }
__device__ __forceinline__ float rb(float f){ return bf2f(f2bf(f)); }

__global__ void gemm_naive(const u16* A, int lda, const u16* B, int ldb, float* C, int ldc, int K){
  __shared__ float As[16][65];
  __shared__ float Bs[16][64];
  int tx=threadIdx.x&15, ty=threadIdx.x>>4;
  int m0=blockIdx.y*64, n0=blockIdx.x*64;
  float acc[4][4]={};
  for(int k0=0;k0<K;k0+=16){
    for(int i=threadIdx.x;i<64*16;i+=256){ int m=i>>4,k=i&15; As[k][m]=bf2f(A[(size_t)(m0+m)*lda+k0+k]); }
    for(int i=threadIdx.x;i<64*16;i+=256){ int k=i>>6,n=i&63; Bs[k][n]=bf2f(B[(size_t)(k0+k)*ldb+n0+n]); }
    __syncthreads();
    for(int k=0;k<16;k++){
      float a[4],b[4];
      for(int i=0;i<4;i++) a[i]=As[k][ty*4+i];
      for(int j=0;j<4;j++) b[j]=Bs[k][tx*4+j];
      for(int i=0;i<4;i++) for(int j=0;j<4;j++) acc[i][j]+=a[i]*b[j];
    }
    __syncthreads();
  }
  for(int i=0;i<4;i++) for(int j=0;j<4;j++) C[(size_t)(m0+ty*4+i)*ldc+n0+tx*4+j]=acc[i][j];
}
__device__ __forceinline__ float phi(float t){ float r = t>0 ? rb(t+1.f) : rb(__expf(t)); return rb(r+1e-6f); }

__global__ void kv_kernel(const float* qkv, float* kv, float* z){
  int h=blockIdx.x; int d=threadIdx.x>>4, e=threadIdx.x&15;
  float acc=0, zz=0;
  for(int s=0;s<2048;s++){
    float pk=phi(rb(qkv[(size_t)s*1536+512+h*16+d]));
    float v=rb(qkv[(size_t)s*1536+1024+h*16+e]);
    acc+=pk*v; zz+=pk;
  }
  kv[h*256+d*16+e]=rb(acc);
  if(e==0) z[h*16+d]=rb(zz);
}
__global__ void o_kernel(const float* qkv, const float* kv, const float* z, u16* o){
  int idx=blockIdx.x*256+threadIdx.x;
  int s=idx>>9, c=idx&511, h=c>>4, e=c&15;
  float num=0, den=0;
  for(int d=0;d<16;d++){ float pq=phi(rb(qkv[(size_t)s*1536+h*16+d])); num+=pq*kv[h*256+d*16+e]; den+=pq*z[h*16+d]; }
  num=rb(num); den=rb(rb(den)+1e-6f);
  o[idx]=f2bf(num/den);
}
__global__ void h_kernel(const float* g, const float* u, u16* h){
  int idx=blockIdx.x*256+threadIdx.x;
  h[idx]=f2bf(rb(g[idx])*rb(u[idx]));
}
__global__ void out_kernel(const u16* x, const u16* gw, const float* a, const float* b, u16* out){
  int s=blockIdx.x; __shared__ float red[256];
  float p=0; for(int i=threadIdx.x;i<512;i+=256) p+=rb(bf2f(x[s*512+i])*bf2f(gw[i]));
  red[threadIdx.x]=p; __syncthreads();
  for(int o=128;o>0;o>>=1){ if(threadIdx.x<o) red[threadIdx.x]+=red[threadIdx.x+o]; __syncthreads(); }
  float t=rb(red[0]); float g=rb(1.f/(1.f+__expf(-t)));
  for(int i=threadIdx.x;i<512;i+=256){
    float av=rb(a[s*512+i]), bv=rb(b[s*512+i]);
    out[s*512+i]=f2bf(rb(g*av)+rb(rb(1.f-g)*bv));
  }
}

extern "C" void kernel_launch(void* const* d_in, const int* in_sizes, int n_in,
                              void* d_out, int out_size, void* d_ws, size_t ws_size,
                              hipStream_t stream) {
  const u16* x=(const u16*)d_in[0]; const u16* wqkv=(const u16*)d_in[1]; const u16* wo=(const u16*)d_in[2];
  const u16* wg=(const u16*)d_in[4]; const u16* wu=(const u16*)d_in[5]; const u16* wd=(const u16*)d_in[6]; const u16* gw=(const u16*)d_in[7];
  char* ws=(char*)d_ws;
  float* qkv=(float*)ws; ws+=2048*1536*4;
  float* g=(float*)ws; ws+=2048*1792*4;
  float* u=(float*)ws; ws+=2048*1792*4;
  float* a=(float*)ws; ws+=2048*512*4;
  float* b=(float*)ws; ws+=2048*512*4;
  float* kv=(float*)ws; ws+=32*256*4;
  float* z=(float*)ws; ws+=32*16*4;
  u16* o=(u16*)ws; ws+=2048*512*2;
  u16* h=(u16*)ws; ws+=2048*1792*2;
  gemm_naive<<<dim3(1536/64,32),256,0,stream>>>(x,512,wqkv,1536,qkv,1536,512);
  gemm_naive<<<dim3(1792/64,32),256,0,stream>>>(x,512,wg,1792,g,1792,512);
  gemm_naive<<<dim3(1792/64,32),256,0,stream>>>(x,512,wu,1792,u,1792,512);
  kv_kernel<<<32,256,0,stream>>>(qkv,kv,z);
  o_kernel<<<2048*512/256,256,0,stream>>>(qkv,kv,z,o);
  h_kernel<<<2048*1792/256,256,0,stream>>>(g,u,h);
  gemm_naive<<<dim3(8,32),256,0,stream>>>(o,512,wo,512,a,512,512);
  gemm_naive<<<dim3(8,32),256,0,stream>>>(h,1792,wd,512,b,512,1792);
  out_kernel<<<2048,256,0,stream>>>(x,gw,a,b,(u16*)d_out);
}
```

```cpp
#include <hip/hip_runtime.h>
#include <stdint.h>
struct BufD { const void* p; uint32_t n; uint32_t f; };
struct Args { BufD d[9]; };
__global__ __launch_bounds__(512, 2) void fused_k(Args a) {
  __shared__ char lds[163840];
  asm volatile(
"LSTART:\n"
"s_load_dwordx16 s[8:23], s[0:1], 0\n"
"s_load_dwordx16 s[24:39], s[0:1], 64\n"
"s_load_dwordx4 s[4:7], s[0:1], 128\n"
"v_lshrrev_b32 v1, 6, v0\n"
"v_and_b32 v0, 63, v0\n"
"s_and_b32 s49, s2, 7\n"
"v_readfirstlane_b32 s48, v1\n"
"s_bfe_u32 s47, s2, 0x30003\n"
"s_lshr_b32 s52, s2, 6\n"
"s_and_b32 s53, s49, 1\n"
"s_lshl_b32 s53, s53, 3\n"
"s_add_u32 s43, s53, s47\n"
"s_and_b32 s53, s49, 6\n"
"s_lshr_b32 s54, s52, 1\n"
"s_add_u32 s44, s53, s54\n"
"s_lshl_b32 s53, s52, 1\n"
"s_add_u32 s53, s53, s47\n"
"s_lshr_b32 s54, s49, 1\n"
"s_add_u32 s53, s53, s54\n"
"s_and_b32 s45, s53, 7\n"
"s_and_b32 s42, s48, 3\n"
"s_and_b32 s56, s52, 1\n"
"s_lshr_b32 s53, s49, 1\n"
"s_lshl_b32 s53, s53, 2\n"
"s_andn2_b32 s54, 1, s49\n"
"s_lshl_b32 s54, s54, 4\n"
"s_add_u32 s53, s53, s54\n"
"s_add_u32 s46, s53, s52\n"
"s_add_u32 s3, s52, s47\n"
"s_cmp_lt_u32 s48, 4\n"
"s_cbranch_scc1 LCD\n"
"v_lshrrev_b32 v20, 3, v0\n"
"v_and_b32 v21, 7, v0\n"
"v_lshrrev_b32 v22, 4, v0\n"
"v_xor_b32 v22, v22, v21\n"
"v_lshlrev_b32 v22, 4, v22\n"
"v_lshl_add_u32 v16, v20, 10, v22\n"
"v_xor_b32 v17, 64, v16\n"
"v_add_u32 v17, 7168, v17\n"
"v_add_u32 v18, 14336, v16\n"
"v_add_u32 v19, 14336, v17\n"
"s_lshl_b32 s49, s43, 2\n"
"s_add_u32 s49, s49, s42\n"
"s_lshl_b32 s57, s49, 15\n"
"s_mul_i32 s59, s42, 18432\n"
"s_add_u32 s59, s59, 81920\n"
"s_lshl_b32 s49, s45, 7\n"
"s_add_u32 s49, s49, s57\n"
"s_mov_b32 m0, s59\n"
"v_lshlrev_b32 v21, 4, v21\n"
"v_and_b32 v22, 16, v0\n"
"v_lshlrev_b32 v22, 2, v22\n"
"v_xor_b32 v21, v21, v22\n"
"s_cmp_eq_u32 s56, 1\n"
"s_cselect_b32 s50, 512, 0\n"
"s_add_u32 s50, s50, 3072\n"
"s_cmp_eq_u32 s56, 1\n"
"s_cselect_b32 s53, 0, 1024\n"
"s_cselect_b32 s54, 0, 2048\n"
"v_mad_u32_u24 v1, v20, s50, v21\n"
"s_lshl_b32 s51, s50, 3\n"
"s_sub_u32 s51, s51, 1024\n"
"v_add_u32 v2, s51, v1\n"
"s_lshl_b32 s51, s42, 4\n"
"s_mul_i32 s58, s51, s50\n"
"s_lshl_b32 s51, s44, 7\n"
"s_add_u32 s58, s58, s51\n"
"s_lshl_b32 s51, s45, 6\n"
"s_mul_i32 s51, s51, s50\n"
"s_add_u32 s51, s51, s58\n"
"s_add_u32 s53, s53, s51\n"
"s_add_u32 s54, s54, s51\n"
"s_lshl_b32 s60, s42, 11\n"
"s_waitcnt lgkmcnt(0)\n"
"buffer_load_dwordx4 v16, s[8:11], s49 offen lds\n"
"buffer_load_dwordx4 v17, s[8:11], s49 offen offset:1024 lds\n"
"buffer_load_dwordx4 v18, s[8:11], s49 offen offset:2048 lds\n"
"buffer_load_dwordx4 v19, s[8:11], s49 offen offset:3072 lds\n"
"s_cmp_eq_u32 s56, 1\n"
"s_cselect_b64 s[76:77], s[16:17], s[12:13]\n"
"s_cselect_b64 s[78:79], s[18:19], s[14:15]\n"
"s_mov_b32 m0, s60\n"
"s_cselect_b64 s[80:81], s[20:21], s[12:13]\n"
"buffer_load_dwordx4 v1, s[76:79], s53 offen lds\n"
"buffer_load_dwordx4 v2, s[76:79], s53 offen offset:1024 lds\n"
"s_cselect_b64 s[82:83], s[22:23], s[14:15]\n"
"s_add_u32 m0, s60, 8192\n"
"s_load_dword s55, s[32:33], 0\n"
"buffer_load_dwordx4 v1, s[80:83], s54 offen lds\n"
"buffer_load_dwordx4 v2, s[80:83], s54 offen offset:1024 lds\n"
"s_cmp_eq_u32 s56, 1\n"
"s_cbranch_scc1 LHB\n"
"s_branch LHA\n"
"LCD:\n"
"s_getpc_b64 s[90:91]\n"
"LPC:\n"
"s_sub_u32 s90, s90, LPC-LSTART\n"
"s_subb_u32 s91, s91, 0\n"
"s_lshr_b32 s92, s2, 3\n"
"s_lshl_b32 s92, s92, 2\n"
"s_add_u32 s92, s92, s42\n"
"s_lshl_b32 s92, s92, 2\n"
"v_add_u32 v41, s92, v0\n"
"v_lshlrev_b32 v41, 7, v41\n"
"v_min_u32 v41, LEND-LSTART-4, v41\n"
"s_mov_b64 exec, 15\n"
"global_load_dword v40, v41, s[90:91]\n"
"s_mov_b64 exec, -1\n"
"s_cmp_eq_u32 s56, 1\n"
"s_cbranch_scc1 LCB\n"
"s_branch LCA\n"
"LHA:\n"
"s_add_u32 s74, s45, 1\n"
"s_and_b32 s74, s74, 7\n"
"s_lshl_b32 s74, s74, 7\n"
"s_add_u32 s74, s74, s57\n"
"s_add_u32 m0, s59, 4096\n"
"s_nop 0\n"
"buffer_load_dwordx4 v16, s[8:11], s74 offen lds\n"
"buffer_load_dwordx4 v17, s[8:11], s74 offen offset:1024 lds\n"
"buffer_load_dwordx4 v18, s[8:11], s74 offen offset:2048 lds\n"
"buffer_load_dwordx4 v19, s[8:11], s74 offen offset:3072 lds\n"
"s_add_u32 s74, s45, 1\n"
"s_and_b32 s74, s74, 7\n"
"s_mul_i32 s74, s74, 196608\n"
"s_add_u32 s74, s74, s58\n"
"s_add_u32 s74, s74, 1024\n"
"s_add_u32 m0, s60, 16384\n"
"s_nop 0\n"
"buffer_load_dwordx4 v1, s[12:15], s74 offen lds\n"
"buffer_load_dwordx4 v2, s[12:15], s74 offen offset:1024 lds\n"
"s_add_u32 s74, s45, 1\n"
"s_and_b32 s74, s74, 7\n"
"s_mul_i32 s74, s74, 196608\n"
"s_add_u32 s74, s74, s58\n"
"s_add_u32 s74, s74, 2048\n"
"s_add_u32 m0, s60, 24576\n"
"s_nop 0\n"
"buffer_load_dwordx4 v1, s[12:15], s74 offen lds\n"
"buffer_load_dwordx4 v2, s[12:15], s74 offen offset:1024 lds\n"
"v_mul_u32_u24 v3, 3584, v20\n"
"v_add_u32 v3, v3, v21\n"
"v_add_u32 v4, 27648, v3\n"
"v_lshl_add_u32 v5, v20, 10, v21\n"
"v_add_u32 v6, 7168, v5\n"
"v_lshrrev_b32 v7, 5, v0\n"
"v_lshlrev_b32 v7, 9, v7\n"
"v_bfe_u32 v22, v0, 2, 2\n"
"v_lshl_add_u32 v7, v22, 7, v7\n"
"v_bfe_u32 v22, v0, 3, 1\n"
"v_lshl_add_u32 v7, v22, 6, v7\n"
"v_bfe_u32 v22, v0, 4, 1\n"
"v_lshl_add_u32 v7, v22, 5, v7\n"
"v_and_b32 v22, 3, v0\n"
"v_lshl_add_u32 v7, v22, 3, v7\n"
"s_and_b32 s49, s42, 1\n"
"s_lshl_b32 s49, s49, 6\n"
"v_xor_b32 v7, s49, v7\n"
"v_add_u32 v7, s59, v7\n"
"v_xor_b32 v8, 64, v7\n"
"s_lshr_b32 s49, s42, 1\n"
"s_mul_i32 s49, s49, 147456\n"
"s_sub_u32 s50, 147456, s49\n"
"v_lshlrev_b32 v22, 4, v0\n"
"v_add_u32 v9, s49, v22\n"
"v_add_u32 v10, s50, v22\n"
"s_lshl_b32 s50, s43, 2\n"
"s_add_u32 s50, s50, s42\n"
"s_mul_i32 s70, s50, 147456\n"
"s_lshl_b32 s50, s44, 12\n"
"s_add_u32 s70, s70, s50\n"
"s_add_u32 s70, s70, 1048576\n"
"s_sub_u32 s70, s70, s49\n"
"s_mul_i32 s49, s46, 294912\n"
"s_lshl_b32 s50, s42, 13\n"
"s_add_u32 s49, s49, s50\n"
"s_add_u32 s49, s49, 1048576\n"
"s_lshl_b32 s50, s47, 7\n"
"s_lshl_b32 s51, s42, 17\n"
"s_add_u32 s50, s50, s51\n"
"s_add_u32 s51, s3, 0\n"
"s_and_b32 s51, s51, 3\n"
"s_lshl_b32 s52, s51, 10\n"
"s_add_u32 s62, s52, s49\n"
"s_lshl_b32 s52, s51, 14\n"
"s_add_u32 s66, s52, s50\n"
"s_add_u32 s51, s3, 1\n"
"s_and_b32 s51, s51, 3\n"
"s_lshl_b32 s52, s51, 10\n"
"s_add_u32 s63, s52, s49\n"
"s_lshl_b32 s52, s51, 14\n"
"s_add_u32 s67, s52, s50\n"
"s_add_u32 s51, s3, 2\n"
"s_and_b32 s51, s51, 3\n"
"s_lshl_b32 s52, s51, 10\n"
"s_add_u32 s64, s52, s49\n"
"s_lshl_b32 s52, s51, 14\n"
"s_add_u32 s68, s52, s50\n"
"s_add_u32 s51, s3, 3\n"
"s_and_b32 s51, s51, 3\n"
"s_lshl_b32 s52, s51, 10\n"
"s_add_u32 s65, s52, s49\n"
"s_lshl_b32 s52, s51, 14\n"
"s_add_u32 s69, s52, s50\n"
"s_lshl_b32 s49, s46, 9\n"
"s_lshl_b32 s50, s42, 5\n"
"s_add_u32 s49, s49, s50\n"
"s_add_u32 s49, s49, 655360\n"
"v_bfe_u32 v22, v0, 2, 2\n"
"v_lshlrev_b32 v22, 7, v22\n"
"v_and_b32 v23, 3, v0\n"
"v_lshlrev_b32 v23, 3, v23\n"
"v_add3_u32 v22, v23, v22, s49\n"
"s_lshl_b32 s49, s46, 7\n"
"s_add_u32 s49, s49, s50\n"
"s_add_u32 s49, s49, 671744\n"
"v_add_u32 v23, s49, v23\n"
"v_cmp_gt_u32 vcc, 16, v0\n"
"s_nop 1\n"
"v_cndmask_b32 v11, v23, v22, vcc\n"
"s_waitcnt lgkmcnt(0)\n"
"s_add_u32 s40, s55, 1\n"
"s_xor_b32 s41, s55, 0x5bd1e995\n"
"v_mov_b32 v14, s40\n"
"v_mov_b32 v15, s41\n"
"s_branch LBHA\n"
"LHB:\n"
"s_add_u32 s74, s45, 1\n"
"s_and_b32 s74, s74, 7\n"
"s_lshl_b32 s74, s74, 7\n"
"s_add_u32 s74, s74, s57\n"
"s_add_u32 m0, s59, 4096\n"
"s_nop 0\n"
"buffer_load_dwordx4 v16, s[8:11], s74 offen lds\n"
"buffer_load_dwordx4 v17, s[8:11], s74 offen offset:1024 lds\n"
"buffer_load_dwordx4 v18, s[8:11], s74 offen offset:2048 lds\n"
"buffer_load_dwordx4 v19, s[8:11], s74 offen offset:3072 lds\n"
"s_add_u32 s74, s45, 1\n"
"s_and_b32 s74, s74, 7\n"
"s_mul_i32 s74, s74, 229376\n"
"s_add_u32 s74, s74, s58\n"
"s_add_u32 m0, s60, 16384\n"
"s_nop 0\n"
"buffer_load_dwordx4 v1, s[16:19], s74 offen lds\n"
"buffer_load_dwordx4 v2, s[16:19], s74 offen offset:1024 lds\n"
"s_add_u32 s74, s45, 1\n"
"s_and_b32 s74, s74, 7\n"
"s_mul_i32 s74, s74, 229376\n"
"s_add_u32 s74, s74, s58\n"
"s_add_u32 m0, s60, 24576\n"
"s_nop 0\n"
"buffer_load_dwordx4 v1, s[20:23], s74 offen lds\n"
"buffer_load_dwordx4 v2, s[20:23], s74 offen offset:1024 lds\n"
"v_lshrrev_b32 v22, 2, v0\n"
"v_mul_u32_u24 v3, 3584, v22\n"
"v_and_b32 v22, 3, v0\n"
"v_lshl_add_u32 v3, v22, 4, v3\n"
"s_lshl_b32 s61, s42, 10\n"
"v_lshl_add_u32 v5, v20, 10, v21\n"
"v_add_u32 v6, 7168, v5\n"
"v_lshrrev_b32 v7, 5, v0\n"
"v_lshlrev_b32 v7, 9, v7\n"
"v_bfe_u32 v22, v0, 2, 2\n"
"v_lshl_add_u32 v7, v22, 7, v7\n"
"v_bfe_u32 v22, v0, 3, 1\n"
"v_lshl_add_u32 v7, v22, 6, v7\n"
"v_bfe_u32 v22, v0, 4, 1\n"
"v_lshl_add_u32 v7, v22, 5, v7\n"
"v_and_b32 v22, 3, v0\n"
"v_lshl_add_u32 v7, v22, 3, v7\n"
"s_and_b32 s49, s42, 1\n"
"s_lshl_b32 s49, s49, 6\n"
"v_xor_b32 v7, s49, v7\n"
"v_add_u32 v7, s59, v7\n"
"v_xor_b32 v8, 64, v7\n"
"s_lshr_b32 s49, s42, 1\n"
"s_mul_i32 s49, s49, 147456\n"
"s_sub_u32 s50, 147456, s49\n"
"v_lshlrev_b32 v22, 4, v0\n"
"v_add_u32 v9, s49, v22\n"
"v_add_u32 v10, s50, v22\n"
"s_lshl_b32 s50, s43, 2\n"
"s_add_u32 s50, s50, s42\n"
"s_mul_i32 s70, s50, 147456\n"
"s_lshl_b32 s50, s44, 12\n"
"s_add_u32 s70, s70, s50\n"
"s_add_u32 s70, s70, 1048576\n"
"s_sub_u32 s70, s70, s49\n"
"s_mul_i32 s49, s46, 294912\n"
"s_lshl_b32 s50, s42, 13\n"
"s_add_u32 s49, s49, s50\n"
"s_add_u32 s49, s49, 1048576\n"
"s_lshl_b32 s50, s47, 7\n"
"s_lshl_b32 s51, s42, 17\n"
"s_add_u32 s50, s50, s51\n"
"s_add_u32 s51, s3, 0\n"
"s_and_b32 s51, s51, 3\n"
"s_lshl_b32 s52, s51, 10\n"
"s_add_u32 s62, s52, s49\n"
"s_lshl_b32 s52, s51, 14\n"
"s_add_u32 s66, s52, s50\n"
"s_add_u32 s51, s3, 1\n"
"s_and_b32 s51, s51, 3\n"
"s_lshl_b32 s52, s51, 10\n"
"s_add_u32 s63, s52, s49\n"
"s_lshl_b32 s52, s51, 14\n"
"s_add_u32 s67, s52, s50\n"
"s_add_u32 s51, s3, 2\n"
"s_and_b32 s51, s51, 3\n"
"s_lshl_b32 s52, s51, 10\n"
"s_add_u32 s64, s52, s49\n"
"s_lshl_b32 s52, s51, 14\n"
"s_add_u32 s68, s52, s50\n"
"s_add_u32 s51, s3, 3\n"
"s_and_b32 s51, s51, 3\n"
"s_lshl_b32 s52, s51, 10\n"
"s_add_u32 s65, s52, s49\n"
"s_lshl_b32 s52, s51, 14\n"
"s_add_u32 s69, s52, s50\n"
"s_lshl_b32 s49, s46, 9\n"
"s_lshl_b32 s50, s42, 5\n"
"s_add_u32 s49, s49, s50\n"
"s_add_u32 s49, s49, 655360\n"
"v_bfe_u32 v22, v0, 2, 2\n"
"v_lshlrev_b32 v22, 7, v22\n"
"v_and_b32 v23, 3, v0\n"
"v_lshlrev_b32 v23, 3, v23\n"
"v_add3_u32 v22, v23, v22, s49\n"
"s_lshl_b32 s49, s46, 7\n"
"s_add_u32 s49, s49, s50\n"
"s_add_u32 s49, s49, 671744\n"
"v_add_u32 v23, s49, v23\n"
"v_cmp_gt_u32 vcc, 16, v0\n"
"s_nop 1\n"
"v_cndmask_b32 v11, v23, v22, vcc\n"
"s_waitcnt lgkmcnt(0)\n"
"s_add_u32 s40, s55, 1\n"
"s_xor_b32 s41, s55, 0x5bd1e995\n"
"v_mov_b32 v14, s40\n"
"v_mov_b32 v15, s41\n"
"s_branch LBHB\n"
"LCA:\n"
"v_lshrrev_b32 v1, 5, v0\n"
"v_lshlrev_b32 v1, 10, v1\n"
"v_bfe_u32 v14, v0, 2, 2\n"
"v_lshl_add_u32 v1, v14, 7, v1\n"
"v_bfe_u32 v14, v0, 3, 1\n"
"v_lshl_add_u32 v1, v14, 6, v1\n"
"v_bfe_u32 v14, v0, 4, 1\n"
"v_lshl_add_u32 v1, v14, 5, v1\n"
"v_and_b32 v14, 3, v0\n"
"v_lshl_add_u32 v1, v14, 3, v1\n"
"v_xor_b32 v2, 64, v1\n"
"v_add_u32 v3, 0x10000, v1\n"
"v_add_u32 v4, 0x10000, v2\n"
"s_mul_i32 s54, s42, 18432\n"
"s_add_u32 s54, s54, 81920\n"
"v_and_b32 v14, 31, v0\n"
"v_lshlrev_b32 v14, 7, v14\n"
"v_add_u32 v14, s54, v14\n"
"v_bfe_u32 v15, v0, 1, 3\n"
"v_lshrrev_b32 v12, 5, v0\n"
"v_xor_b32 v15, v15, v12\n"
"v_xor_b32 v96, 0, v15\n"
"v_lshl_add_u32 v96, v96, 4, v14\n"
"v_xor_b32 v97, 2, v15\n"
"v_lshl_add_u32 v97, v97, 4, v14\n"
"v_xor_b32 v98, 4, v15\n"
"v_lshl_add_u32 v98, v98, 4, v14\n"
"v_xor_b32 v99, 6, v15\n"
"v_lshl_add_u32 v99, v99, 4, v14\n"
"v_lshlrev_b32 v12, 4, v12\n"
"v_bfe_u32 v14, v0, 2, 2\n"
"v_lshl_add_u32 v12, v14, 5, v12\n"
"s_lshl_b32 s49, s45, 7\n"
"v_add_u32 v16, s49, v12\n"
"v_and_b32 v12, 31, v0\n"
"v_lshlrev_b32 v12, 2, v12\n"
"s_lshl_b32 s49, s43, 3\n"
"s_add_u32 s49, s49, s44\n"
"s_lshl_b32 s49, s49, 2\n"
"s_add_u32 s49, s49, s42\n"
"s_lshl_b32 s59, s49, 7\n"
"s_add_u32 s59, s59, 393216\n"
"v_lshlrev_b32 v6, 4, v0\n"
"v_add_u32 v13, 155648, v6\n"
"v_add_u32 v6, s54, v6\n"
"s_lshl_b32 s49, s43, 2\n"
"s_add_u32 s49, s49, s42\n"
"s_mul_i32 s57, s49, 147456\n"
"s_lshl_b32 s50, s44, 12\n"
"s_add_u32 s57, s57, s50\n"
"s_add_u32 s57, s57, 1163264\n"
"s_sub_u32 s57, s57, s54\n"
"s_lshr_b32 s49, s49, 1\n"
"s_lshl_b32 s58, s49, 9\n"
"s_and_b32 s50, s42, 1\n"
"s_lshl_b32 s50, s50, 6\n"
"s_lshl_b32 s51, s44, 3\n"
"s_add_u32 s58, s58, s50\n"
"s_add_u32 s58, s58, s51\n"
"s_add_u32 s58, s58, 655744\n"
"s_mov_b64 exec, 0\n"
"s_cmp_eq_u32 s42, 0\n"
"s_cbranch_scc1 LPAD0\n"
"s_cmp_eq_u32 s42, 1\n"
"s_cbranch_scc1 LPAD4\n"
"s_cmp_eq_u32 s42, 2\n"
"s_cbranch_scc1 LPAD3\n"
"s_cmp_eq_u32 s42, 3\n"
"s_cbranch_scc1 LPAD1\n"
"LBKA:\n"
"s_mov_b64 exec, -1\n"
"s_waitcnt vmcnt(0)\n"
"s_waitcnt lgkmcnt(0)\n"
"buffer_load_dwordx4 v[100:103], v16, s[4:7], 0 offen\n"
"s_load_dword s55, s[32:33], 0\n"
"s_waitcnt lgkmcnt(0)\n"
"s_add_u32 s40, s55, 1\n"
"s_xor_b32 s41, s55, 0x5bd1e995\n"
"s_branch LBCA\n"
"LCB:\n"
"v_lshrrev_b32 v1, 5, v0\n"
"v_lshlrev_b32 v1, 10, v1\n"
"v_bfe_u32 v14, v0, 2, 2\n"
"v_lshl_add_u32 v1, v14, 7, v1\n"
"v_bfe_u32 v14, v0, 3, 1\n"
"v_lshl_add_u32 v1, v14, 6, v1\n"
"v_bfe_u32 v14, v0, 4, 1\n"
"v_lshl_add_u32 v1, v14, 5, v1\n"
"v_and_b32 v14, 3, v0\n"
"v_lshl_add_u32 v1, v14, 3, v1\n"
"v_xor_b32 v2, 64, v1\n"
"v_add_u32 v3, 0x10000, v1\n"
"v_add_u32 v4, 0x10000, v2\n"
"v_lshrrev_b32 v5, 5, v0\n"
"v_lshlrev_b32 v5, 9, v5\n"
"v_bfe_u32 v14, v0, 2, 2\n"
"v_lshl_add_u32 v5, v14, 6, v5\n"
"v_bfe_u32 v14, v0, 4, 1\n"
"v_lshl_add_u32 v5, v14, 5, v5\n"
"v_and_b32 v14, 3, v0\n"
"v_lshl_add_u32 v5, v14, 3, v5\n"
"v_add_u32 v5, 0x4000, v5\n"
"s_mul_i32 s54, s42, 18432\n"
"s_add_u32 s54, s54, 81920\n"
"v_and_b32 v14, 31, v0\n"
"v_lshlrev_b32 v14, 7, v14\n"
"v_add_u32 v14, s54, v14\n"
"v_bfe_u32 v15, v0, 1, 3\n"
"v_lshrrev_b32 v12, 5, v0\n"
"v_xor_b32 v15, v15, v12\n"
"v_xor_b32 v96, 0, v15\n"
"v_lshl_add_u32 v96, v96, 4, v14\n"
"v_xor_b32 v97, 2, v15\n"
"v_lshl_add_u32 v97, v97, 4, v14\n"
"v_xor_b32 v98, 4, v15\n"
"v_lshl_add_u32 v98, v98, 4, v14\n"
"v_xor_b32 v99, 6, v15\n"
"v_lshl_add_u32 v99, v99, 4, v14\n"
"v_lshlrev_b32 v6, 4, v0\n"
"v_add_u32 v6, s54, v6\n"
"s_lshl_b32 s49, s43, 2\n"
"s_add_u32 s49, s49, s42\n"
"s_mul_i32 s57, s49, 147456\n"
"s_lshl_b32 s50, s44, 11\n"
"s_add_u32 s57, s57, s50\n"
"s_add_u32 s57, s57, 1146880\n"
"s_sub_u32 s57, s57, s54\n"
"s_lshr_b32 s49, s49, 1\n"
"s_lshl_b32 s58, s49, 9\n"
"s_and_b32 s50, s42, 1\n"
"s_lshl_b32 s50, s50, 3\n"
"s_lshl_b32 s51, s44, 4\n"
"s_add_u32 s58, s58, s50\n"
"s_add_u32 s58, s58, s51\n"
"s_add_u32 s58, s58, 655616\n"
"s_mov_b64 exec, 0\n"
"s_cmp_eq_u32 s42, 0\n"
"s_cbranch_scc1 LPAD8\n"
"s_cmp_eq_u32 s42, 1\n"
"s_cbranch_scc1 LPAD11\n"
"s_cmp_eq_u32 s42, 2\n"
"s_cbranch_scc1 LPAD10\n"
"s_cmp_eq_u32 s42, 3\n"
"s_cbranch_scc1 LPAD12\n"
"LBKB:\n"
"s_mov_b64 exec, -1\n"
"s_waitcnt vmcnt(0)\n"
"s_waitcnt lgkmcnt(0)\n"
"s_load_dword s55, s[32:33], 0\n"
"s_waitcnt lgkmcnt(0)\n"
"s_add_u32 s40, s55, 1\n"
"s_xor_b32 s41, s55, 0x5bd1e995\n"
"s_branch LBCB\n"
"LBHA:\n"
"s_waitcnt vmcnt(8)\n"
"s_barrier\n"
"s_add_u32 s74, s45, 2\n"
"s_and_b32 s74, s74, 7\n"
"s_lshl_b32 s74, s74, 7\n"
"s_add_u32 s74, s74, s57\n"
"s_add_u32 m0, s59, 8192\n"
"s_nop 0\n"
"buffer_load_dwordx4 v16, s[8:11], s74 offen lds\n"
"buffer_load_dwordx4 v17, s[8:11], s74 offen offset:1024 lds\n"
"buffer_load_dwordx4 v18, s[8:11], s74 offen offset:2048 lds\n"
"s_nop 1\n"
"buffer_load_dwordx4 v19, s[8:11], s74 offen offset:3072 lds\n"
"s_sleep 1\n"
"s_add_u32 s74, s45, 2\n"
"s_and_b32 s74, s74, 7\n"
"s_mul_i32 s74, s74, 196608\n"
"s_add_u32 s74, s74, s58\n"
"s_add_u32 s74, s74, 1024\n"
"s_add_u32 m0, s60, 32768\n"
"s_nop 0\n"
"buffer_load_dwordx4 v1, s[12:15], s74 offen lds\n"
"s_nop 3\n"
"buffer_load_dwordx4 v2, s[12:15], s74 offen offset:1024 lds\n"
"s_sleep 1\n"
"s_add_u32 s74, s45, 2\n"
"s_and_b32 s74, s74, 7\n"
"s_mul_i32 s74, s74, 196608\n"
"s_add_u32 s74, s74, s58\n"
"s_add_u32 s74, s74, 2048\n"
"s_add_u32 m0, s60, 40960\n"
"s_nop 0\n"
"buffer_load_dwordx4 v1, s[12:15], s74 offen lds\n"
"s_nop 3\n"
"buffer_load_dwordx4 v2, s[12:15], s74 offen offset:1024 lds\n"
"s_sleep 1\n"
"s_lshl_b32 s74, s2, 3\n"
"s_add_u32 s74, s74, 679936\n"
"s_cmp_eq_u32 s42, 0\n"
"s_cselect_b64 exec, 1, 0\n"
"buffer_store_dwordx2 v[14:15], off, s[32:35], s74 sc1\n"
"s_mov_b64 exec, -1\n"
"s_waitcnt vmcnt(9)\n"
"s_barrier\n"
"s_add_u32 s74, s45, 3\n"
"s_and_b32 s74, s74, 7\n"
"s_lshl_b32 s74, s74, 7\n"
"s_add_u32 s74, s74, s57\n"
"s_add_u32 m0, s59, 12288\n"
"s_nop 0\n"
"buffer_load_dwordx4 v16, s[8:11], s74 offen lds\n"
"buffer_load_dwordx4 v17, s[8:11], s74 offen offset:1024 lds\n"
"buffer_load_dwordx4 v18, s[8:11], s74 offen offset:2048 lds\n"
"s_nop 5\n"
"buffer_load_dwordx4 v19, s[8:11], s74 offen offset:3072 lds\n"
"s_sleep 1\n"
"s_add_u32 s74, s45, 3\n"
"s_and_b32 s74, s74, 7\n"
"s_mul_i32 s74, s74, 196608\n"
"s_add_u32 s74, s74, s58\n"
"s_add_u32 s74, s74, 1024\n"
"s_add_u32 m0, s60, 49152\n"
"s_nop 0\n"
"buffer_load_dwordx4 v1, s[12:15], s74 offen lds\n"
"s_nop 3\n"
"buffer_load_dwordx4 v2, s[12:15], s74 offen offset:1024 lds\n"
"s_sleep 1\n"
"s_add_u32 s74, s45, 3\n"
"s_and_b32 s74, s74, 7\n"
"s_mul_i32 s74, s74, 196608\n"
"s_add_u32 s74, s74, s58\n"
"s_add_u32 s74, s74, 2048\n"
"s_add_u32 m0, s60, 57344\n"
"s_nop 0\n"
"buffer_load_dwordx4 v1, s[12:15], s74 offen lds\n"
"s_nop 3\n"
"buffer_load_dwordx4 v2, s[12:15], s74 offen offset:1024 lds\n"
"s_sleep 1\n"
"s_add_u32 s74, s45, 4\n"
"s_and_b32 s74, s74, 7\n"
"s_lshl_b32 s74, s74, 7\n"
"s_add_u32 s74, s74, s57\n"
"s_add_u32 m0, s59, 0\n"
"s_nop 0\n"
"buffer_load_dwordx4 v16, s[8:11], s74 offen lds\n"
"s_nop 4\n"
"buffer_load_dwordx4 v17, s[8:11], s74 offen offset:1024 lds\n"
"s_sleep 1\n"
"buffer_load_dwordx4 v18, s[8:11], s74 offen offset:2048 lds\n"
"s_nop 10\n"
"buffer_load_dwordx4 v19, s[8:11], s74 offen offset:3072 lds\n"
"s_sleep 1\n"
"s_add_u32 s74, s45, 4\n"
"s_and_b32 s74, s74, 7\n"
"s_mul_i32 s74, s74, 196608\n"
"s_add_u32 s74, s74, s58\n"
"s_add_u32 s74, s74, 1024\n"
"s_add_u32 m0, s60, 65536\n"
"s_nop 0\n"
"buffer_load_dwordx4 v1, s[12:15], s74 offen lds\n"
"s_nop 3\n"
"buffer_load_dwordx4 v2, s[12:15], s74 offen offset:1024 lds\n"
"s_sleep 1\n"
"s_add_u32 s74, s45, 4\n"
"s_and_b32 s74, s74, 7\n"
"s_mul_i32 s74, s74, 196608\n"
"s_add_u32 s74, s74, s58\n"
"s_add_u32 s74, s74, 2048\n"
"s_add_u32 m0, s60, 73728\n"
"s_nop 0\n"
"buffer_load_dwordx4 v1, s[12:15], s74 offen lds\n"
"s_nop 3\n"
"buffer_load_dwordx4 v2, s[12:15], s74 offen offset:1024 lds\n"
"s_waitcnt vmcnt(17)\n"
"s_barrier\n"
"s_sleep 1\n"
"s_add_u32 s74, s45, 5\n"
"s_and_b32 s74, s74, 7\n"
"s_lshl_b32 s74, s74, 7\n"
"s_add_u32 s74, s74, s57\n"
"s_add_u32 m0, s59, 4096\n"
"s_nop 0\n"
"buffer_load_dwordx4 v16, s[8:11], s74 offen lds\n"
"s_nop 4\n"
"buffer_load_dwordx4 v17, s[8:11], s74 offen offset:1024 lds\n"
"s_sleep 1\n"
"buffer_load_dwordx4 v18, s[8:11], s74 offen offset:2048 lds\n"
"s_nop 10\n"
"buffer_load_dwordx4 v19, s[8:11], s74 offen offset:3072 lds\n"
"s_sleep 1\n"
"s_add_u32 s74, s45, 5\n"
"s_and_b32 s74, s74, 7\n"
"s_mul_i32 s74, s74, 196608\n"
"s_add_u32 s74, s74, s58\n"
"s_add_u32 s74, s74, 1024\n"
"s_add_u32 m0, s60, 0\n"
"s_nop 0\n"
"buffer_load_dwordx4 v1, s[12:15], s74 offen lds\n"
"s_nop 3\n"
"buffer_load_dwordx4 v2, s[12:15], s74 offen offset:1024 lds\n"
"s_sleep 1\n"
"s_add_u32 s74, s45, 5\n"
"s_and_b32 s74, s74, 7\n"
"s_mul_i32 s74, s74, 196608\n"
"s_add_u32 s74, s74, s58\n"
"s_add_u32 s74, s74, 2048\n"
"s_add_u32 m0, s60, 8192\n"
"s_nop 0\n"
"buffer_load_dwordx4 v1, s[12:15], s74 offen lds\n"
"s_nop 3\n"
"buffer_load_dwordx4 v2, s[12:15], s74 offen offset:1024 lds\n"
"s_waitcnt vmcnt(16)\n"
"s_barrier\n"
"s_nop 1\n"
"s_add_u32 s74, s45, 6\n"
"s_and_b32 s74, s74, 7\n"
"s_lshl_b32 s74, s74, 7\n"
"s_add_u32 s74, s74, s57\n"
"s_add_u32 m0, s59, 8192\n"
"s_nop 0\n"
"buffer_load_dwordx4 v16, s[8:11], s74 offen lds\n"
"s_nop 7\n"
"buffer_load_dwordx4 v17, s[8:11], s74 offen offset:1024 lds\n"
"s_sleep 1\n"
"buffer_load_dwordx4 v18, s[8:11], s74 offen offset:2048 lds\n"
"s_nop 10\n"
"buffer_load_dwordx4 v19, s[8:11], s74 offen offset:3072 lds\n"
"s_sleep 1\n"
"s_add_u32 s74, s45, 6\n"
"s_and_b32 s74, s74, 7\n"
"s_mul_i32 s74, s74, 196608\n"
"s_add_u32 s74, s74, s58\n"
"s_add_u32 s74, s74, 1024\n"
"s_add_u32 m0, s60, 16384\n"
"s_nop 0\n"
"buffer_load_dwordx4 v1, s[12:15], s74 offen lds\n"
"s_nop 3\n"
"buffer_load_dwordx4 v2, s[12:15], s74 offen offset:1024 lds\n"
"s_sleep 1\n"
"s_add_u32 s74, s45, 6\n"
"s_and_b32 s74, s74, 7\n"
"s_mul_i32 s74, s74, 196608\n"
"s_add_u32 s74, s74, s58\n"
"s_add_u32 s74, s74, 2048\n"
"s_add_u32 m0, s60, 24576\n"
"s_nop 0\n"
"buffer_load_dwordx4 v1, s[12:15], s74 offen lds\n"
"s_nop 3\n"
"buffer_load_dwordx4 v2, s[12:15], s74 offen offset:1024 lds\n"
"s_waitcnt vmcnt(16)\n"
"s_barrier\n"
"s_add_u32 s74, s45, 7\n"
"s_and_b32 s74, s74, 7\n"
"s_lshl_b32 s74, s74, 7\n"
"s_add_u32 s74, s74, s57\n"
"s_add_u32 m0, s59, 12288\n"
"s_nop 0\n"
"buffer_load_dwordx4 v16, s[8:11], s74 offen lds\n"
"buffer_load_dwordx4 v17, s[8:11], s74 offen offset:1024 lds\n"
"s_nop 11\n"
"buffer_load_dwordx4 v18, s[8:11], s74 offen offset:2048 lds\n"
"s_sleep 1\n"
"buffer_load_dwordx4 v19, s[8:11], s74 offen offset:3072 lds\n"
"s_nop 10\n"
"s_add_u32 s74, s45, 7\n"
"s_and_b32 s74, s74, 7\n"
"s_mul_i32 s74, s74, 196608\n"
"s_add_u32 s74, s74, s58\n"
"s_add_u32 s74, s74, 1024\n"
"s_add_u32 m0, s60, 32768\n"
"s_nop 0\n"
"buffer_load_dwordx4 v1, s[12:15], s74 offen lds\n"
"s_nop 6\n"
"buffer_load_dwordx4 v2, s[12:15], s74 offen offset:1024 lds\n"
"s_sleep 1\n"
"s_add_u32 s74, s45, 7\n"
"s_and_b32 s74, s74, 7\n"
"s_mul_i32 s74, s74, 196608\n"
"s_add_u32 s74, s74, s58\n"
"s_add_u32 s74, s74, 2048\n"
"s_add_u32 m0, s60, 40960\n"
"s_nop 0\n"
"buffer_load_dwordx4 v1, s[12:15], s74 offen lds\n"
"s_nop 3\n"
"buffer_load_dwordx4 v2, s[12:15], s74 offen offset:1024 lds\n"
"s_waitcnt vmcnt(16)\n"
"s_barrier\n"
"s_lshl_b32 s74, s42, 13\n"
"s_add_u32 s58, s58, s74\n"
"s_add_u32 s74, s45, 0\n"
"s_and_b32 s74, s74, 7\n"
"s_mul_i32 s74, s74, 229376\n"
"s_add_u32 s74, s74, s58\n"
"s_add_u32 s74, s74, 2048\n"
"s_add_u32 m0, s60, 49152\n"
"s_nop 0\n"
"buffer_load_dwordx4 v3, s[16:19], s74 offen lds\n"
"buffer_load_dwordx4 v4, s[16:19], s74 offen offset:1024 lds\n"
"s_add_u32 s74, s45, 0\n"
"s_and_b32 s74, s74, 7\n"
"s_mul_i32 s74, s74, 229376\n"
"s_add_u32 s74, s74, s58\n"
"s_add_u32 s74, s74, 2048\n"
"s_add_u32 m0, s60, 57344\n"
"s_nop 0\n"
"buffer_load_dwordx4 v3, s[20:23], s74 offen lds\n"
"s_nop 3\n"
"buffer_load_dwordx4 v4, s[20:23], s74 offen offset:1024 lds\n"
"s_waitcnt vmcnt(12)\n"
"s_barrier\n"
"s_add_u32 s74, s45, 1\n"
"s_and_b32 s74, s74, 7\n"
"s_mul_i32 s74, s74, 229376\n"
"s_add_u32 s74, s74, s58\n"
"s_add_u32 s74, s74, 2048\n"
"s_add_u32 m0, s60, 65536\n"
"s_nop 0\n"
"buffer_load_dwordx4 v3, s[16:19], s74 offen lds\n"
"buffer_load_dwordx4 v4, s[16:19], s74 offen offset:1024 lds\n"
"s_add_u32 s74, s45, 1\n"
"s_and_b32 s74, s74, 7\n"
"s_mul_i32 s74, s74, 229376\n"
"s_add_u32 s74, s74, s58\n"
"s_add_u32 s74, s74, 2048\n"
"s_add_u32 m0, s60, 73728\n"
"s_nop 0\n"
"buffer_load_dwordx4 v3, s[20:23], s74 offen lds\n"
"buffer_load_dwordx4 v4, s[20:23], s74 offen offset:1024 lds\n"
"s_waitcnt vmcnt(8)\n"
"s_barrier\n"
"s_add_u32 s74, s45, 2\n"
"s_and_b32 s74, s74, 7\n"
"LPAD0:\n"
"s_cbranch_execz LPAD5\n"
"s_mul_i32 s74, s74, 229376\n"
"s_add_u32 s74, s74, s58\n"
"s_add_u32 s74, s74, 2048\n"
"s_add_u32 m0, s60, 0\n"
"s_nop 0\n"
"buffer_load_dwordx4 v3, s[16:19], s74 offen lds\n"
"buffer_load_dwordx4 v4, s[16:19], s74 offen offset:1024 lds\n"
"s_add_u32 s74, s45, 2\n"
"s_and_b32 s74, s74, 7\n"
"s_mul_i32 s74, s74, 229376\n"
"s_add_u32 s74, s74, s58\n"
"s_add_u32 s74, s74, 2048\n"
"s_add_u32 m0, s60, 8192\n"
"s_nop 0\n"
"buffer_load_dwordx4 v3, s[20:23], s74 offen lds\n"
"buffer_load_dwordx4 v4, s[20:23], s74 offen offset:1024 lds\n"
"s_nop 8\n"
"s_add_u32 s74, s66, 0\n"
"s_add_u32 m0, s59, 16384\n"
"s_nop 0\n"
"buffer_load_dwordx4 v5, s[24:27], s74 offen lds\n"
"s_nop 10\n"
"buffer_load_dwordx4 v6, s[24:27], s74 offen offset:1024 lds\n"
"s_sleep 1\n"
"s_add_u32 s74, s66, 65536\n"
"s_add_u32 m0, s59, 6144\n"
"s_nop 0\n"
"buffer_load_dwordx4 v5, s[24:27], s74 offen lds\n"
"s_nop 7\n"
"buffer_load_dwordx4 v6, s[24:27], s74 offen offset:1024 lds\n"
"s_sleep 1\n"
"s_add_u32 s74, s67, 0\n"
"s_add_u32 m0, s59, 4096\n"
"s_nop 0\n"
"buffer_load_dwordx4 v5, s[24:27], s74 offen lds\n"
"s_nop 7\n"
"buffer_load_dwordx4 v6, s[24:27], s74 offen offset:1024 lds\n"
"s_waitcnt vmcnt(14)\n"
"s_barrier\n"
"s_add_u32 s74, s45, 3\n"
"s_and_b32 s74, s74, 7\n"
"s_mul_i32 s74, s74, 229376\n"
"s_add_u32 s74, s74, s58\n"
"s_add_u32 s74, s74, 2048\n"
"s_add_u32 m0, s60, 16384\n"
"s_nop 0\n"
"buffer_load_dwordx4 v3, s[16:19], s74 offen lds\n"
"buffer_load_dwordx4 v4, s[16:19], s74 offen offset:1024 lds\n"
"s_lshr_b32 s49, s42, 1\n"
"s_lshl_b32 s49, s49, 11\n"
"s_and_b32 s50, s42, 1\n"
"s_lshl_b32 s50, s50, 8\n"
"s_add_u32 s49, s49, s50\n"
"s_add_u32 s49, s49, 90112\n"
"v_and_b32 v76, 15, v0\n"
"v_lshlrev_b32 v76, 4, v76\n"
"s_add_u32 s74, s45, 3\n"
"s_and_b32 s74, s74, 7\n"
"s_mul_i32 s74, s74, 229376\n"
"s_add_u32 s74, s74, s58\n"
"s_add_u32 s74, s74, 2048\n"
"s_add_u32 m0, s60, 24576\n"
"s_nop 0\n"
"buffer_load_dwordx4 v3, s[20:23], s74 offen lds\n"
"v_lshrrev_b32 v77, 4, v0\n"
"v_lshl_add_u32 v76, v77, 9, v76\n"
"v_add_u32 v76, s49, v76\n"
"v_mov_b32 v72, 0x3f803f80\n"
"v_mov_b32 v73, 0x3f803f80\n"
"v_mov_b32 v74, 0x3f803f80\n"
"v_mov_b32 v75, 0x3f803f80\n"
"buffer_load_dwordx4 v4, s[20:23], s74 offen offset:1024 lds\n"
"v_bfe_u32 v78, v0, 4, 1\n"
"v_lshrrev_b32 v79, 5, v0\n"
"v_and_b32 v77, 15, v0\n"
"v_lshlrev_b32 v77, 4, v77\n"
"v_lshl_add_u32 v77, v78, 8, v77\n"
"v_lshl_add_u32 v77, v79, 3, v77\n"
"v_lshlrev_b32 v78, 5, v78\n"
"v_lshl_add_u32 v78, v79, 4, v78\n"
"s_add_u32 s74, s67, 65536\n"
"s_add_u32 m0, s59, 2048\n"
"s_nop 0\n"
"buffer_load_dwordx4 v5, s[24:27], s74 offen lds\n"
"s_nop 10\n"
"buffer_load_dwordx4 v6, s[24:27], s74 offen offset:1024 lds\n"
"s_sleep 1\n"
"s_add_u32 s74, s68, 0\n"
"s_add_u32 m0, s59, 0\n"
"s_nop 0\n"
"buffer_load_dwordx4 v5, s[24:27], s74 offen lds\n"
"s_nop 7\n"
"buffer_load_dwordx4 v6, s[24:27], s74 offen offset:1024 lds\n"
"s_waitcnt vmcnt(18)\n"
"s_barrier\n"
"ds_read_b128 v[16:19], v76 offset:0\n"
"ds_read_b128 v[20:23], v76 offset:18432\n"
"ds_read_b128 v[24:27], v76 offset:36864\n"
"ds_read_b128 v[28:31], v76 offset:55296\n"
"ds_read_b128 v[32:35], v76 offset:4096\n"
"ds_read_b128 v[36:39], v76 offset:22528\n"
"ds_read_b128 v[40:43], v76 offset:40960\n"
"ds_read_b128 v[44:47], v76 offset:59392\n"
"s_add_u32 s74, s45, 4\n"
"s_and_b32 s74, s74, 7\n"
"s_mul_i32 s74, s74, 229376\n"
"s_add_u32 s74, s74, s58\n"
"s_add_u32 s74, s74, 2048\n"
"s_add_u32 m0, s60, 32768\n"
"s_nop 0\n"
"buffer_load_dwordx4 v3, s[16:19], s74 offen lds\n"
"buffer_load_dwordx4 v4, s[16:19], s74 offen offset:1024 lds\n"
"s_add_u32 s74, s45, 4\n"
"s_and_b32 s74, s74, 7\n"
"s_mul_i32 s74, s74, 229376\n"
"s_add_u32 s74, s74, s58\n"
"s_add_u32 s74, s74, 2048\n"
"s_add_u32 m0, s60, 40960\n"
"s_nop 0\n"
"buffer_load_dwordx4 v3, s[20:23], s74 offen lds\n"
"buffer_load_dwordx4 v4, s[20:23], s74 offen offset:1024 lds\n"
"s_waitcnt vmcnt(16)\n"
"ds_read_b64_tr_b16 v[80:81], v7 offset:16384\n"
"ds_read_b64_tr_b16 v[82:83], v7 offset:17408\n"
"ds_read_b64_tr_b16 v[84:85], v8 offset:16384\n"
"ds_read_b64_tr_b16 v[86:87], v8 offset:17408\n"
"s_waitcnt vmcnt(14)\n"
"ds_read_b64_tr_b16 v[88:89], v7 offset:6144\n"
"ds_read_b64_tr_b16 v[90:91], v7 offset:7168\n"
"ds_read_b64_tr_b16 v[92:93], v8 offset:6144\n"
"s_waitcnt lgkmcnt(14)\n"
"ds_read_b64_tr_b16 v[94:95], v8 offset:7168\n"
"s_waitcnt lgkmcnt(4)\n"
"s_add_u32 s74, s68, 65536\n"
"s_add_u32 m0, s59, 16384\n"
"s_nop 0\n"
"buffer_load_dwordx4 v5, s[24:27], s74 offen lds\n"
"v_lshlrev_b32 v48, 16, v16\n"
"v_and_b32 v49, 0xffff0000, v16\n"
"v_lshlrev_b32 v50, 16, v17\n"
"v_and_b32 v51, 0xffff0000, v17\n"
"buffer_load_dwordx4 v6, s[24:27], s74 offen offset:1024 lds\n"
"s_waitcnt vmcnt(14)\n"
"ds_read_b64_tr_b16 v[96:97], v7 offset:4096\n"
"ds_read_b64_tr_b16 v[98:99], v7 offset:5120\n"
"ds_read_b64_tr_b16 v[100:101], v8 offset:4096\n"
"ds_read_b64_tr_b16 v[102:103], v8 offset:5120\n"
"v_lshlrev_b32 v52, 16, v18\n"
"v_and_b32 v53, 0xffff0000, v18\n"
"v_lshlrev_b32 v54, 16, v19\n"
"v_and_b32 v55, 0xffff0000, v19\n"
"v_mul_f32 v56, 0x3fb8aa3b, v48\n"
"v_mul_f32 v57, 0x3fb8aa3b, v49\n"
"v_mul_f32 v58, 0x3fb8aa3b, v50\n"
"v_mul_f32 v59, 0x3fb8aa3b, v51\n"
"s_waitcnt lgkmcnt(4)\n"
"s_add_u32 s74, s69, 0\n"
"s_add_u32 m0, s59, 6144\n"
"s_nop 0\n"
"buffer_load_dwordx4 v5, s[24:27], s74 offen lds\n"
"v_mul_f32 v60, 0x3fb8aa3b, v52\n"
"v_mul_f32 v61, 0x3fb8aa3b, v53\n"
"v_mul_f32 v62, 0x3fb8aa3b, v54\n"
"v_mul_f32 v63, 0x3fb8aa3b, v55\n"
"v_exp_f32 v56, v56 clamp\n"
"v_max_f32 v48, 0, v48\n"
"v_exp_f32 v57, v57 clamp\n"
"v_max_f32 v49, 0, v49\n"
"buffer_load_dwordx4 v6, s[24:27], s74 offen offset:1024 lds\n"
"v_exp_f32 v58, v58 clamp\n"
"v_max_f32 v50, 0, v50\n"
"v_exp_f32 v59, v59 clamp\n"
"v_max_f32 v51, 0, v51\n"
"v_exp_f32 v60, v60 clamp\n"
"v_max_f32 v52, 0, v52\n"
"v_exp_f32 v61, v61 clamp\n"
"v_max_f32 v53, 0, v53\n"
"s_waitcnt lgkmcnt(0)\n"
"s_add_u32 s74, s69, 65536\n"
"s_add_u32 m0, s59, 4096\n"
"s_nop 0\n"
"buffer_load_dwordx4 v5, s[24:27], s74 offen lds\n"
"v_exp_f32 v62, v62 clamp\n"
"v_max_f32 v54, 0, v54\n"
"v_exp_f32 v63, v63 clamp\n"
"v_max_f32 v55, 0, v55\n"
"buffer_load_dwordx4 v6, s[24:27], s74 offen offset:1024 lds\n"
"v_add_f32 v48, v48, v56\n"
"v_add_f32 v49, v49, v57\n"
"v_add_f32 v50, v50, v58\n"
"v_add_f32 v51, v51, v59\n"
"v_add_f32 v52, v52, v60\n"
"v_add_f32 v53, v53, v61\n"
"v_add_f32 v54, v54, v62\n"
"v_add_f32 v55, v55, v63\n"
"v_add_f32 v48, 0x358637bd, v48\n"
"v_add_f32 v49, 0x358637bd, v49\n"
"v_add_f32 v50, 0x358637bd, v50\n"
"v_add_f32 v51, 0x358637bd, v51\n"
"s_waitcnt vmcnt(12)\n"
"ds_read_b64_tr_b16 v[104:105], v7 offset:2048\n"
"ds_read_b64_tr_b16 v[106:107], v7 offset:3072\n"
"ds_read_b64_tr_b16 v[108:109], v8 offset:2048\n"
"ds_read_b64_tr_b16 v[110:111], v8 offset:3072\n"
"v_add_f32 v52, 0x358637bd, v52\n"
"v_add_f32 v53, 0x358637bd, v53\n"
"v_add_f32 v54, 0x358637bd, v54\n"
"v_add_f32 v55, 0x358637bd, v55\n"
"v_cvt_pk_bf16_f32 v16, v48, v49\n"
"v_cvt_pk_bf16_f32 v17, v50, v51\n"
"v_cvt_pk_bf16_f32 v18, v52, v53\n"
"v_cvt_pk_bf16_f32 v19, v54, v55\n"
"s_nop 1\n"
"v_mfma_f32_16x16x32_bf16 v[64:67], v[16:19], v[32:35], 0\n"
"v_mfma_f32_16x16x32_bf16 v[68:71], v[16:19], v[72:75], 0\n"
"s_barrier\n"
"s_add_u32 s74, s45, 5\n"
"s_and_b32 s74, s74, 7\n"
"s_mul_i32 s74, s74, 229376\n"
"s_add_u32 s74, s74, s58\n"
"s_add_u32 s74, s74, 2048\n"
"s_add_u32 m0, s60, 49152\n"
"s_nop 0\n"
"buffer_load_dwordx4 v3, s[16:19], s74 offen lds\n"
"buffer_load_dwordx4 v4, s[16:19], s74 offen offset:1024 lds\n"
"s_add_u32 s74, s45, 5\n"
"s_and_b32 s74, s74, 7\n"
"s_mul_i32 s74, s74, 229376\n"
"s_add_u32 s74, s74, s58\n"
"s_add_u32 s74, s74, 2048\n"
"s_add_u32 m0, s60, 57344\n"
"s_nop 0\n"
"buffer_load_dwordx4 v3, s[20:23], s74 offen lds\n"
"buffer_load_dwordx4 v4, s[20:23], s74 offen offset:1024 lds\n"
"v_lshlrev_b32 v48, 16, v20\n"
"v_and_b32 v49, 0xffff0000, v20\n"
"v_lshlrev_b32 v50, 16, v21\n"
"v_and_b32 v51, 0xffff0000, v21\n"
"v_lshlrev_b32 v52, 16, v22\n"
"v_and_b32 v53, 0xffff0000, v22\n"
"v_lshlrev_b32 v54, 16, v23\n"
"v_and_b32 v55, 0xffff0000, v23\n"
"s_add_u32 s74, s66, 524288\n"
"s_add_u32 m0, s59, 12288\n"
"s_nop 0\n"
"buffer_load_dwordx4 v5, s[24:27], s74 offen lds\n"
"v_mul_f32 v56, 0x3fb8aa3b, v48\n"
"v_mul_f32 v57, 0x3fb8aa3b, v49\n"
"v_mul_f32 v58, 0x3fb8aa3b, v50\n"
"v_mul_f32 v59, 0x3fb8aa3b, v51\n"
"v_mul_f32 v60, 0x3fb8aa3b, v52\n"
"v_mul_f32 v61, 0x3fb8aa3b, v53\n"
"v_mul_f32 v62, 0x3fb8aa3b, v54\n"
"v_mul_f32 v63, 0x3fb8aa3b, v55\n"
"buffer_load_dwordx4 v6, s[24:27], s74 offen offset:1024 lds\n"
"v_exp_f32 v56, v56 clamp\n"
"v_max_f32 v48, 0, v48\n"
"v_exp_f32 v57, v57 clamp\n"
"v_max_f32 v49, 0, v49\n"
"v_exp_f32 v58, v58 clamp\n"
"v_max_f32 v50, 0, v50\n"
"v_exp_f32 v59, v59 clamp\n"
"v_max_f32 v51, 0, v51\n"
"s_add_u32 s74, s66, 589824\n"
"s_add_u32 m0, s59, 14336\n"
"s_nop 0\n"
"buffer_load_dwordx4 v5, s[24:27], s74 offen lds\n"
"v_exp_f32 v60, v60 clamp\n"
"v_max_f32 v52, 0, v52\n"
"v_exp_f32 v61, v61 clamp\n"
"v_max_f32 v53, 0, v53\n"
"v_exp_f32 v62, v62 clamp\n"
"v_max_f32 v54, 0, v54\n"
"v_exp_f32 v63, v63 clamp\n"
"v_max_f32 v55, 0, v55\n"
"buffer_load_dwordx4 v6, s[24:27], s74 offen offset:1024 lds\n"
"s_waitcnt vmcnt(18)\n"
"ds_read_b64_tr_b16 v[112:113], v7 offset:0\n"
"ds_read_b64_tr_b16 v[114:115], v7 offset:1024\n"
"ds_read_b64_tr_b16 v[116:117], v8 offset:0\n"
"ds_read_b64_tr_b16 v[118:119], v8 offset:1024\n"
"v_add_f32 v48, v48, v56\n"
"v_add_f32 v49, v49, v57\n"
"v_add_f32 v50, v50, v58\n"
"v_add_f32 v51, v51, v59\n"
"s_waitcnt lgkmcnt(4)\n"
"s_add_u32 s74, s67, 524288\n"
"s_add_u32 m0, s59, 2048\n"
"s_nop 0\n"
"buffer_load_dwordx4 v5, s[24:27], s74 offen lds\n"
"v_add_f32 v52, v52, v60\n"
"v_add_f32 v53, v53, v61\n"
"v_add_f32 v54, v54, v62\n"
"v_add_f32 v55, v55, v63\n"
"v_add_f32 v48, 0x358637bd, v48\n"
"v_add_f32 v49, 0x358637bd, v49\n"
"v_add_f32 v50, 0x358637bd, v50\n"
"v_add_f32 v51, 0x358637bd, v51\n"
"buffer_load_dwordx4 v6, s[24:27], s74 offen offset:1024 lds\n"
"v_add_f32 v52, 0x358637bd, v52\n"
"v_add_f32 v53, 0x358637bd, v53\n"
"v_add_f32 v54, 0x358637bd, v54\n"
"v_add_f32 v55, 0x358637bd, v55\n"
"v_cvt_pk_bf16_f32 v20, v48, v49\n"
"v_cvt_pk_bf16_f32 v21, v50, v51\n"
"v_cvt_pk_bf16_f32 v22, v52, v53\n"
"v_cvt_pk_bf16_f32 v23, v54, v55\n"
"s_nop 1\n"
"v_mfma_f32_16x16x32_bf16 v[64:67], v[20:23], v[36:39], v[64:67]\n"
"v_mfma_f32_16x16x32_bf16 v[68:71], v[20:23], v[72:75], v[68:71]\n"
"s_barrier\n"
"s_add_u32 s74, s45, 6\n"
"s_and_b32 s74, s74, 7\n"
"s_mul_i32 s74, s74, 229376\n"
"s_add_u32 s74, s74, s58\n"
"s_add_u32 s74, s74, 2048\n"
"s_add_u32 m0, s60, 65536\n"
"s_nop 0\n"
"buffer_load_dwordx4 v3, s[16:19], s74 offen lds\n"
"buffer_load_dwordx4 v4, s[16:19], s74 offen offset:1024 lds\n"
"v_lshlrev_b32 v48, 16, v24\n"
"v_and_b32 v49, 0xffff0000, v24\n"
"v_lshlrev_b32 v50, 16, v25\n"
"v_and_b32 v51, 0xffff0000, v25\n"
"v_lshlrev_b32 v52, 16, v26\n"
"v_and_b32 v53, 0xffff0000, v26\n"
"v_lshlrev_b32 v54, 16, v27\n"
"v_and_b32 v55, 0xffff0000, v27\n"
"s_add_u32 s74, s45, 6\n"
"s_and_b32 s74, s74, 7\n"
"s_mul_i32 s74, s74, 229376\n"
"s_add_u32 s74, s74, s58\n"
"s_add_u32 s74, s74, 2048\n"
"s_add_u32 m0, s60, 73728\n"
"s_nop 0\n"
"buffer_load_dwordx4 v3, s[20:23], s74 offen lds\n"
"v_mul_f32 v56, 0x3fb8aa3b, v48\n"
"v_mul_f32 v57, 0x3fb8aa3b, v49\n"
"v_mul_f32 v58, 0x3fb8aa3b, v50\n"
"v_mul_f32 v59, 0x3fb8aa3b, v51\n"
"buffer_load_dwordx4 v4, s[20:23], s74 offen offset:1024 lds\n"
"s_waitcnt vmcnt(18)\n"
"ds_read_b64_tr_b16 v[120:121], v7 offset:16384\n"
"ds_read_b64_tr_b16 v[122:123], v7 offset:17408\n"
"ds_read_b64_tr_b16 v[124:125], v8 offset:16384\n"
"ds_read_b64_tr_b16 v[126:127], v8 offset:17408\n"
"v_mul_f32 v60, 0x3fb8aa3b, v52\n"
"v_mul_f32 v61, 0x3fb8aa3b, v53\n"
"v_mul_f32 v62, 0x3fb8aa3b, v54\n"
"v_mul_f32 v63, 0x3fb8aa3b, v55\n"
"v_exp_f32 v56, v56 clamp\n"
"v_max_f32 v48, 0, v48\n"
"v_exp_f32 v57, v57 clamp\n"
"v_max_f32 v49, 0, v49\n"
"s_waitcnt lgkmcnt(4)\n"
"s_add_u32 s74, s67, 589824\n"
"s_add_u32 m0, s59, 0\n"
"s_nop 0\n"
"buffer_load_dwordx4 v5, s[24:27], s74 offen lds\n"
"v_exp_f32 v58, v58 clamp\n"
"v_max_f32 v50, 0, v50\n"
"v_exp_f32 v59, v59 clamp\n"
"v_max_f32 v51, 0, v51\n"
"buffer_load_dwordx4 v6, s[24:27], s74 offen offset:1024 lds\n"
"s_waitcnt vmcnt(18)\n"
"ds_read_b64_tr_b16 v[128:129], v7 offset:6144\n"
"ds_read_b64_tr_b16 v[130:131], v7 offset:7168\n"
"ds_read_b64_tr_b16 v[132:133], v8 offset:6144\n"
"ds_read_b64_tr_b16 v[134:135], v8 offset:7168\n"
"v_exp_f32 v60, v60 clamp\n"
"v_max_f32 v52, 0, v52\n"
"v_exp_f32 v61, v61 clamp\n"
"v_max_f32 v53, 0, v53\n"
"s_waitcnt lgkmcnt(4)\n"
"s_add_u32 s74, s68, 524288\n"
"s_add_u32 m0, s59, 16384\n"
"s_nop 0\n"
"buffer_load_dwordx4 v5, s[24:27], s74 offen lds\n"
"v_exp_f32 v62, v62 clamp\n"
"v_max_f32 v54, 0, v54\n"
"v_exp_f32 v63, v63 clamp\n"
"v_max_f32 v55, 0, v55\n"
"v_add_f32 v48, v48, v56\n"
"v_add_f32 v49, v49, v57\n"
"v_add_f32 v50, v50, v58\n"
"v_add_f32 v51, v51, v59\n"
"buffer_load_dwordx4 v6, s[24:27], s74 offen offset:1024 lds\n"
"s_waitcnt vmcnt(18)\n"
"ds_read_b64_tr_b16 v[136:137], v7 offset:4096\n"
"ds_read_b64_tr_b16 v[138:139], v7 offset:5120\n"
"ds_read_b64_tr_b16 v[140:141], v8 offset:4096\n"
"ds_read_b64_tr_b16 v[142:143], v8 offset:5120\n"
"v_add_f32 v52, v52, v60\n"
"v_add_f32 v53, v53, v61\n"
"v_add_f32 v54, v54, v62\n"
"v_add_f32 v55, v55, v63\n"
"s_waitcnt lgkmcnt(4)\n"
"s_add_u32 s74, s68, 589824\n"
"s_add_u32 m0, s59, 6144\n"
"s_nop 0\n"
"buffer_load_dwordx4 v5, s[24:27], s74 offen lds\n"
"v_add_f32 v48, 0x358637bd, v48\n"
"v_add_f32 v49, 0x358637bd, v49\n"
"v_add_f32 v50, 0x358637bd, v50\n"
"v_add_f32 v51, 0x358637bd, v51\n"
"v_add_f32 v52, 0x358637bd, v52\n"
"v_add_f32 v53, 0x358637bd, v53\n"
"v_add_f32 v54, 0x358637bd, v54\n"
"v_add_f32 v55, 0x358637bd, v55\n"
"buffer_load_dwordx4 v6, s[24:27], s74 offen offset:1024 lds\n"
"v_cvt_pk_bf16_f32 v24, v48, v49\n"
"v_cvt_pk_bf16_f32 v25, v50, v51\n"
"v_cvt_pk_bf16_f32 v26, v52, v53\n"
"v_cvt_pk_bf16_f32 v27, v54, v55\n"
"s_nop 1\n"
"v_mfma_f32_16x16x32_bf16 v[64:67], v[24:27], v[40:43], v[64:67]\n"
"v_mfma_f32_16x16x32_bf16 v[68:71], v[24:27], v[72:75], v[68:71]\n"
"s_barrier\n"
"s_add_u32 s74, s45, 7\n"
"s_and_b32 s74, s74, 7\n"
"s_mul_i32 s74, s74, 229376\n"
"s_add_u32 s74, s74, s58\n"
"s_add_u32 s74, s74, 2048\n"
"s_add_u32 m0, s60, 0\n"
"s_nop 0\n"
"buffer_load_dwordx4 v3, s[16:19], s74 offen lds\n"
"v_lshlrev_b32 v48, 16, v28\n"
"v_and_b32 v49, 0xffff0000, v28\n"
"v_lshlrev_b32 v50, 16, v29\n"
"v_and_b32 v51, 0xffff0000, v29\n"
"buffer_load_dwordx4 v4, s[16:19], s74 offen offset:1024 lds\n"
"v_lshlrev_b32 v52, 16, v30\n"
"v_and_b32 v53, 0xffff0000, v30\n"
"v_lshlrev_b32 v54, 16, v31\n"
"v_and_b32 v55, 0xffff0000, v31\n"
"v_mul_f32 v56, 0x3fb8aa3b, v48\n"
"v_mul_f32 v57, 0x3fb8aa3b, v49\n"
"v_mul_f32 v58, 0x3fb8aa3b, v50\n"
"v_mul_f32 v59, 0x3fb8aa3b, v51\n"
"v_mul_f32 v60, 0x3fb8aa3b, v52\n"
"v_mul_f32 v61, 0x3fb8aa3b, v53\n"
"v_mul_f32 v62, 0x3fb8aa3b, v54\n"
"v_mul_f32 v63, 0x3fb8aa3b, v55\n"
"s_add_u32 s74, s45, 7\n"
"s_and_b32 s74, s74, 7\n"
"s_mul_i32 s74, s74, 229376\n"
"s_add_u32 s74, s74, s58\n"
"s_add_u32 s74, s74, 2048\n"
"s_add_u32 m0, s60, 8192\n"
"s_nop 0\n"
"buffer_load_dwordx4 v3, s[20:23], s74 offen lds\n"
"v_exp_f32 v56, v56 clamp\n"
"v_max_f32 v48, 0, v48\n"
"v_exp_f32 v57, v57 clamp\n"
"v_max_f32 v49, 0, v49\n"
"buffer_load_dwordx4 v4, s[20:23], s74 offen offset:1024 lds\n"
"s_waitcnt vmcnt(18)\n"
"ds_read_b64_tr_b16 v[144:145], v7 offset:12288\n"
"ds_read_b64_tr_b16 v[146:147], v7 offset:13312\n"
"ds_read_b64_tr_b16 v[148:149], v8 offset:12288\n"
"ds_read_b64_tr_b16 v[150:151], v8 offset:13312\n"
"v_exp_f32 v58, v58 clamp\n"
"v_max_f32 v50, 0, v50\n"
"v_exp_f32 v59, v59 clamp\n"
"v_max_f32 v51, 0, v51\n"
"s_waitcnt lgkmcnt(4)\n"
"s_add_u32 s74, s69, 524288\n"
"s_add_u32 m0, s59, 4096\n"
"s_nop 0\n"
"buffer_load_dwordx4 v5, s[24:27], s74 offen lds\n"
"v_exp_f32 v60, v60 clamp\n"
"v_max_f32 v52, 0, v52\n"
"v_exp_f32 v61, v61 clamp\n"
"v_max_f32 v53, 0, v53\n"
"buffer_load_dwordx4 v6, s[24:27], s74 offen offset:1024 lds\n"
"s_waitcnt vmcnt(18)\n"
"ds_read_b64_tr_b16 v[152:153], v7 offset:14336\n"
"ds_read_b64_tr_b16 v[154:155], v7 offset:15360\n"
"ds_read_b64_tr_b16 v[156:157], v8 offset:14336\n"
"ds_read_b64_tr_b16 v[158:159], v8 offset:15360\n"
"v_exp_f32 v62, v62 clamp\n"
"v_max_f32 v54, 0, v54\n"
"v_exp_f32 v63, v63 clamp\n"
"v_max_f32 v55, 0, v55\n"
"s_waitcnt lgkmcnt(4)\n"
"s_add_u32 s74, s69, 589824\n"
"s_add_u32 m0, s59, 12288\n"
"s_nop 0\n"
"buffer_load_dwordx4 v5, s[24:27], s74 offen lds\n"
"v_add_f32 v48, v48, v56\n"
"v_add_f32 v49, v49, v57\n"
"v_add_f32 v50, v50, v58\n"
"v_add_f32 v51, v51, v59\n"
"v_add_f32 v52, v52, v60\n"
"v_add_f32 v53, v53, v61\n"
"v_add_f32 v54, v54, v62\n"
"v_add_f32 v55, v55, v63\n"
"buffer_load_dwordx4 v6, s[24:27], s74 offen offset:1024 lds\n"
"v_add_f32 v48, 0x358637bd, v48\n"
"v_add_f32 v49, 0x358637bd, v49\n"
"v_add_f32 v50, 0x358637bd, v50\n"
"v_add_f32 v51, 0x358637bd, v51\n"
"v_add_f32 v52, 0x358637bd, v52\n"
"v_add_f32 v53, 0x358637bd, v53\n"
"v_add_f32 v54, 0x358637bd, v54\n"
"v_add_f32 v55, 0x358637bd, v55\n"
"v_cvt_pk_bf16_f32 v28, v48, v49\n"
"v_cvt_pk_bf16_f32 v29, v50, v51\n"
"v_cvt_pk_bf16_f32 v30, v52, v53\n"
"v_cvt_pk_bf16_f32 v31, v54, v55\n"
"s_nop 1\n"
"v_mfma_f32_16x16x32_bf16 v[64:67], v[28:31], v[44:47], v[64:67]\n"
"v_mfma_f32_16x16x32_bf16 v[68:71], v[28:31], v[72:75], v[68:71]\n"
"s_lshl_b32 s49, s44, 2\n"
"s_add_u32 s49, s49, s42\n"
"s_lshl_b32 s49, s49, 4\n"
"s_add_u32 s49, s49, s43\n"
"s_lshl_b32 s50, s49, 9\n"
"s_add_u32 s50, s50, 32768\n"
"s_lshl_b32 s49, s49, 6\n"
"s_add_u32 s49, s49, 327680\n"
"s_nop 2\n"
"v_cvt_pk_bf16_f32 v64, v64, v65\n"
"v_cvt_pk_bf16_f32 v65, v66, v67\n"
"buffer_store_dwordx2 v[64:65], v77, s[32:35], s50 offen sc1\n"
"s_mov_b32 exec_lo, 0x10001\n"
"s_mov_b32 exec_hi, 0x10001\n"
"buffer_store_dwordx4 v[68:71], v78, s[32:35], s49 offen sc1\n"
"s_mov_b64 exec, -1\n"
"s_barrier\n"
"s_lshl_b32 s74, s42, 13\n"
"s_sub_u32 s58, s58, s74\n"
"s_add_u32 s74, s45, 0\n"
"s_and_b32 s74, s74, 7\n"
"s_mul_i32 s74, s74, 196608\n"
"s_add_u32 s74, s74, s58\n"
"s_add_u32 m0, s60, 16384\n"
"s_nop 0\n"
"buffer_load_dwordx4 v1, s[12:15], s74 offen lds\n"
"s_nop 2\n"
"buffer_load_dwordx4 v2, s[12:15], s74 offen offset:1024 lds\n"
"s_sleep 1\n"
"s_add_u32 s74, s45, 1\n"
"s_and_b32 s74, s74, 7\n"
"s_mul_i32 s74, s74, 196608\n"
"s_add_u32 s74, s74, s58\n"
"s_add_u32 m0, s60, 24576\n"
"s_nop 0\n"
"buffer_load_dwordx4 v1, s[12:15], s74 offen lds\n"
"s_nop 4\n"
"buffer_load_dwordx4 v2, s[12:15], s74 offen offset:1024 lds\n"
"s_waitcnt vmcnt(24)\n"
"ds_read_b64_tr_b16 v[160:161], v7 offset:2048\n"
"ds_read_b64_tr_b16 v[162:163], v7 offset:3072\n"
"ds_read_b64_tr_b16 v[164:165], v8 offset:2048\n"
"ds_read_b64_tr_b16 v[166:167], v8 offset:3072\n"
"s_nop 6\n"
"s_waitcnt lgkmcnt(4)\n"
"s_add_u32 s74, s66, 1048576\n"
"s_add_u32 m0, s59, 14336\n"
"s_nop 0\n"
"buffer_load_dwordx4 v5, s[24:27], s74 offen lds\n"
"s_nop 9\n"
"buffer_load_dwordx4 v6, s[24:27], s74 offen offset:1024 lds\n"
"s_waitcnt vmcnt(20)\n"
"ds_read_b64_tr_b16 v[168:169], v7 offset:0\n"
"ds_read_b64_tr_b16 v[170:171], v7 offset:1024\n"
"ds_read_b64_tr_b16 v[172:173], v8 offset:0\n"
"ds_read_b64_tr_b16 v[174:175], v8 offset:1024\n"
"s_nop 6\n"
"s_waitcnt lgkmcnt(4)\n"
"s_add_u32 s74, s66, 1114112\n"
"s_add_u32 m0, s59, 2048\n"
"s_nop 0\n"
"buffer_load_dwordx4 v5, s[24:27], s74 offen lds\n"
"s_nop 9\n"
"buffer_load_dwordx4 v6, s[24:27], s74 offen offset:1024 lds\n"
"s_barrier\n"
"s_add_u32 s74, s45, 2\n"
"s_and_b32 s74, s74, 7\n"
"s_mul_i32 s74, s74, 196608\n"
"s_add_u32 s74, s74, s58\n"
"s_add_u32 m0, s60, 32768\n"
"s_nop 0\n"
"buffer_load_dwordx4 v1, s[12:15], s74 offen lds\n"
"buffer_load_dwordx4 v2, s[12:15], s74 offen offset:1024 lds\n"
"s_nop 6\n"
"s_add_u32 s74, s45, 3\n"
"s_and_b32 s74, s74, 7\n"
"s_mul_i32 s74, s74, 196608\n"
"s_add_u32 s74, s74, s58\n"
"s_add_u32 m0, s60, 40960\n"
"s_nop 0\n"
"buffer_load_dwordx4 v1, s[12:15], s74 offen lds\n"
"s_nop 7\n"
"buffer_load_dwordx4 v2, s[12:15], s74 offen offset:1024 lds\n"
"s_waitcnt vmcnt(24)\n"
"ds_read_b64_tr_b16 v[176:177], v7 offset:16384\n"
"ds_read_b64_tr_b16 v[178:179], v7 offset:17408\n"
"ds_read_b64_tr_b16 v[180:181], v8 offset:16384\n"
"ds_read_b64_tr_b16 v[182:183], v8 offset:17408\n"
"s_nop 6\n"
"s_waitcnt lgkmcnt(4)\n"
"s_add_u32 s74, s67, 1048576\n"
"s_add_u32 m0, s59, 0\n"
"s_nop 0\n"
"buffer_load_dwordx4 v5, s[24:27], s74 offen lds\n"
"s_nop 9\n"
"buffer_load_dwordx4 v6, s[24:27], s74 offen offset:1024 lds\n"
"s_waitcnt vmcnt(24)\n"
"ds_read_b64_tr_b16 v[184:185], v7 offset:6144\n"
"ds_read_b64_tr_b16 v[186:187], v7 offset:7168\n"
"ds_read_b64_tr_b16 v[188:189], v8 offset:6144\n"
"ds_read_b64_tr_b16 v[190:191], v8 offset:7168\n"
"s_nop 6\n"
"s_waitcnt lgkmcnt(4)\n"
"s_add_u32 s74, s67, 1114112\n"
"s_add_u32 m0, s59, 16384\n"
"s_nop 0\n"
"buffer_load_dwordx4 v5, s[24:27], s74 offen lds\n"
"s_nop 9\n"
"buffer_load_dwordx4 v6, s[24:27], s74 offen offset:1024 lds\n"
"s_waitcnt vmcnt(20)\n"
"ds_read_b64_tr_b16 v[192:193], v7 offset:4096\n"
"ds_read_b64_tr_b16 v[194:195], v7 offset:5120\n"
"ds_read_b64_tr_b16 v[196:197], v8 offset:4096\n"
"ds_read_b64_tr_b16 v[198:199], v8 offset:5120\n"
"s_nop 6\n"
"s_waitcnt lgkmcnt(4)\n"
"s_add_u32 s74, s68, 1048576\n"
"s_add_u32 m0, s59, 6144\n"
"s_nop 0\n"
"buffer_load_dwordx4 v5, s[24:27], s74 offen lds\n"
"s_nop 9\n"
"buffer_load_dwordx4 v6, s[24:27], s74 offen offset:1024 lds\n"
"s_waitcnt vmcnt(18)\n"
"s_lshl_b32 s49, s44, 2\n"
"s_add_u32 s49, s49, s42\n"
"s_lshl_b32 s49, s49, 7\n"
"s_lshl_b32 s50, s43, 3\n"
"s_add_u32 s49, s49, s50\n"
"s_add_u32 s49, s49, 675840\n"
"s_mov_b64 exec, 1\n"
"buffer_store_dwordx2 v[14:15], off, s[32:35], s49 sc1\n"
"s_mov_b64 exec, -1\n"
"s_barrier\n"
"v_lshrrev_b32 v46, 3, v0\n"
"v_lshrrev_b32 v47, 4, v0\n"
"LPAD1:\n"
"s_cbranch_execz LPAD2\n"
"v_xor_b32 v46, v46, v47\n"
"v_and_b32 v46, 1, v46\n"
"v_lshlrev_b32 v46, 2, v46\n"
"v_bfe_u32 v47, v0, 1, 2\n"
"v_add_u32 v46, v46, v47\n"
"v_and_b32 v47, 1, v0\n"
"v_lshlrev_b32 v47, 4, v47\n"
"v_mov_b32 v36, 0x3f80\n"
"v_lshlrev_b32 v47, v47, v36\n"
"v_cmp_eq_u32 vcc, 0, v46\n"
"s_nop 1\n"
"v_cndmask_b32 v36, 0, v47, vcc\n"
"v_cmp_eq_u32 vcc, 1, v46\n"
"s_nop 1\n"
"v_cndmask_b32 v37, 0, v47, vcc\n"
"v_cmp_eq_u32 vcc, 2, v46\n"
"s_nop 1\n"
"v_cndmask_b32 v38, 0, v47, vcc\n"
"v_cmp_eq_u32 vcc, 3, v46\n"
"s_nop 1\n"
"v_cndmask_b32 v39, 0, v47, vcc\n"
"s_lshl_b32 s49, s44, 2\n"
"s_add_u32 s49, s49, s42\n"
"s_lshl_b32 s50, s49, 7\n"
"s_add_u32 s50, s50, 675840\n"
"v_and_b32 v40, 15, v0\n"
"v_lshlrev_b32 v40, 3, v40\n"
"v_add_u32 v40, s50, v40\n"
"s_lshl_b32 s50, s49, 13\n"
"s_add_u32 s50, s50, 32768\n"
"v_lshlrev_b32 v35, 4, v0\n"
"v_add_u32 v32, s50, v35\n"
"v_add_u32 v33, 4096, v32\n"
"v_and_b32 v34, 15, v0\n"
"v_lshlrev_b32 v34, 6, v34\n"
"v_lshrrev_b32 v44, 4, v0\n"
"v_lshl_add_u32 v34, v44, 4, v34\n"
"s_lshl_b32 s50, s49, 10\n"
"s_add_u32 s50, s50, 327680\n"
"v_add_u32 v34, s50, v34\n"
"s_lshl_b32 s50, s42, 10\n"
"s_add_u32 s50, s50, 155648\n"
"v_add_u32 v35, s50, v35\n"
"buffer_load_dwordx2 v[12:13], v11, s[32:35], 0 offen sc1\n"
"s_sleep 1\n"
"s_add_u32 s74, s45, 4\n"
"s_and_b32 s74, s74, 7\n"
"s_mul_i32 s74, s74, 196608\n"
"s_add_u32 s74, s74, s58\n"
"s_add_u32 m0, s60, 49152\n"
"s_nop 0\n"
"buffer_load_dwordx4 v1, s[12:15], s74 offen lds\n"
"s_sleep 1\n"
"buffer_load_dwordx4 v2, s[12:15], s74 offen offset:1024 lds\n"
"s_sleep 1\n"
"s_add_u32 s74, s45, 5\n"
"s_and_b32 s74, s74, 7\n"
"s_mul_i32 s74, s74, 196608\n"
"s_add_u32 s74, s74, s58\n"
"s_add_u32 m0, s60, 57344\n"
"s_nop 0\n"
"buffer_load_dwordx4 v1, s[12:15], s74 offen lds\n"
"s_sleep 1\n"
"buffer_load_dwordx4 v2, s[12:15], s74 offen offset:1024 lds\n"
"s_waitcnt vmcnt(20)\n"
"s_barrier\n"
"s_waitcnt vmcnt(4)\n"
"v_cmp_eq_u64 vcc, s[40:41], v[12:13]\n"
"s_nop 0\n"
"s_and_b32 s74, vcc_lo, 0xf\n"
"s_cmp_eq_u32 s74, 0xf\n"
"s_cbranch_scc0 LHA_slow_1\n"
"LHA_back_2:\n"
"s_nop 6\n"
"s_add_u32 s74, s45, 6\n"
"s_and_b32 s74, s74, 7\n"
"s_mul_i32 s74, s74, 196608\n"
"s_add_u32 s74, s74, s58\n"
"s_add_u32 m0, s60, 65536\n"
"s_nop 0\n"
"buffer_load_dwordx4 v1, s[12:15], s74 offen lds\n"
"s_sleep 1\n"
"buffer_load_dwordx4 v2, s[12:15], s74 offen offset:1024 lds\n"
"s_sleep 1\n"
"s_add_u32 s74, s45, 7\n"
"s_and_b32 s74, s74, 7\n"
"s_mul_i32 s74, s74, 196608\n"
"s_add_u32 s74, s74, s58\n"
"s_add_u32 m0, s60, 73728\n"
"s_nop 0\n"
"buffer_load_dwordx4 v1, s[12:15], s74 offen lds\n"
"s_sleep 1\n"
"buffer_load_dwordx4 v2, s[12:15], s74 offen offset:1024 lds\n"
"s_sleep 1\n"
"buffer_load_dwordx2 v[42:43], v40, s[32:35], 0 offen sc1\n"
"s_sleep 1\n"
"s_add_u32 s74, s62, 0\n"
"buffer_load_dwordx4 v[200:203], v9, s[32:35], s74 offen sc1\n"
"s_sleep 1\n"
"buffer_load_dwordx4 v[204:207], v10, s[32:35], s74 offen sc1\n"
"s_barrier\n"
"v_lshlrev_b32 v220, 4, v0\n"
"v_add_u32 v220, s59, v220\n"
"ds_read_b128 v[208:211], v220 offset:8192\n"
"ds_read_b128 v[212:215], v220 offset:9216\n"
"ds_read_b128 v[216:219], v220 offset:10240\n"
"ds_read_b128 v[220:223], v220 offset:11264\n"
"s_waitcnt vmcnt(2)\n"
"v_cmp_eq_u64 vcc, s[40:41], v[42:43]\n"
"s_nop 0\n"
"s_and_b32 s74, vcc_lo, 0xffff\n"
"s_cmp_eq_u32 s74, 0xffff\n"
"s_cbranch_scc0 LHA_slow_3\n"
"LHA_back_4:\n"
"buffer_load_dwordx4 v[48:51], v32, s[32:35], 0 offen sc1\n"
"buffer_load_dwordx4 v[52:55], v32, s[32:35], 0 offen offset:1024 sc1\n"
"buffer_load_dwordx4 v[56:59], v32, s[32:35], 0 offen offset:2048 sc1\n"
"s_nop 1\n"
"buffer_load_dwordx4 v[60:63], v32, s[32:35], 0 offen offset:3072 sc1\n"
"s_sleep 1\n"
"buffer_load_dwordx4 v[64:67], v33, s[32:35], 0 offen sc1\n"
"s_nop 10\n"
"buffer_load_dwordx4 v[68:71], v33, s[32:35], 0 offen offset:1024 sc1\n"
"s_sleep 1\n"
"buffer_load_dwordx4 v[72:75], v33, s[32:35], 0 offen offset:2048 sc1\n"
"s_nop 10\n"
"buffer_load_dwordx4 v[76:79], v33, s[32:35], 0 offen offset:3072 sc1\n"
"s_sleep 1\n"
"buffer_load_dwordx4 v[40:43], v34, s[32:35], 0 offen sc1\n"
"s_nop 10\n"
"s_add_u32 s74, s66, 0\n"
"s_lshl_b32 s75, s42, 12\n"
"s_add_u32 m0, s75, 0\n"
"s_nop 0\n"
"buffer_load_dwordx4 v5, s[28:31], s74 offen lds\n"
"s_nop 9\n"
"buffer_load_dwordx4 v6, s[28:31], s74 offen offset:1024 lds\n"
"s_sleep 1\n"
"s_add_u32 s74, s66, 65536\n"
"s_lshl_b32 s75, s42, 12\n"
"s_add_u32 m0, s75, 2048\n"
"s_nop 0\n"
"buffer_load_dwordx4 v5, s[28:31], s74 offen lds\n"
"s_nop 6\n"
"buffer_load_dwordx4 v6, s[28:31], s74 offen offset:1024 lds\n"
"s_sleep 1\n"
"s_add_u32 s74, s70, 65536\n"
"s_waitcnt lgkmcnt(3)\n"
"buffer_store_dwordx4 v[208:211], v9, s[32:35], s74 offen sc1\n"
"s_nop 8\n"
"s_waitcnt lgkmcnt(2)\n"
"buffer_store_dwordx4 v[212:215], v9, s[32:35], s74 offen offset:1024 sc1\n"
"s_nop 12\n"
"s_waitcnt lgkmcnt(1)\n"
"buffer_store_dwordx4 v[216:219], v9, s[32:35], s74 offen offset:2048 sc1\n"
"s_nop 12\n"
"s_waitcnt lgkmcnt(0)\n"
"buffer_store_dwordx4 v[220:223], v9, s[32:35], s74 offen offset:3072 sc1\n"
"s_barrier\n"
"s_nop 12\n"
"s_add_u32 s74, s67, 0\n"
"s_lshl_b32 s75, s42, 12\n"
"s_add_u32 m0, s75, 16384\n"
"s_nop 0\n"
"buffer_load_dwordx4 v5, s[28:31], s74 offen lds\n"
"s_nop 9\n"
"buffer_load_dwordx4 v6, s[28:31], s74 offen offset:1024 lds\n"
"s_sleep 1\n"
"s_add_u32 s74, s67, 65536\n"
"s_lshl_b32 s75, s42, 12\n"
"s_add_u32 m0, s75, 18432\n"
"s_nop 0\n"
"buffer_load_dwordx4 v5, s[28:31], s74 offen lds\n"
"s_nop 6\n"
"buffer_load_dwordx4 v6, s[28:31], s74 offen offset:1024 lds\n"
"s_sleep 1\n"
"s_add_u32 s74, s62, 4096\n"
"buffer_load_dwordx4 v[224:227], v9, s[32:35], s74 offen sc1\n"
"s_nop 9\n"
"buffer_load_dwordx4 v[228:231], v10, s[32:35], s74 offen sc1\n"
"s_barrier\n"
"s_add_u32 s74, s68, 0\n"
"s_lshl_b32 s75, s42, 12\n"
"s_add_u32 m0, s75, 32768\n"
"s_nop 0\n"
"buffer_load_dwordx4 v5, s[28:31], s74 offen lds\n"
"buffer_load_dwordx4 v6, s[28:31], s74 offen offset:1024 lds\n"
"s_add_u32 s74, s68, 65536\n"
"s_lshl_b32 s75, s42, 12\n"
"s_add_u32 m0, s75, 34816\n"
"s_nop 0\n"
"buffer_load_dwordx4 v5, s[28:31], s74 offen lds\n"
"s_waitcnt vmcnt(25)\n"
"v_mfma_f32_16x16x32_bf16 v[44:47], v[36:39], v[48:51], 0\n"
"buffer_load_dwordx4 v6, s[28:31], s74 offen offset:1024 lds\n"
"s_waitcnt vmcnt(25)\n"
"v_mfma_f32_16x16x32_bf16 v[44:47], v[36:39], v[52:55], v[44:47]\n"
"s_waitcnt vmcnt(24)\n"
"v_mfma_f32_16x16x32_bf16 v[44:47], v[36:39], v[56:59], v[44:47]\n"
"s_add_u32 s74, s63, 0\n"
"buffer_load_dwordx4 v[232:235], v9, s[32:35], s74 offen sc1\n"
"s_waitcnt vmcnt(24)\n"
"v_mfma_f32_16x16x32_bf16 v[44:47], v[36:39], v[60:63], v[44:47]\n"
"s_waitcnt vmcnt(23)\n"
"v_mfma_f32_16x16x32_bf16 v[44:47], v[36:39], v[64:67], v[44:47]\n"
"buffer_load_dwordx4 v[236:239], v10, s[32:35], s74 offen sc1\n"
"s_waitcnt vmcnt(23)\n"
"v_mfma_f32_16x16x32_bf16 v[44:47], v[36:39], v[68:71], v[44:47]\n"
"s_waitcnt vmcnt(22)\n"
"v_mfma_f32_16x16x32_bf16 v[44:47], v[36:39], v[72:75], v[44:47]\n"
"s_add_u32 s74, s63, 4096\n"
"buffer_load_dwordx4 v[240:243], v9, s[32:35], s74 offen sc1\n"
"s_waitcnt vmcnt(22)\n"
"v_mfma_f32_16x16x32_bf16 v[44:47], v[36:39], v[76:79], v[44:47]\n"
"s_waitcnt vmcnt(21)\n"
"v_add_f32 v40, v40, v40 row_ror:8 row_mask:0xf bank_mask:0xf\n"
"v_add_f32 v41, v41, v41 row_ror:8 row_mask:0xf bank_mask:0xf\n"
"v_add_f32 v42, v42, v42 row_ror:8 row_mask:0xf bank_mask:0xf\n"
"v_add_f32 v43, v43, v43 row_ror:8 row_mask:0xf bank_mask:0xf\n"
"v_add_f32 v40, v40, v40 row_ror:4 row_mask:0xf bank_mask:0xf\n"
"v_add_f32 v41, v41, v41 row_ror:4 row_mask:0xf bank_mask:0xf\n"
"v_add_f32 v42, v42, v42 row_ror:4 row_mask:0xf bank_mask:0xf\n"
"v_add_f32 v43, v43, v43 row_ror:4 row_mask:0xf bank_mask:0xf\n"
"buffer_load_dwordx4 v[244:247], v10, s[32:35], s74 offen sc1\n"
"v_add_f32 v40, v40, v40 row_ror:2 row_mask:0xf bank_mask:0xf\n"
"v_add_f32 v41, v41, v41 row_ror:2 row_mask:0xf bank_mask:0xf\n"
"v_add_f32 v42, v42, v42 row_ror:2 row_mask:0xf bank_mask:0xf\n"
"v_add_f32 v43, v43, v43 row_ror:2 row_mask:0xf bank_mask:0xf\n"
"v_add_f32 v40, v40, v40 row_ror:1 row_mask:0xf bank_mask:0xf\n"
"v_add_f32 v41, v41, v41 row_ror:1 row_mask:0xf bank_mask:0xf\n"
"v_add_f32 v42, v42, v42 row_ror:1 row_mask:0xf bank_mask:0xf\n"
"v_add_f32 v43, v43, v43 row_ror:1 row_mask:0xf bank_mask:0xf\n"
"v_cvt_pk_bf16_f32 v44, v44, v45\n"
"v_cvt_pk_bf16_f32 v45, v46, v47\n"
"v_cvt_pk_bf16_f32 v46, v40, v41\n"
"v_cvt_pk_bf16_f32 v47, v42, v43\n"
"s_nop 0\n"
"v_permlane16_swap_b32 v44, v46\n"
"v_permlane16_swap_b32 v45, v47\n"
"ds_write_b128 v35, v[44:47]\n"
"s_waitcnt vmcnt(14)\n"
"s_lshl_b32 s49, s43, 2\n"
"s_add_u32 s49, s49, s42\n"
"s_lshr_b32 s49, s49, 1\n"
"s_lshl_b32 s49, s49, 7\n"
"s_lshl_b32 s50, s44, 4\n"
"s_add_u32 s49, s49, s50\n"
"s_and_b32 s50, s42, 1\n"
"s_lshl_b32 s50, s50, 3\n"
"s_add_u32 s49, s49, s50\n"
"s_add_u32 s49, s49, 671744\n"
"s_mov_b64 exec, 1\n"
"buffer_store_dwordx2 v[14:15], off, s[32:35], s49 sc1\n"
"s_mov_b64 exec, -1\n"
"s_waitcnt lgkmcnt(0)\n"
"s_barrier\n"
"s_add_u32 s74, s69, 0\n"
"s_lshl_b32 s75, s42, 12\n"
"s_add_u32 m0, s75, 49152\n"
"s_nop 0\n"
"buffer_load_dwordx4 v5, s[28:31], s74 offen lds\n"
"buffer_load_dwordx4 v6, s[28:31], s74 offen offset:1024 lds\n"
"s_add_u32 s74, s69, 65536\n"
"s_lshl_b32 s75, s42, 12\n"
"s_add_u32 m0, s75, 51200\n"
"s_nop 0\n"
"buffer_load_dwordx4 v5, s[28:31], s74 offen lds\n"
"v_mfma_f32_32x32x16_bf16 v[32:47], v[84:87], v[200:203], 0\n"
"v_mfma_f32_32x32x16_bf16 v[48:63], v[80:83], v[204:207], 0\n"
"buffer_load_dwordx4 v6, s[28:31], s74 offen offset:1024 lds\n"
"v_mfma_f32_32x32x16_bf16 v[64:79], v[84:87], v[204:207], 0\n"
"v_mfma_f32_32x32x16_bf16 v[16:31], v[80:83], v[200:203], 0\n"
"s_add_u32 s74, s64, 0\n"
"buffer_load_dwordx4 v[248:251], v9, s[32:35], s74 offen sc1\n"
"s_nop 9\n"
"buffer_load_dwordx4 v[252:255], v10, s[32:35], s74 offen sc1\n"
"s_sleep 1\n"
"s_add_u32 s74, s64, 4096\n"
"buffer_load_dwordx4 v[208:211], v9, s[32:35], s74 offen sc1\n"
"s_nop 9\n"
"buffer_load_dwordx4 v[212:215], v10, s[32:35], s74 offen sc1\n"
"s_sleep 1\n"
"s_add_u32 s74, s65, 0\n"
"buffer_load_dwordx4 v[216:219], v9, s[32:35], s74 offen sc1\n"
"s_nop 9\n"
"buffer_load_dwordx4 v[220:223], v10, s[32:35], s74 offen sc1\n"
"s_sleep 1\n"
"s_add_u32 s74, s65, 4096\n"
"buffer_load_dwordx4 v[200:203], v9, s[32:35], s74 offen sc1\n"
"s_nop 9\n"
"buffer_load_dwordx4 v[204:207], v10, s[32:35], s74 offen sc1\n"
"s_sleep 1\n"
"s_add_u32 s74, s68, 1114112\n"
"s_add_u32 m0, s59, 8192\n"
"s_nop 0\n"
"buffer_load_dwordx4 v5, s[24:27], s74 offen lds\n"
"s_waitcnt vmcnt(23)\n"
"v_mfma_f32_32x32x16_bf16 v[32:47], v[92:95], v[224:227], v[32:47]\n"
"s_waitcnt vmcnt(22)\n"
"v_mfma_f32_32x32x16_bf16 v[48:63], v[88:91], v[228:231], v[48:63]\n"
"buffer_load_dwordx4 v6, s[24:27], s74 offen offset:1024 lds\n"
"v_mfma_f32_32x32x16_bf16 v[64:79], v[92:95], v[228:231], v[64:79]\n"
"v_mfma_f32_32x32x16_bf16 v[16:31], v[88:91], v[224:227], v[16:31]\n"
"s_add_u32 s74, s69, 1048576\n"
"s_add_u32 m0, s59, 10240\n"
"s_nop 0\n"
"buffer_load_dwordx4 v5, s[24:27], s74 offen lds\n"
"s_nop 7\n"
"buffer_load_dwordx4 v6, s[24:27], s74 offen offset:1024 lds\n"
"s_sleep 1\n"
"s_add_u32 s74, s69, 1114112\n"
"s_add_u32 m0, s59, 4096\n"
"s_nop 0\n"
"buffer_load_dwordx4 v5, s[24:27], s74 offen lds\n"
"s_nop 7\n"
"buffer_load_dwordx4 v6, s[24:27], s74 offen offset:1024 lds\n"
"s_waitcnt vmcnt(22)\n"
"v_mfma_f32_32x32x16_bf16 v[32:47], v[100:103], v[232:235], v[32:47]\n"
"s_waitcnt vmcnt(21)\n"
"v_mfma_f32_32x32x16_bf16 v[48:63], v[96:99], v[236:239], v[48:63]\n"
"v_mfma_f32_32x32x16_bf16 v[64:79], v[100:103], v[236:239], v[64:79]\n"
"buffer_load_dwordx2 v[12:13], v11, s[32:35], 0 offen sc1\n"
"v_mfma_f32_32x32x16_bf16 v[16:31], v[96:99], v[232:235], v[16:31]\n"
"ds_read_b64_tr_b16 v[80:81], v7 offset:12288\n"
"ds_read_b64_tr_b16 v[82:83], v7 offset:13312\n"
"ds_read_b64_tr_b16 v[84:85], v8 offset:12288\n"
"ds_read_b64_tr_b16 v[86:87], v8 offset:13312\n"
"s_waitcnt lgkmcnt(0)\n"
"s_lshl_b32 s74, s42, 16\n"
"s_sub_u32 s74, s66, s74\n"
"s_add_u32 s74, s74, 1572864\n"
"s_add_u32 m0, s59, 12288\n"
"s_nop 0\n"
"buffer_load_dwordx4 v5, s[24:27], s74 offen lds\n"
"s_waitcnt vmcnt(22)\n"
"v_mfma_f32_32x32x16_bf16 v[32:47], v[108:111], v[240:243], v[32:47]\n"
"buffer_load_dwordx4 v6, s[24:27], s74 offen offset:1024 lds\n"
"s_waitcnt vmcnt(22)\n"
"v_mfma_f32_32x32x16_bf16 v[48:63], v[104:107], v[244:247], v[48:63]\n"
"v_mfma_f32_32x32x16_bf16 v[64:79], v[108:111], v[244:247], v[64:79]\n"
"v_mfma_f32_32x32x16_bf16 v[16:31], v[104:107], v[240:243], v[16:31]\n"
"ds_read_b64_tr_b16 v[224:225], v7 offset:14336\n"
"ds_read_b64_tr_b16 v[226:227], v7 offset:15360\n"
"ds_read_b64_tr_b16 v[228:229], v8 offset:14336\n"
"ds_read_b64_tr_b16 v[230:231], v8 offset:15360\n"
"s_waitcnt lgkmcnt(0)\n"
"s_lshl_b32 s74, s42, 16\n"
"s_sub_u32 s74, s67, s74\n"
"s_add_u32 s74, s74, 1572864\n"
"s_add_u32 m0, s59, 14336\n"
"s_nop 0\n"
"buffer_load_dwordx4 v5, s[24:27], s74 offen lds\n"
"buffer_load_dwordx4 v6, s[24:27], s74 offen offset:1024 lds\n"
"ds_read_b64_tr_b16 v[88:89], v7 offset:2048\n"
"ds_read_b64_tr_b16 v[90:91], v7 offset:3072\n"
"ds_read_b64_tr_b16 v[92:93], v8 offset:2048\n"
"ds_read_b64_tr_b16 v[94:95], v8 offset:3072\n"
"s_nop 1\n"
"s_waitcnt lgkmcnt(0)\n"
"s_lshl_b32 s74, s42, 16\n"
"s_sub_u32 s74, s68, s74\n"
"s_add_u32 s74, s74, 1572864\n"
"s_add_u32 m0, s59, 2048\n"
"s_nop 0\n"
"buffer_load_dwordx4 v5, s[24:27], s74 offen lds\n"
"s_nop 7\n"
"buffer_load_dwordx4 v6, s[24:27], s74 offen offset:1024 lds\n"
"s_waitcnt vmcnt(20)\n"
"v_mfma_f32_32x32x16_bf16 v[32:47], v[116:119], v[248:251], v[32:47]\n"
"s_waitcnt vmcnt(19)\n"
"v_mfma_f32_32x32x16_bf16 v[48:63], v[112:115], v[252:255], v[48:63]\n"
"v_mfma_f32_32x32x16_bf16 v[64:79], v[116:119], v[252:255], v[64:79]\n"
"v_mfma_f32_32x32x16_bf16 v[16:31], v[112:115], v[248:251], v[16:31]\n"
"ds_read_b64_tr_b16 v[232:233], v7 offset:0\n"
"ds_read_b64_tr_b16 v[234:235], v7 offset:1024\n"
"ds_read_b64_tr_b16 v[236:237], v8 offset:0\n"
"ds_read_b64_tr_b16 v[238:239], v8 offset:1024\n"
"s_waitcnt lgkmcnt(0)\n"
"s_lshl_b32 s74, s42, 16\n"
"s_sub_u32 s74, s69, s74\n"
"s_add_u32 s74, s74, 1572864\n"
"s_add_u32 m0, s59, 0\n"
"s_nop 0\n"
"buffer_load_dwordx4 v5, s[24:27], s74 offen lds\n"
"buffer_load_dwordx4 v6, s[24:27], s74 offen offset:1024 lds\n"
"s_waitcnt vmcnt(20)\n"
"v_mfma_f32_32x32x16_bf16 v[32:47], v[124:127], v[208:211], v[32:47]\n"
"s_waitcnt vmcnt(19)\n"
"v_mfma_f32_32x32x16_bf16 v[48:63], v[120:123], v[212:215], v[48:63]\n"
"v_mfma_f32_32x32x16_bf16 v[64:79], v[124:127], v[212:215], v[64:79]\n"
"v_mfma_f32_32x32x16_bf16 v[16:31], v[120:123], v[208:211], v[16:31]\n"
"s_waitcnt vmcnt(18)\n"
"v_mfma_f32_32x32x16_bf16 v[32:47], v[132:135], v[216:219], v[32:47]\n"
"s_waitcnt vmcnt(17)\n"
"v_mfma_f32_32x32x16_bf16 v[48:63], v[128:131], v[220:223], v[48:63]\n"
"v_mfma_f32_32x32x16_bf16 v[64:79], v[132:135], v[220:223], v[64:79]\n"
"v_mfma_f32_32x32x16_bf16 v[16:31], v[128:131], v[216:219], v[16:31]\n"
"s_waitcnt vmcnt(8)\n"
"v_cmp_eq_u64 vcc, s[40:41], v[12:13]\n"
"s_nop 0\n"
"s_and_b32 s74, vcc_lo, 0xf0\n"
"s_cmp_eq_u32 s74, 0xf0\n"
"s_cbranch_scc0 LHA_slow_5\n"
"LHA_back_6:\n"
"v_cmp_eq_u64 vcc, s[40:41], v[12:13]\n"
"s_nop 0\n"
"s_and_b32 s74, vcc_lo, 0xf0000\n"
"s_cmp_eq_u32 s74, 0xf0000\n"
"s_cbranch_scc0 LHA_slow_7\n"
"LHA_back_8:\n"
"s_lshr_b32 s74, s46, 1\n"
"s_lshl_b32 s74, s74, 2\n"
"s_add_u32 s74, s74, s42\n"
"s_lshl_b32 s74, s74, 10\n"
"s_and_b32 s75, s46, 1\n"
"s_lshl_b32 s75, s75, 8\n"
"s_add_u32 s74, s74, s75\n"
"s_add_u32 s74, s74, 393216\n"
"v_lshlrev_b32 v3, 2, v0\n"
"buffer_load_dword v4, v3, s[32:35], s74 offen offset:512 sc1\n"
"buffer_load_dword v3, v3, s[32:35], s74 offen sc1\n"
"s_barrier\n"
"s_add_u32 s74, s62, 32768\n"
"buffer_load_dwordx4 v[96:99], v9, s[32:35], s74 offen sc1\n"
"v_mfma_f32_32x32x16_bf16 v[32:47], v[140:143], v[200:203], v[32:47]\n"
"buffer_load_dwordx4 v[100:103], v10, s[32:35], s74 offen sc1\n"
"v_mfma_f32_32x32x16_bf16 v[48:63], v[136:139], v[204:207], v[48:63]\n"
"v_mfma_f32_32x32x16_bf16 v[64:79], v[140:143], v[204:207], v[64:79]\n"
"s_add_u32 s74, s62, 36864\n"
"buffer_load_dwordx4 v[240:243], v9, s[32:35], s74 offen sc1\n"
"v_mfma_f32_32x32x16_bf16 v[16:31], v[136:139], v[200:203], v[16:31]\n"
"s_nop 6\n"
"buffer_load_dwordx4 v[244:247], v10, s[32:35], s74 offen sc1\n"
"s_sleep 1\n"
"s_add_u32 s74, s63, 32768\n"
"buffer_load_dwordx4 v[104:107], v9, s[32:35], s74 offen sc1\n"
"s_nop 9\n"
"buffer_load_dwordx4 v[108:111], v10, s[32:35], s74 offen sc1\n"
"s_sleep 1\n"
"s_add_u32 s74, s63, 36864\n"
"buffer_load_dwordx4 v[248:251], v9, s[32:35], s74 offen sc1\n"
"s_nop 9\n"
"buffer_load_dwordx4 v[252:255], v10, s[32:35], s74 offen sc1\n"
"s_sleep 1\n"
"s_add_u32 s74, s64, 32768\n"
"buffer_load_dwordx4 v[112:115], v9, s[32:35], s74 offen sc1\n"
"s_nop 9\n"
"buffer_load_dwordx4 v[116:119], v10, s[32:35], s74 offen sc1\n"
"s_sleep 1\n"
"s_add_u32 s74, s64, 36864\n"
"buffer_load_dwordx4 v[208:211], v9, s[32:35], s74 offen sc1\n"
"s_nop 9\n"
"buffer_load_dwordx4 v[212:215], v10, s[32:35], s74 offen sc1\n"
"s_sleep 1\n"
"s_add_u32 s74, s65, 32768\n"
"buffer_load_dwordx4 v[120:123], v9, s[32:35], s74 offen sc1\n"
"s_nop 9\n"
"buffer_load_dwordx4 v[124:127], v10, s[32:35], s74 offen sc1\n"
"s_sleep 1\n"
"s_add_u32 s74, s65, 36864\n"
"buffer_load_dwordx4 v[216:219], v9, s[32:35], s74 offen sc1\n"
"s_nop 9\n"
"buffer_load_dwordx4 v[220:223], v10, s[32:35], s74 offen sc1\n"
"s_sleep 1\n"
"s_add_u32 s74, s62, 65536\n"
"buffer_load_dwordx4 v[128:131], v9, s[32:35], s74 offen sc1\n"
"s_nop 9\n"
"buffer_load_dwordx4 v[132:135], v10, s[32:35], s74 offen sc1\n"
"s_sleep 1\n"
"s_add_u32 s74, s62, 69632\n"
"buffer_load_dwordx4 v[200:203], v9, s[32:35], s74 offen sc1\n"
"s_nop 9\n"
"buffer_load_dwordx4 v[204:207], v10, s[32:35], s74 offen sc1\n"
"s_sleep 1\n"
"s_add_u32 s74, s63, 65536\n"
"buffer_load_dwordx4 v[136:139], v9, s[32:35], s74 offen sc1\n"
"s_nop 9\n"
"buffer_load_dwordx4 v[140:143], v10, s[32:35], s74 offen sc1\n"
"buffer_load_dwordx2 v[12:13], v11, s[32:35], 0 offen sc1\n"
"s_waitcnt vmcnt(22)\n"
"v_mfma_f32_32x32x16_bf16 v[32:47], v[148:151], v[96:99], v[32:47]\n"
"s_waitcnt vmcnt(21)\n"
"v_mfma_f32_32x32x16_bf16 v[48:63], v[144:147], v[100:103], v[48:63]\n"
"v_mfma_f32_32x32x16_bf16 v[64:79], v[148:151], v[100:103], v[64:79]\n"
"v_mfma_f32_32x32x16_bf16 v[16:31], v[144:147], v[96:99], v[16:31]\n"
"s_add_u32 s74, s63, 69632\n"
"buffer_load_dwordx4 v[96:99], v9, s[32:35], s74 offen sc1\n"
"buffer_load_dwordx4 v[100:103], v10, s[32:35], s74 offen sc1\n"
"s_waitcnt vmcnt(22)\n"
"v_mfma_f32_32x32x16_bf16 v[32:47], v[156:159], v[240:243], v[32:47]\n"
"s_waitcnt vmcnt(21)\n"
"v_mfma_f32_32x32x16_bf16 v[48:63], v[152:155], v[244:247], v[48:63]\n"
"v_mfma_f32_32x32x16_bf16 v[64:79], v[156:159], v[244:247], v[64:79]\n"
"s_add_u32 s74, s64, 65536\n"
"buffer_load_dwordx4 v[144:147], v9, s[32:35], s74 offen sc1\n"
"v_mfma_f32_32x32x16_bf16 v[16:31], v[152:155], v[240:243], v[16:31]\n"
"buffer_load_dwordx4 v[148:151], v10, s[32:35], s74 offen sc1\n"
"s_sleep 1\n"
"s_add_u32 s74, s64, 69632\n"
"buffer_load_dwordx4 v[240:243], v9, s[32:35], s74 offen sc1\n"
"s_waitcnt vmcnt(23)\n"
"v_mfma_f32_32x32x16_bf16 v[32:47], v[164:167], v[104:107], v[32:47]\n"
"s_waitcnt vmcnt(22)\n"
"v_mfma_f32_32x32x16_bf16 v[48:63], v[160:163], v[108:111], v[48:63]\n"
"buffer_load_dwordx4 v[244:247], v10, s[32:35], s74 offen sc1\n"
"v_mfma_f32_32x32x16_bf16 v[64:79], v[164:167], v[108:111], v[64:79]\n"
"v_mfma_f32_32x32x16_bf16 v[16:31], v[160:163], v[104:107], v[16:31]\n"
"s_add_u32 s74, s65, 65536\n"
"buffer_load_dwordx4 v[152:155], v9, s[32:35], s74 offen sc1\n"
"s_waitcnt vmcnt(23)\n"
"v_mfma_f32_32x32x16_bf16 v[32:47], v[172:175], v[248:251], v[32:47]\n"
"s_waitcnt vmcnt(22)\n"
"v_mfma_f32_32x32x16_bf16 v[48:63], v[168:171], v[252:255], v[48:63]\n"
"buffer_load_dwordx4 v[156:159], v10, s[32:35], s74 offen sc1\n"
"v_mfma_f32_32x32x16_bf16 v[64:79], v[172:175], v[252:255], v[64:79]\n"
"v_mfma_f32_32x32x16_bf16 v[16:31], v[168:171], v[248:251], v[16:31]\n"
"s_add_u32 s74, s65, 69632\n"
"buffer_load_dwordx4 v[104:107], v9, s[32:35], s74 offen sc1\n"
"s_waitcnt vmcnt(23)\n"
"v_mfma_f32_32x32x16_bf16 v[32:47], v[180:183], v[112:115], v[32:47]\n"
"s_waitcnt vmcnt(22)\n"
"v_mfma_f32_32x32x16_bf16 v[48:63], v[176:179], v[116:119], v[48:63]\n"
"buffer_load_dwordx4 v[108:111], v10, s[32:35], s74 offen sc1\n"
"v_mfma_f32_32x32x16_bf16 v[64:79], v[180:183], v[116:119], v[64:79]\n"
"v_mfma_f32_32x32x16_bf16 v[16:31], v[176:179], v[112:115], v[16:31]\n"
"s_waitcnt vmcnt(22)\n"
"v_mfma_f32_32x32x16_bf16 v[32:47], v[188:191], v[208:211], v[32:47]\n"
"s_waitcnt vmcnt(21)\n"
"v_mfma_f32_32x32x16_bf16 v[48:63], v[184:187], v[212:215], v[48:63]\n"
"v_mfma_f32_32x32x16_bf16 v[64:79], v[188:191], v[212:215], v[64:79]\n"
"v_mfma_f32_32x32x16_bf16 v[16:31], v[184:187], v[208:211], v[16:31]\n"
"s_waitcnt vmcnt(20)\n"
"v_mfma_f32_32x32x16_bf16 v[32:47], v[196:199], v[120:123], v[32:47]\n"
"s_waitcnt vmcnt(19)\n"
"v_mfma_f32_32x32x16_bf16 v[48:63], v[192:195], v[124:127], v[48:63]\n"
"v_mfma_f32_32x32x16_bf16 v[64:79], v[196:199], v[124:127], v[64:79]\n"
"s_waitcnt vmcnt(10)\n"
"v_cmp_eq_u64 vcc, s[40:41], v[12:13]\n"
"s_nop 0\n"
"s_and_b32 s74, vcc_lo, 0xf00\n"
"s_cmp_eq_u32 s74, 0xf00\n"
"s_cbranch_scc0 LHA_slow_9\n"
"LHA_back_10:\n"
"s_lshl_b32 s74, s42, 12\n"
"s_sub_u32 s74, s62, s74\n"
"s_add_u32 s74, s74, 98304\n"
"buffer_load_dwordx4 v[160:163], v9, s[32:35], s74 offen sc1\n"
"buffer_load_dwordx4 v[164:167], v10, s[32:35], s74 offen sc1\n"
"s_lshl_b32 s74, s42, 12\n"
"s_sub_u32 s74, s63, s74\n"
"s_add_u32 s74, s74, 98304\n"
"buffer_load_dwordx4 v[248:251], v9, s[32:35], s74 offen sc1\n"
"buffer_load_dwordx4 v[252:255], v10, s[32:35], s74 offen sc1\n"
"v_mfma_f32_32x32x16_bf16 v[16:31], v[192:195], v[120:123], v[16:31]\n"
"v_mfma_f32_32x32x16_bf16 v[32:47], v[84:87], v[216:219], v[32:47]\n"
"v_mfma_f32_32x32x16_bf16 v[48:63], v[80:83], v[220:223], v[48:63]\n"
"s_lshl_b32 s74, s42, 12\n"
"s_sub_u32 s74, s64, s74\n"
"s_add_u32 s74, s74, 98304\n"
"buffer_load_dwordx4 v[168:171], v9, s[32:35], s74 offen sc1\n"
"v_mfma_f32_32x32x16_bf16 v[64:79], v[84:87], v[220:223], v[64:79]\n"
"v_mfma_f32_32x32x16_bf16 v[16:31], v[80:83], v[216:219], v[16:31]\n"
"buffer_load_dwordx4 v[172:175], v10, s[32:35], s74 offen sc1\n"
"ds_read_b64_tr_b16 v[176:177], v7 offset:16384\n"
"ds_read_b64_tr_b16 v[178:179], v7 offset:17408\n"
"ds_read_b64_tr_b16 v[180:181], v8 offset:16384\n"
"ds_read_b64_tr_b16 v[182:183], v8 offset:17408\n"
"v_mfma_f32_32x32x16_bf16 v[32:47], v[228:231], v[128:131], v[32:47]\n"
"v_mfma_f32_32x32x16_bf16 v[48:63], v[224:227], v[132:135], v[48:63]\n"
"s_lshl_b32 s74, s42, 12\n"
"s_sub_u32 s74, s65, s74\n"
"s_add_u32 s74, s74, 98304\n"
"buffer_load_dwordx4 v[112:115], v9, s[32:35], s74 offen sc1\n"
"v_mfma_f32_32x32x16_bf16 v[64:79], v[228:231], v[132:135], v[64:79]\n"
"v_mfma_f32_32x32x16_bf16 v[16:31], v[224:227], v[128:131], v[16:31]\n"
"buffer_load_dwordx4 v[116:119], v10, s[32:35], s74 offen sc1\n"
"v_lshlrev_b32 v5, 4, v0\n"
"v_and_b32 v2, 31, v0\n"
"v_lshlrev_b32 v2, 10, v2\n"
"v_lshrrev_b32 v10, 5, v0\n"
"v_lshl_add_u32 v2, v10, 4, v2\n"
"s_lshl_b32 s72, s46, 16\n"
"s_lshr_b32 s50, s42, 1\n"
"s_lshl_b32 s50, s50, 15\n"
"s_add_u32 s72, s72, s50\n"
"s_lshl_b32 s50, s47, 7\n"
"s_add_u32 s72, s72, s50\n"
"s_and_b32 s50, s42, 1\n"
"s_lshl_b32 s50, s50, 6\n"
"s_add_u32 s72, s72, s50\n"
"s_xor_b32 s49, s42, 1\n"
"s_mul_i32 s49, s49, 18432\n"
"s_add_u32 s49, s49, 81920\n"
"v_add_u32 v11, s49, v5\n"
"s_xor_b32 s49, s42, 2\n"
"s_mul_i32 s49, s49, 18432\n"
"s_add_u32 s49, s49, 86016\n"
"v_add_u32 v12, s49, v5\n"
"s_xor_b32 s49, s42, 3\n"
"s_mul_i32 s49, s49, 18432\n"
"s_add_u32 s49, s49, 90112\n"
"v_add_u32 v13, s49, v5\n"
"s_xor_b32 s49, s42, 1\n"
"s_lshl_b32 s49, s49, 12\n"
"s_add_u32 s49, s49, 65536\n"
"v_add_u32 v14, s49, v5\n"
"s_lshl_b32 s49, s42, 11\n"
"s_add_u32 s49, s49, 155648\n"
"v_add_u32 v15, s49, v5\n"
"s_lshr_b32 s49, s42, 1\n"
"s_lshl_b32 s49, s49, 7\n"
"s_add_u32 s49, s49, 96256\n"
"v_and_b32 v10, 31, v0\n"
"v_lshl_add_u32 v10, v10, 2, s49\n"
"v_add_u32 v1, s59, v5\n"
"ds_read_b64_tr_b16 v[208:209], v7 offset:6144\n"
"ds_read_b64_tr_b16 v[210:211], v7 offset:7168\n"
"ds_read_b64_tr_b16 v[212:213], v8 offset:6144\n"
"ds_read_b64_tr_b16 v[214:215], v8 offset:7168\n"
"v_mfma_f32_32x32x16_bf16 v[32:47], v[92:95], v[200:203], v[32:47]\n"
"v_mfma_f32_32x32x16_bf16 v[48:63], v[88:91], v[204:207], v[48:63]\n"
"v_mfma_f32_32x32x16_bf16 v[64:79], v[92:95], v[204:207], v[64:79]\n"
"v_mfma_f32_32x32x16_bf16 v[16:31], v[88:91], v[200:203], v[16:31]\n"
"ds_read_b64_tr_b16 v[184:185], v7 offset:8192\n"
"ds_read_b64_tr_b16 v[186:187], v7 offset:9216\n"
"ds_read_b64_tr_b16 v[188:189], v8 offset:8192\n"
"ds_read_b64_tr_b16 v[190:191], v8 offset:9216\n"
"v_mfma_f32_32x32x16_bf16 v[32:47], v[236:239], v[136:139], v[32:47]\n"
"v_mfma_f32_32x32x16_bf16 v[48:63], v[232:235], v[140:143], v[48:63]\n"
"v_mfma_f32_32x32x16_bf16 v[64:79], v[236:239], v[140:143], v[64:79]\n"
"v_mfma_f32_32x32x16_bf16 v[16:31], v[232:235], v[136:139], v[16:31]\n"
"ds_read_b64_tr_b16 v[120:121], v7 offset:10240\n"
"ds_read_b64_tr_b16 v[122:123], v7 offset:11264\n"
"ds_read_b64_tr_b16 v[124:125], v8 offset:10240\n"
"s_waitcnt lgkmcnt(14)\n"
"ds_read_b64_tr_b16 v[126:127], v8 offset:11264\n"
"s_waitcnt vmcnt(17) lgkmcnt(12)\n"
"v_mfma_f32_32x32x16_bf16 v[32:47], v[180:183], v[96:99], v[32:47]\n"
"s_waitcnt vmcnt(16)\n"
"v_mfma_f32_32x32x16_bf16 v[48:63], v[176:179], v[100:103], v[48:63]\n"
"v_mfma_f32_32x32x16_bf16 v[64:79], v[180:183], v[100:103], v[64:79]\n"
"v_mfma_f32_32x32x16_bf16 v[16:31], v[176:179], v[96:99], v[16:31]\n"
"ds_read_b64_tr_b16 v[192:193], v7 offset:4096\n"
"ds_read_b64_tr_b16 v[194:195], v7 offset:5120\n"
"ds_read_b64_tr_b16 v[196:197], v8 offset:4096\n"
"s_waitcnt lgkmcnt(14)\n"
"ds_read_b64_tr_b16 v[198:199], v8 offset:5120\n"
"s_waitcnt vmcnt(15) lgkmcnt(12)\n"
"v_mfma_f32_32x32x16_bf16 v[32:47], v[212:215], v[144:147], v[32:47]\n"
"s_waitcnt vmcnt(14)\n"
"v_mfma_f32_32x32x16_bf16 v[48:63], v[208:211], v[148:151], v[48:63]\n"
"v_mfma_f32_32x32x16_bf16 v[64:79], v[212:215], v[148:151], v[64:79]\n"
"v_mfma_f32_32x32x16_bf16 v[16:31], v[208:211], v[144:147], v[16:31]\n"
"ds_read_b64_tr_b16 v[216:217], v7 offset:12288\n"
"ds_read_b64_tr_b16 v[218:219], v7 offset:13312\n"
"ds_read_b64_tr_b16 v[220:221], v8 offset:12288\n"
"s_waitcnt lgkmcnt(14)\n"
"ds_read_b64_tr_b16 v[222:223], v8 offset:13312\n"
"s_waitcnt vmcnt(13) lgkmcnt(12)\n"
"v_mfma_f32_32x32x16_bf16 v[32:47], v[188:191], v[240:243], v[32:47]\n"
"s_waitcnt vmcnt(12)\n"
"v_mfma_f32_32x32x16_bf16 v[48:63], v[184:187], v[244:247], v[48:63]\n"
"v_mfma_f32_32x32x16_bf16 v[64:79], v[188:191], v[244:247], v[64:79]\n"
"v_mfma_f32_32x32x16_bf16 v[16:31], v[184:187], v[240:243], v[16:31]\n"
"ds_read_b64_tr_b16 v[80:81], v7 offset:14336\n"
"ds_read_b64_tr_b16 v[82:83], v7 offset:15360\n"
"LPAD2:\n"
"s_cbranch_execz LBKA\n"
"ds_read_b64_tr_b16 v[84:85], v8 offset:14336\n"
"s_waitcnt lgkmcnt(14)\n"
"ds_read_b64_tr_b16 v[86:87], v8 offset:15360\n"
"s_waitcnt vmcnt(11) lgkmcnt(12)\n"
"v_mfma_f32_32x32x16_bf16 v[32:47], v[124:127], v[152:155], v[32:47]\n"
"s_waitcnt vmcnt(10)\n"
"v_mfma_f32_32x32x16_bf16 v[48:63], v[120:123], v[156:159], v[48:63]\n"
"v_mfma_f32_32x32x16_bf16 v[64:79], v[124:127], v[156:159], v[64:79]\n"
"v_mfma_f32_32x32x16_bf16 v[16:31], v[120:123], v[152:155], v[16:31]\n"
"ds_read_b64_tr_b16 v[128:129], v7 offset:2048\n"
"ds_read_b64_tr_b16 v[130:131], v7 offset:3072\n"
"ds_read_b64_tr_b16 v[132:133], v8 offset:2048\n"
"s_waitcnt lgkmcnt(14)\n"
"ds_read_b64_tr_b16 v[134:135], v8 offset:3072\n"
"s_waitcnt vmcnt(9) lgkmcnt(12)\n"
"v_mfma_f32_32x32x16_bf16 v[32:47], v[196:199], v[104:107], v[32:47]\n"
"s_waitcnt vmcnt(8)\n"
"v_mfma_f32_32x32x16_bf16 v[48:63], v[192:195], v[108:111], v[48:63]\n"
"v_mfma_f32_32x32x16_bf16 v[64:79], v[196:199], v[108:111], v[64:79]\n"
"v_mfma_f32_32x32x16_bf16 v[16:31], v[192:195], v[104:107], v[16:31]\n"
"ds_read_b64_tr_b16 v[224:225], v7 offset:0\n"
"ds_read_b64_tr_b16 v[226:227], v7 offset:1024\n"
"ds_read_b64_tr_b16 v[228:229], v8 offset:0\n"
"s_waitcnt lgkmcnt(14)\n"
"ds_read_b64_tr_b16 v[230:231], v8 offset:1024\n"
"s_waitcnt vmcnt(7) lgkmcnt(12)\n"
"v_mfma_f32_32x32x16_bf16 v[32:47], v[220:223], v[160:163], v[32:47]\n"
"s_waitcnt vmcnt(6)\n"
"v_mfma_f32_32x32x16_bf16 v[48:63], v[216:219], v[164:167], v[48:63]\n"
"v_mfma_f32_32x32x16_bf16 v[64:79], v[220:223], v[164:167], v[64:79]\n"
"v_mfma_f32_32x32x16_bf16 v[16:31], v[216:219], v[160:163], v[16:31]\n"
"s_waitcnt vmcnt(5) lgkmcnt(8)\n"
"v_mfma_f32_32x32x16_bf16 v[32:47], v[84:87], v[248:251], v[32:47]\n"
"s_waitcnt vmcnt(4)\n"
"v_mfma_f32_32x32x16_bf16 v[48:63], v[80:83], v[252:255], v[48:63]\n"
"v_mfma_f32_32x32x16_bf16 v[64:79], v[84:87], v[252:255], v[64:79]\n"
"v_mfma_f32_32x32x16_bf16 v[16:31], v[80:83], v[248:251], v[16:31]\n"
"s_waitcnt vmcnt(3) lgkmcnt(4)\n"
"v_mfma_f32_32x32x16_bf16 v[32:47], v[132:135], v[168:171], v[32:47]\n"
"s_waitcnt vmcnt(2)\n"
"v_mfma_f32_32x32x16_bf16 v[48:63], v[128:131], v[172:175], v[48:63]\n"
"v_mfma_f32_32x32x16_bf16 v[64:79], v[132:135], v[172:175], v[64:79]\n"
"v_mfma_f32_32x32x16_bf16 v[16:31], v[128:131], v[168:171], v[16:31]\n"
"s_waitcnt vmcnt(1) lgkmcnt(0)\n"
"v_mfma_f32_32x32x16_bf16 v[32:47], v[228:231], v[112:115], v[32:47]\n"
"s_waitcnt vmcnt(0)\n"
"v_mfma_f32_32x32x16_bf16 v[48:63], v[224:227], v[116:119], v[48:63]\n"
"v_mfma_f32_32x32x16_bf16 v[64:79], v[228:231], v[116:119], v[64:79]\n"
"v_mfma_f32_32x32x16_bf16 v[16:31], v[224:227], v[112:115], v[16:31]\n"
"v_add_f32 v3, v3, v4\n"
"v_lshlrev_b32 v4, 2, v0\n"
"v_add_u32 v4, s59, v4\n"
"ds_write_b32 v4, v3 offset:14336\n"
"s_nop 3\n"
"ds_write_b128 v1, v[32:35] offset:0\n"
"ds_write_b128 v1, v[36:39] offset:1024\n"
"ds_write_b128 v1, v[40:43] offset:2048\n"
"ds_write_b128 v1, v[44:47] offset:3072\n"
"ds_write_b128 v1, v[48:51] offset:4096\n"
"ds_write_b128 v1, v[52:55] offset:5120\n"
"ds_write_b128 v1, v[56:59] offset:6144\n"
"ds_write_b128 v1, v[60:63] offset:7168\n"
"ds_write_b128 v1, v[64:67] offset:8192\n"
"ds_write_b128 v1, v[68:71] offset:9216\n"
"ds_write_b128 v1, v[72:75] offset:10240\n"
"ds_write_b128 v1, v[76:79] offset:11264\n"
"ds_write_b128 v1, v[24:27] offset:12288\n"
"ds_write_b128 v1, v[28:31] offset:13312\n"
"s_waitcnt lgkmcnt(0)\n"
"s_barrier\n"
"ds_read_b32 v120, v10 offset:0\n"
"ds_read_b32 v121, v10 offset:18432\n"
"ds_read_b32 v122, v10 offset:36864\n"
"ds_read_b32 v123, v10 offset:55296\n"
"ds_read_b128 v[104:107], v14 offset:0\n"
"ds_read_b128 v[108:111], v14 offset:1024\n"
"ds_read_b128 v[112:115], v15 offset:0\n"
"ds_read_b128 v[116:119], v15 offset:1024\n"
"ds_read_b128 v[80:83], v11 offset:0\n"
"ds_read_b128 v[84:87], v11 offset:1024\n"
"ds_read_b128 v[88:91], v12 offset:0\n"
"ds_read_b128 v[92:95], v12 offset:1024\n"
"ds_read_b128 v[96:99], v13 offset:0\n"
"ds_read_b128 v[100:103], v13 offset:1024\n"
"s_waitcnt lgkmcnt(12)\n"
"v_add_f32 v120, v120, v121\n"
"s_waitcnt lgkmcnt(10)\n"
"v_add_f32 v122, v122, v123\n"
"v_add_f32 v120, v120, v122\n"
"v_mul_f32 v120, 0xbfb8aa3b, v120\n"
"v_exp_f32 v120, v120\n"
"s_nop 0\n"
"v_add_f32 v120, 1.0, v120\n"
"v_rcp_f32 v120, v120\n"
"s_waitcnt lgkmcnt(7)\n"
"v_add_f32 v104, v104, v112\n"
"v_add_f32 v105, v105, v113\n"
"v_add_f32 v106, v106, v114\n"
"v_add_f32 v107, v107, v115\n"
"s_waitcnt lgkmcnt(6)\n"
"v_add_f32 v108, v108, v116\n"
"v_add_f32 v109, v109, v117\n"
"v_add_f32 v110, v110, v118\n"
"v_add_f32 v111, v111, v119\n"
"s_waitcnt lgkmcnt(5)\n"
"v_add_f32 v16, v16, v80\n"
"v_add_f32 v17, v17, v81\n"
"v_add_f32 v18, v18, v82\n"
"v_add_f32 v19, v19, v83\n"
"s_waitcnt lgkmcnt(4)\n"
"v_add_f32 v20, v20, v84\n"
"v_add_f32 v21, v21, v85\n"
"v_add_f32 v22, v22, v86\n"
"v_add_f32 v23, v23, v87\n"
"s_waitcnt lgkmcnt(3)\n"
"v_add_f32 v16, v16, v88\n"
"v_add_f32 v17, v17, v89\n"
"v_add_f32 v18, v18, v90\n"
"v_add_f32 v19, v19, v91\n"
"s_waitcnt lgkmcnt(2)\n"
"v_add_f32 v20, v20, v92\n"
"v_add_f32 v21, v21, v93\n"
"v_add_f32 v22, v22, v94\n"
"v_add_f32 v23, v23, v95\n"
"s_waitcnt lgkmcnt(1)\n"
"v_add_f32 v16, v16, v96\n"
"v_add_f32 v17, v17, v97\n"
"v_add_f32 v18, v18, v98\n"
"v_add_f32 v19, v19, v99\n"
"s_waitcnt lgkmcnt(0)\n"
"v_add_f32 v20, v20, v100\n"
"v_add_f32 v21, v21, v101\n"
"v_add_f32 v22, v22, v102\n"
"v_add_f32 v23, v23, v103\n"
"v_sub_f32 v104, v104, v16\n"
"v_sub_f32 v105, v105, v17\n"
"v_sub_f32 v106, v106, v18\n"
"v_sub_f32 v107, v107, v19\n"
"v_sub_f32 v108, v108, v20\n"
"v_sub_f32 v109, v109, v21\n"
"v_sub_f32 v110, v110, v22\n"
"v_sub_f32 v111, v111, v23\n"
"v_fma_f32 v16, v120, v104, v16\n"
"v_fma_f32 v17, v120, v105, v17\n"
"v_fma_f32 v18, v120, v106, v18\n"
"v_fma_f32 v19, v120, v107, v19\n"
"v_fma_f32 v20, v120, v108, v20\n"
"v_fma_f32 v21, v120, v109, v21\n"
"v_fma_f32 v22, v120, v110, v22\n"
"v_fma_f32 v23, v120, v111, v23\n"
"v_cvt_pk_bf16_f32 v16, v16, v17\n"
"v_cvt_pk_bf16_f32 v17, v18, v19\n"
"v_cvt_pk_bf16_f32 v18, v20, v21\n"
"v_cvt_pk_bf16_f32 v19, v22, v23\n"
"s_nop 0\n"
"v_permlane32_swap_b32 v16, v18\n"
"v_permlane32_swap_b32 v17, v19\n"
"buffer_store_dwordx4 v[16:19], v2, s[36:39], s72 offen\n"
"s_endpgm\n"
"LPAD3:\n"
"s_cbranch_execz LPAD6\n"
"LTHA:\n"
"LHA_slow_1:\n"
"s_mov_b32 s75, 131072\n"
"LHA_slow_1l:\n"
"buffer_load_dwordx2 v[12:13], v11, s[32:35], 0 offen sc1\n"
"s_waitcnt vmcnt(0)\n"
"v_cmp_eq_u64 vcc, s[40:41], v[12:13]\n"
"s_nop 1\n"
"s_and_b32 s74, vcc_lo, 0xf\n"
"s_cmp_eq_u32 s74, 0xf\n"
"s_cbranch_scc1 LHA_back_2\n"
"s_sleep 1\n"
"s_sub_u32 s75, s75, 1\n"
"s_cmp_lg_u32 s75, 0\n"
"s_cbranch_scc1 LHA_slow_1l\n"
"s_branch LHA_back_2\n"
"LHA_slow_3:\n"
"s_mov_b32 s75, 131072\n"
"LHA_slow_3l:\n"
"buffer_load_dwordx2 v[42:43], v40, s[32:35], 0 offen sc1\n"
"s_waitcnt vmcnt(0)\n"
"v_cmp_eq_u64 vcc, s[40:41], v[42:43]\n"
"s_nop 1\n"
"s_and_b32 s74, vcc_lo, 0xffff\n"
"s_cmp_eq_u32 s74, 0xffff\n"
"s_cbranch_scc1 LHA_back_4\n"
"s_sleep 1\n"
"s_sub_u32 s75, s75, 1\n"
"s_cmp_lg_u32 s75, 0\n"
"s_cbranch_scc1 LHA_slow_3l\n"
"s_branch LHA_back_4\n"
"LHA_slow_5:\n"
"s_mov_b32 s75, 131072\n"
"LHA_slow_5l:\n"
"buffer_load_dwordx2 v[12:13], v11, s[32:35], 0 offen sc1\n"
"s_waitcnt vmcnt(0)\n"
"v_cmp_eq_u64 vcc, s[40:41], v[12:13]\n"
"s_nop 1\n"
"s_and_b32 s74, vcc_lo, 0xf0\n"
"s_cmp_eq_u32 s74, 0xf0\n"
"s_cbranch_scc1 LHA_back_6\n"
"s_sleep 1\n"
"s_sub_u32 s75, s75, 1\n"
"s_cmp_lg_u32 s75, 0\n"
"s_cbranch_scc1 LHA_slow_5l\n"
"s_branch LHA_back_6\n"
"LHA_slow_7:\n"
"s_mov_b32 s75, 131072\n"
"LHA_slow_7l:\n"
"buffer_load_dwordx2 v[12:13], v11, s[32:35], 0 offen sc1\n"
"s_waitcnt vmcnt(0)\n"
"v_cmp_eq_u64 vcc, s[40:41], v[12:13]\n"
"s_nop 1\n"
"s_and_b32 s74, vcc_lo, 0xf0000\n"
"s_cmp_eq_u32 s74, 0xf0000\n"
"s_cbranch_scc1 LHA_back_8\n"
"s_sleep 1\n"
"s_sub_u32 s75, s75, 1\n"
"s_cmp_lg_u32 s75, 0\n"
"s_cbranch_scc1 LHA_slow_7l\n"
"s_branch LHA_back_8\n"
"LHA_slow_9:\n"
"s_mov_b32 s75, 131072\n"
"LHA_slow_9l:\n"
"buffer_load_dwordx2 v[12:13], v11, s[32:35], 0 offen sc1\n"
"s_waitcnt vmcnt(0)\n"
"v_cmp_eq_u64 vcc, s[40:41], v[12:13]\n"
"s_nop 1\n"
"s_and_b32 s74, vcc_lo, 0xf00\n"
"s_cmp_eq_u32 s74, 0xf00\n"
"s_cbranch_scc1 LHA_back_10\n"
"s_sleep 1\n"
"s_sub_u32 s75, s75, 1\n"
"s_cmp_lg_u32 s75, 0\n"
"s_cbranch_scc1 LHA_slow_9l\n"
"s_branch LHA_back_10\n"
"LBCA:\n"
"s_barrier\n"
"ds_read_b128 v[128:131], v96 offset:0\n"
"ds_read_b64_tr_b16 v[16:17], v1 offset:0\n"
"ds_read_b64_tr_b16 v[18:19], v1 offset:512\n"
"ds_read_b64_tr_b16 v[20:21], v2 offset:0\n"
"ds_read_b64_tr_b16 v[22:23], v2 offset:512\n"
"ds_read_b64_tr_b16 v[24:25], v1 offset:8192\n"
"ds_read_b64_tr_b16 v[26:27], v1 offset:8704\n"
"ds_read_b64_tr_b16 v[28:29], v2 offset:8192\n"
"ds_read_b64_tr_b16 v[30:31], v2 offset:8704\n"
"s_waitcnt lgkmcnt(6)\n"
"v_mfma_f32_32x32x16_bf16 v[32:47], v[128:131], v[16:19], 0\n"
"s_waitcnt vmcnt(0)\n"
"v_mfma_f32_4x4x4_16b_bf16 v[8:11], v[100:101], v[128:129], 0 cbsz:3 abid:0\n"
"ds_read_b128 v[132:135], v97 offset:0\n"
"ds_read_b64_tr_b16 v[16:17], v1 offset:2048\n"
"ds_read_b64_tr_b16 v[18:19], v1 offset:2560\n"
"s_waitcnt lgkmcnt(7)\n"
"v_mfma_f32_32x32x16_bf16 v[48:63], v[128:131], v[20:23], 0\n"
"v_mfma_f32_4x4x4_16b_bf16 v[8:11], v[102:103], v[130:131], v[8:11] cbsz:3 abid:0\n"
"ds_read_b64_tr_b16 v[20:21], v2 offset:2048\n"
"ds_read_b64_tr_b16 v[22:23], v2 offset:2560\n"
"s_waitcnt lgkmcnt(7)\n"
"v_mfma_f32_32x32x16_bf16 v[64:79], v[128:131], v[24:27], 0\n"
"ds_read_b64_tr_b16 v[24:25], v1 offset:10240\n"
"ds_read_b64_tr_b16 v[26:27], v1 offset:10752\n"
"s_waitcnt lgkmcnt(7)\n"
"v_mfma_f32_32x32x16_bf16 v[80:95], v[128:131], v[28:31], 0\n"
"ds_read_b64_tr_b16 v[28:29], v2 offset:10240\n"
"ds_read_b64_tr_b16 v[30:31], v2 offset:10752\n"
"s_waitcnt lgkmcnt(6)\n"
"v_mfma_f32_32x32x16_bf16 v[32:47], v[132:135], v[16:19], v[32:47]\n"
"v_mfma_f32_4x4x4_16b_bf16 v[8:11], v[100:101], v[132:133], v[8:11] cbsz:3 abid:1\n"
"ds_read_b128 v[136:139], v98 offset:0\n"
"ds_read_b64_tr_b16 v[16:17], v1 offset:4096\n"
"ds_read_b64_tr_b16 v[18:19], v1 offset:4608\n"
"s_waitcnt lgkmcnt(7)\n"
"v_mfma_f32_32x32x16_bf16 v[48:63], v[132:135], v[20:23], v[48:63]\n"
"v_mfma_f32_4x4x4_16b_bf16 v[8:11], v[102:103], v[134:135], v[8:11] cbsz:3 abid:1\n"
"ds_read_b64_tr_b16 v[20:21], v2 offset:4096\n"
"ds_read_b64_tr_b16 v[22:23], v2 offset:4608\n"
"s_waitcnt lgkmcnt(7)\n"
"v_mfma_f32_32x32x16_bf16 v[64:79], v[132:135], v[24:27], v[64:79]\n"
"ds_read_b64_tr_b16 v[24:25], v1 offset:12288\n"
"ds_read_b64_tr_b16 v[26:27], v1 offset:12800\n"
"s_waitcnt lgkmcnt(7)\n"
"v_mfma_f32_32x32x16_bf16 v[80:95], v[132:135], v[28:31], v[80:95]\n"
"ds_read_b64_tr_b16 v[28:29], v2 offset:12288\n"
"ds_read_b64_tr_b16 v[30:31], v2 offset:12800\n"
"s_waitcnt lgkmcnt(6)\n"
"v_mfma_f32_32x32x16_bf16 v[32:47], v[136:139], v[16:19], v[32:47]\n"
"v_mfma_f32_4x4x4_16b_bf16 v[8:11], v[100:101], v[136:137], v[8:11] cbsz:3 abid:2\n"
"ds_read_b128 v[140:143], v99 offset:0\n"
"ds_read_b64_tr_b16 v[16:17], v1 offset:6144\n"
"ds_read_b64_tr_b16 v[18:19], v1 offset:6656\n"
"s_waitcnt lgkmcnt(7)\n"
"v_mfma_f32_32x32x16_bf16 v[48:63], v[136:139], v[20:23], v[48:63]\n"
"v_mfma_f32_4x4x4_16b_bf16 v[8:11], v[102:103], v[138:139], v[8:11] cbsz:3 abid:2\n"
"ds_read_b64_tr_b16 v[20:21], v2 offset:6144\n"
"ds_read_b64_tr_b16 v[22:23], v2 offset:6656\n"
"s_waitcnt lgkmcnt(7)\n"
"v_mfma_f32_32x32x16_bf16 v[64:79], v[136:139], v[24:27], v[64:79]\n"
"ds_read_b64_tr_b16 v[24:25], v1 offset:14336\n"
"ds_read_b64_tr_b16 v[26:27], v1 offset:14848\n"
"s_waitcnt lgkmcnt(7)\n"
"v_mfma_f32_32x32x16_bf16 v[80:95], v[136:139], v[28:31], v[80:95]\n"
"ds_read_b64_tr_b16 v[28:29], v2 offset:14336\n"
"ds_read_b64_tr_b16 v[30:31], v2 offset:14848\n"
"s_waitcnt lgkmcnt(6)\n"
"v_mfma_f32_32x32x16_bf16 v[32:47], v[140:143], v[16:19], v[32:47]\n"
"v_mfma_f32_4x4x4_16b_bf16 v[8:11], v[100:101], v[140:141], v[8:11] cbsz:3 abid:3\n"
"s_barrier\n"
"ds_read_b128 v[144:147], v96 offset:4096\n"
"ds_read_b64_tr_b16 v[16:17], v1 offset:16384\n"
"ds_read_b64_tr_b16 v[18:19], v1 offset:16896\n"
"s_waitcnt lgkmcnt(7)\n"
"v_mfma_f32_32x32x16_bf16 v[48:63], v[140:143], v[20:23], v[48:63]\n"
"v_mfma_f32_4x4x4_16b_bf16 v[8:11], v[102:103], v[142:143], v[8:11] cbsz:3 abid:3\n"
"ds_read_b64_tr_b16 v[20:21], v2 offset:16384\n"
"ds_read_b64_tr_b16 v[22:23], v2 offset:16896\n"
"s_waitcnt lgkmcnt(7)\n"
"v_mfma_f32_32x32x16_bf16 v[64:79], v[140:143], v[24:27], v[64:79]\n"
"ds_read_b64_tr_b16 v[24:25], v1 offset:24576\n"
"ds_read_b64_tr_b16 v[26:27], v1 offset:25088\n"
"s_waitcnt lgkmcnt(7)\n"
"v_mfma_f32_32x32x16_bf16 v[80:95], v[140:143], v[28:31], v[80:95]\n"
"ds_read_b64_tr_b16 v[28:29], v2 offset:24576\n"
"ds_read_b64_tr_b16 v[30:31], v2 offset:25088\n"
"s_waitcnt lgkmcnt(6)\n"
"v_mfma_f32_32x32x16_bf16 v[32:47], v[144:147], v[16:19], v[32:47]\n"
"ds_read_b128 v[148:151], v97 offset:4096\n"
"ds_read_b64_tr_b16 v[16:17], v1 offset:18432\n"
"ds_read_b64_tr_b16 v[18:19], v1 offset:18944\n"
"s_waitcnt lgkmcnt(7)\n"
"v_mfma_f32_32x32x16_bf16 v[48:63], v[144:147], v[20:23], v[48:63]\n"
"ds_read_b64_tr_b16 v[20:21], v2 offset:18432\n"
"ds_read_b64_tr_b16 v[22:23], v2 offset:18944\n"
"s_waitcnt lgkmcnt(7)\n"
"v_mfma_f32_32x32x16_bf16 v[64:79], v[144:147], v[24:27], v[64:79]\n"
"ds_read_b64_tr_b16 v[24:25], v1 offset:26624\n"
"ds_read_b64_tr_b16 v[26:27], v1 offset:27136\n"
"s_waitcnt lgkmcnt(7)\n"
"v_mfma_f32_32x32x16_bf16 v[80:95], v[144:147], v[28:31], v[80:95]\n"
"ds_read_b64_tr_b16 v[28:29], v2 offset:26624\n"
"ds_read_b64_tr_b16 v[30:31], v2 offset:27136\n"
"s_waitcnt lgkmcnt(6)\n"
"v_mfma_f32_32x32x16_bf16 v[32:47], v[148:151], v[16:19], v[32:47]\n"
"ds_read_b128 v[152:155], v98 offset:4096\n"
"ds_read_b64_tr_b16 v[16:17], v1 offset:20480\n"
"ds_read_b64_tr_b16 v[18:19], v1 offset:20992\n"
"v_permlane32_swap_b32 v8, v9\n"
"s_waitcnt lgkmcnt(7)\n"
"v_mfma_f32_32x32x16_bf16 v[48:63], v[148:151], v[20:23], v[48:63]\n"
"ds_read_b64_tr_b16 v[20:21], v2 offset:20480\n"
"ds_read_b64_tr_b16 v[22:23], v2 offset:20992\n"
"v_add_f32 v8, v8, v9\n"
"s_waitcnt lgkmcnt(7)\n"
"v_mfma_f32_32x32x16_bf16 v[64:79], v[148:151], v[24:27], v[64:79]\n"
"ds_read_b64_tr_b16 v[24:25], v1 offset:28672\n"
"ds_read_b64_tr_b16 v[26:27], v1 offset:29184\n"
"buffer_store_dword v8, v12, s[32:35], s59 offen sc1\n"
"s_waitcnt lgkmcnt(7)\n"
"v_mfma_f32_32x32x16_bf16 v[80:95], v[148:151], v[28:31], v[80:95]\n"
"ds_read_b64_tr_b16 v[28:29], v2 offset:28672\n"
"ds_read_b64_tr_b16 v[30:31], v2 offset:29184\n"
"s_waitcnt lgkmcnt(6)\n"
"v_mfma_f32_32x32x16_bf16 v[32:47], v[152:155], v[16:19], v[32:47]\n"
"ds_read_b128 v[156:159], v99 offset:4096\n"
"ds_read_b64_tr_b16 v[16:17], v1 offset:22528\n"
"ds_read_b64_tr_b16 v[18:19], v1 offset:23040\n"
"s_waitcnt lgkmcnt(7)\n"
"v_mfma_f32_32x32x16_bf16 v[48:63], v[152:155], v[20:23], v[48:63]\n"
"ds_read_b64_tr_b16 v[20:21], v2 offset:22528\n"
"ds_read_b64_tr_b16 v[22:23], v2 offset:23040\n"
"s_waitcnt lgkmcnt(7)\n"
"v_mfma_f32_32x32x16_bf16 v[64:79], v[152:155], v[24:27], v[64:79]\n"
"ds_read_b64_tr_b16 v[24:25], v1 offset:30720\n"
"ds_read_b64_tr_b16 v[26:27], v1 offset:31232\n"
"s_waitcnt lgkmcnt(7)\n"
"v_mfma_f32_32x32x16_bf16 v[80:95], v[152:155], v[28:31], v[80:95]\n"
"ds_read_b64_tr_b16 v[28:29], v2 offset:30720\n"
"ds_read_b64_tr_b16 v[30:31], v2 offset:31232\n"
"s_waitcnt lgkmcnt(6)\n"
"v_mfma_f32_32x32x16_bf16 v[32:47], v[156:159], v[16:19], v[32:47]\n"
"s_barrier\n"
"ds_read_b128 v[160:163], v96 offset:8192\n"
"ds_read_b64_tr_b16 v[16:17], v1 offset:32768\n"
"ds_read_b64_tr_b16 v[18:19], v1 offset:33280\n"
"s_waitcnt lgkmcnt(7)\n"
"v_mfma_f32_32x32x16_bf16 v[48:63], v[156:159], v[20:23], v[48:63]\n"
"ds_read_b64_tr_b16 v[20:21], v2 offset:32768\n"
"ds_read_b64_tr_b16 v[22:23], v2 offset:33280\n"
"s_waitcnt lgkmcnt(7)\n"
"v_mfma_f32_32x32x16_bf16 v[64:79], v[156:159], v[24:27], v[64:79]\n"
"ds_read_b64_tr_b16 v[24:25], v1 offset:40960\n"
"ds_read_b64_tr_b16 v[26:27], v1 offset:41472\n"
"s_waitcnt lgkmcnt(7)\n"
"v_mfma_f32_32x32x16_bf16 v[80:95], v[156:159], v[28:31], v[80:95]\n"
"ds_read_b64_tr_b16 v[28:29], v2 offset:40960\n"
"ds_read_b64_tr_b16 v[30:31], v2 offset:41472\n"
"s_waitcnt lgkmcnt(6)\n"
"v_mfma_f32_32x32x16_bf16 v[32:47], v[160:163], v[16:19], v[32:47]\n"
"ds_read_b128 v[164:167], v97 offset:8192\n"
"ds_read_b64_tr_b16 v[16:17], v1 offset:34816\n"
"ds_read_b64_tr_b16 v[18:19], v1 offset:35328\n"
"s_waitcnt lgkmcnt(7)\n"
"v_mfma_f32_32x32x16_bf16 v[48:63], v[160:163], v[20:23], v[48:63]\n"
"ds_read_b64_tr_b16 v[20:21], v2 offset:34816\n"
"ds_read_b64_tr_b16 v[22:23], v2 offset:35328\n"
"s_waitcnt lgkmcnt(7)\n"
"v_mfma_f32_32x32x16_bf16 v[64:79], v[160:163], v[24:27], v[64:79]\n"
"ds_read_b64_tr_b16 v[24:25], v1 offset:43008\n"
"ds_read_b64_tr_b16 v[26:27], v1 offset:43520\n"
"s_waitcnt lgkmcnt(7)\n"
"v_mfma_f32_32x32x16_bf16 v[80:95], v[160:163], v[28:31], v[80:95]\n"
"ds_read_b64_tr_b16 v[28:29], v2 offset:43008\n"
"ds_read_b64_tr_b16 v[30:31], v2 offset:43520\n"
"s_waitcnt lgkmcnt(6)\n"
"v_mfma_f32_32x32x16_bf16 v[32:47], v[164:167], v[16:19], v[32:47]\n"
"ds_read_b128 v[168:171], v98 offset:8192\n"
"ds_read_b64_tr_b16 v[16:17], v1 offset:36864\n"
"ds_read_b64_tr_b16 v[18:19], v1 offset:37376\n"
"s_waitcnt lgkmcnt(7)\n"
"v_mfma_f32_32x32x16_bf16 v[48:63], v[164:167], v[20:23], v[48:63]\n"
"ds_read_b64_tr_b16 v[20:21], v2 offset:36864\n"
"ds_read_b64_tr_b16 v[22:23], v2 offset:37376\n"
"s_waitcnt lgkmcnt(7)\n"
"v_mfma_f32_32x32x16_bf16 v[64:79], v[164:167], v[24:27], v[64:79]\n"
"ds_read_b64_tr_b16 v[24:25], v1 offset:45056\n"
"ds_read_b64_tr_b16 v[26:27], v1 offset:45568\n"
"s_waitcnt lgkmcnt(7)\n"
"v_mfma_f32_32x32x16_bf16 v[80:95], v[164:167], v[28:31], v[80:95]\n"
"ds_read_b64_tr_b16 v[28:29], v2 offset:45056\n"
"ds_read_b64_tr_b16 v[30:31], v2 offset:45568\n"
"s_waitcnt lgkmcnt(6)\n"
"v_mfma_f32_32x32x16_bf16 v[32:47], v[168:171], v[16:19], v[32:47]\n"
"ds_read_b128 v[172:175], v99 offset:8192\n"
"ds_read_b64_tr_b16 v[16:17], v1 offset:38912\n"
"ds_read_b64_tr_b16 v[18:19], v1 offset:39424\n"
"s_waitcnt lgkmcnt(7)\n"
"v_mfma_f32_32x32x16_bf16 v[48:63], v[168:171], v[20:23], v[48:63]\n"
"ds_read_b64_tr_b16 v[20:21], v2 offset:38912\n"
"ds_read_b64_tr_b16 v[22:23], v2 offset:39424\n"
"s_waitcnt lgkmcnt(7)\n"
"v_mfma_f32_32x32x16_bf16 v[64:79], v[168:171], v[24:27], v[64:79]\n"
"ds_read_b64_tr_b16 v[24:25], v1 offset:47104\n"
"ds_read_b64_tr_b16 v[26:27], v1 offset:47616\n"
"s_waitcnt lgkmcnt(7)\n"
"v_mfma_f32_32x32x16_bf16 v[80:95], v[168:171], v[28:31], v[80:95]\n"
"ds_read_b64_tr_b16 v[28:29], v2 offset:47104\n"
"ds_read_b64_tr_b16 v[30:31], v2 offset:47616\n"
"s_waitcnt lgkmcnt(6)\n"
"v_mfma_f32_32x32x16_bf16 v[32:47], v[172:175], v[16:19], v[32:47]\n"
"s_barrier\n"
"ds_read_b128 v[176:179], v96 offset:12288\n"
"ds_read_b64_tr_b16 v[16:17], v1 offset:49152\n"
"ds_read_b64_tr_b16 v[18:19], v1 offset:49664\n"
"s_waitcnt lgkmcnt(7)\n"
"v_mfma_f32_32x32x16_bf16 v[48:63], v[172:175], v[20:23], v[48:63]\n"
"ds_read_b64_tr_b16 v[20:21], v2 offset:49152\n"
"ds_read_b64_tr_b16 v[22:23], v2 offset:49664\n"
"s_waitcnt lgkmcnt(7)\n"
"v_mfma_f32_32x32x16_bf16 v[64:79], v[172:175], v[24:27], v[64:79]\n"
"ds_read_b64_tr_b16 v[24:25], v1 offset:57344\n"
"ds_read_b64_tr_b16 v[26:27], v1 offset:57856\n"
"s_waitcnt lgkmcnt(7)\n"
"v_mfma_f32_32x32x16_bf16 v[80:95], v[172:175], v[28:31], v[80:95]\n"
"ds_read_b64_tr_b16 v[28:29], v2 offset:57344\n"
"ds_read_b64_tr_b16 v[30:31], v2 offset:57856\n"
"s_waitcnt lgkmcnt(6)\n"
"v_mfma_f32_32x32x16_bf16 v[32:47], v[176:179], v[16:19], v[32:47]\n"
"ds_read_b128 v[180:183], v97 offset:12288\n"
"ds_read_b64_tr_b16 v[16:17], v1 offset:51200\n"
"ds_read_b64_tr_b16 v[18:19], v1 offset:51712\n"
"s_waitcnt lgkmcnt(7)\n"
"v_mfma_f32_32x32x16_bf16 v[48:63], v[176:179], v[20:23], v[48:63]\n"
"ds_read_b64_tr_b16 v[20:21], v2 offset:51200\n"
"ds_read_b64_tr_b16 v[22:23], v2 offset:51712\n"
"s_waitcnt lgkmcnt(7)\n"
"v_mfma_f32_32x32x16_bf16 v[64:79], v[176:179], v[24:27], v[64:79]\n"
"ds_read_b64_tr_b16 v[24:25], v1 offset:59392\n"
"ds_read_b64_tr_b16 v[26:27], v1 offset:59904\n"
"s_waitcnt lgkmcnt(7)\n"
"v_mfma_f32_32x32x16_bf16 v[80:95], v[176:179], v[28:31], v[80:95]\n"
"ds_read_b64_tr_b16 v[28:29], v2 offset:59392\n"
"ds_read_b64_tr_b16 v[30:31], v2 offset:59904\n"
"s_waitcnt lgkmcnt(6)\n"
"v_mfma_f32_32x32x16_bf16 v[32:47], v[180:183], v[16:19], v[32:47]\n"
"ds_read_b128 v[184:187], v98 offset:12288\n"
"ds_read_b64_tr_b16 v[16:17], v1 offset:53248\n"
"ds_read_b64_tr_b16 v[18:19], v1 offset:53760\n"
"s_waitcnt lgkmcnt(7)\n"
"v_mfma_f32_32x32x16_bf16 v[48:63], v[180:183], v[20:23], v[48:63]\n"
"ds_read_b64_tr_b16 v[20:21], v2 offset:53248\n"
"ds_read_b64_tr_b16 v[22:23], v2 offset:53760\n"
"s_waitcnt lgkmcnt(7)\n"
"v_mfma_f32_32x32x16_bf16 v[64:79], v[180:183], v[24:27], v[64:79]\n"
"ds_read_b64_tr_b16 v[24:25], v1 offset:61440\n"
"ds_read_b64_tr_b16 v[26:27], v1 offset:61952\n"
"s_waitcnt lgkmcnt(7)\n"
"v_mfma_f32_32x32x16_bf16 v[80:95], v[180:183], v[28:31], v[80:95]\n"
"ds_read_b64_tr_b16 v[28:29], v2 offset:61440\n"
"ds_read_b64_tr_b16 v[30:31], v2 offset:61952\n"
"s_waitcnt lgkmcnt(6)\n"
"v_mfma_f32_32x32x16_bf16 v[32:47], v[184:187], v[16:19], v[32:47]\n"
"ds_read_b128 v[188:191], v99 offset:12288\n"
"ds_read_b64_tr_b16 v[16:17], v1 offset:55296\n"
"ds_read_b64_tr_b16 v[18:19], v1 offset:55808\n"
"s_waitcnt lgkmcnt(7)\n"
"v_mfma_f32_32x32x16_bf16 v[48:63], v[184:187], v[20:23], v[48:63]\n"
"ds_read_b64_tr_b16 v[20:21], v2 offset:55296\n"
"ds_read_b64_tr_b16 v[22:23], v2 offset:55808\n"
"s_waitcnt lgkmcnt(7)\n"
"v_mfma_f32_32x32x16_bf16 v[64:79], v[184:187], v[24:27], v[64:79]\n"
"ds_read_b64_tr_b16 v[24:25], v1 offset:63488\n"
"ds_read_b64_tr_b16 v[26:27], v1 offset:64000\n"
"s_waitcnt lgkmcnt(7)\n"
"v_mfma_f32_32x32x16_bf16 v[80:95], v[184:187], v[28:31], v[80:95]\n"
"ds_read_b64_tr_b16 v[28:29], v2 offset:63488\n"
"ds_read_b64_tr_b16 v[30:31], v2 offset:64000\n"
"s_waitcnt lgkmcnt(6)\n"
"v_mfma_f32_32x32x16_bf16 v[32:47], v[188:191], v[16:19], v[32:47]\n"
"s_barrier\n"
"ds_read_b128 v[192:195], v96 offset:0\n"
"ds_read_b64_tr_b16 v[16:17], v3 offset:0\n"
"ds_read_b64_tr_b16 v[18:19], v3 offset:512\n"
"s_waitcnt lgkmcnt(7)\n"
"v_mfma_f32_32x32x16_bf16 v[48:63], v[188:191], v[20:23], v[48:63]\n"
"ds_read_b64_tr_b16 v[20:21], v4 offset:0\n"
"ds_read_b64_tr_b16 v[22:23], v4 offset:512\n"
"s_waitcnt lgkmcnt(7)\n"
"v_mfma_f32_32x32x16_bf16 v[64:79], v[188:191], v[24:27], v[64:79]\n"
"ds_read_b64_tr_b16 v[24:25], v3 offset:8192\n"
"ds_read_b64_tr_b16 v[26:27], v3 offset:8704\n"
"s_waitcnt lgkmcnt(7)\n"
"v_mfma_f32_32x32x16_bf16 v[80:95], v[188:191], v[28:31], v[80:95]\n"
"ds_read_b64_tr_b16 v[28:29], v4 offset:8192\n"
"ds_read_b64_tr_b16 v[30:31], v4 offset:8704\n"
"s_waitcnt lgkmcnt(6)\n"
"v_mfma_f32_32x32x16_bf16 v[32:47], v[192:195], v[16:19], v[32:47]\n"
"ds_read_b128 v[196:199], v97 offset:0\n"
"ds_read_b64_tr_b16 v[16:17], v3 offset:2048\n"
"ds_read_b64_tr_b16 v[18:19], v3 offset:2560\n"
"s_waitcnt lgkmcnt(7)\n"
"v_mfma_f32_32x32x16_bf16 v[48:63], v[192:195], v[20:23], v[48:63]\n"
"ds_read_b64_tr_b16 v[20:21], v4 offset:2048\n"
"ds_read_b64_tr_b16 v[22:23], v4 offset:2560\n"
"s_waitcnt lgkmcnt(7)\n"
"v_mfma_f32_32x32x16_bf16 v[64:79], v[192:195], v[24:27], v[64:79]\n"
"ds_read_b64_tr_b16 v[24:25], v3 offset:10240\n"
"ds_read_b64_tr_b16 v[26:27], v3 offset:10752\n"
"s_waitcnt lgkmcnt(7)\n"
"v_mfma_f32_32x32x16_bf16 v[80:95], v[192:195], v[28:31], v[80:95]\n"
"ds_read_b64_tr_b16 v[28:29], v4 offset:10240\n"
"ds_read_b64_tr_b16 v[30:31], v4 offset:10752\n"
"s_waitcnt lgkmcnt(6)\n"
"v_mfma_f32_32x32x16_bf16 v[32:47], v[196:199], v[16:19], v[32:47]\n"
"ds_read_b128 v[200:203], v98 offset:0\n"
"ds_read_b64_tr_b16 v[16:17], v3 offset:4096\n"
"ds_read_b64_tr_b16 v[18:19], v3 offset:4608\n"
"s_waitcnt lgkmcnt(7)\n"
"v_mfma_f32_32x32x16_bf16 v[48:63], v[196:199], v[20:23], v[48:63]\n"
"ds_read_b64_tr_b16 v[20:21], v4 offset:4096\n"
"ds_read_b64_tr_b16 v[22:23], v4 offset:4608\n"
"s_waitcnt lgkmcnt(7)\n"
"v_mfma_f32_32x32x16_bf16 v[64:79], v[196:199], v[24:27], v[64:79]\n"
"ds_read_b64_tr_b16 v[24:25], v3 offset:12288\n"
"ds_read_b64_tr_b16 v[26:27], v3 offset:12800\n"
"s_waitcnt lgkmcnt(7)\n"
"v_mfma_f32_32x32x16_bf16 v[80:95], v[196:199], v[28:31], v[80:95]\n"
"ds_read_b64_tr_b16 v[28:29], v4 offset:12288\n"
"ds_read_b64_tr_b16 v[30:31], v4 offset:12800\n"
"s_waitcnt lgkmcnt(6)\n"
"v_mfma_f32_32x32x16_bf16 v[32:47], v[200:203], v[16:19], v[32:47]\n"
"ds_read_b128 v[204:207], v99 offset:0\n"
"ds_read_b64_tr_b16 v[16:17], v3 offset:6144\n"
"ds_read_b64_tr_b16 v[18:19], v3 offset:6656\n"
"s_waitcnt lgkmcnt(7)\n"
"v_mfma_f32_32x32x16_bf16 v[48:63], v[200:203], v[20:23], v[48:63]\n"
"ds_read_b64_tr_b16 v[20:21], v4 offset:6144\n"
"ds_read_b64_tr_b16 v[22:23], v4 offset:6656\n"
"s_waitcnt lgkmcnt(7)\n"
"v_mfma_f32_32x32x16_bf16 v[64:79], v[200:203], v[24:27], v[64:79]\n"
"ds_read_b64_tr_b16 v[24:25], v3 offset:14336\n"
"ds_read_b64_tr_b16 v[26:27], v3 offset:14848\n"
"s_waitcnt lgkmcnt(7)\n"
"v_mfma_f32_32x32x16_bf16 v[80:95], v[200:203], v[28:31], v[80:95]\n"
"ds_read_b64_tr_b16 v[28:29], v4 offset:14336\n"
"ds_read_b64_tr_b16 v[30:31], v4 offset:14848\n"
"s_waitcnt lgkmcnt(6)\n"
"v_mfma_f32_32x32x16_bf16 v[32:47], v[204:207], v[16:19], v[32:47]\n"
"s_barrier\n"
"ds_read_b128 v[208:211], v96 offset:4096\n"
"ds_read_b64_tr_b16 v[16:17], v1 offset:0\n"
"ds_read_b64_tr_b16 v[18:19], v1 offset:512\n"
"s_waitcnt lgkmcnt(7)\n"
"v_mfma_f32_32x32x16_bf16 v[48:63], v[204:207], v[20:23], v[48:63]\n"
"ds_read_b64_tr_b16 v[20:21], v2 offset:0\n"
"ds_read_b64_tr_b16 v[22:23], v2 offset:512\n"
"s_waitcnt lgkmcnt(7)\n"
"v_mfma_f32_32x32x16_bf16 v[64:79], v[204:207], v[24:27], v[64:79]\n"
"ds_read_b64_tr_b16 v[24:25], v1 offset:8192\n"
"ds_read_b64_tr_b16 v[26:27], v1 offset:8704\n"
"s_waitcnt lgkmcnt(7)\n"
"v_mfma_f32_32x32x16_bf16 v[80:95], v[204:207], v[28:31], v[80:95]\n"
"ds_read_b64_tr_b16 v[28:29], v2 offset:8192\n"
"ds_read_b64_tr_b16 v[30:31], v2 offset:8704\n"
"s_waitcnt lgkmcnt(6)\n"
"v_mfma_f32_32x32x16_bf16 v[32:47], v[208:211], v[16:19], v[32:47]\n"
"ds_read_b128 v[212:215], v97 offset:4096\n"
"ds_read_b64_tr_b16 v[16:17], v1 offset:2048\n"
"ds_read_b64_tr_b16 v[18:19], v1 offset:2560\n"
"s_waitcnt lgkmcnt(7)\n"
"v_mfma_f32_32x32x16_bf16 v[48:63], v[208:211], v[20:23], v[48:63]\n"
"ds_read_b64_tr_b16 v[20:21], v2 offset:2048\n"
"ds_read_b64_tr_b16 v[22:23], v2 offset:2560\n"
"s_waitcnt lgkmcnt(7)\n"
"v_mfma_f32_32x32x16_bf16 v[64:79], v[208:211], v[24:27], v[64:79]\n"
"ds_read_b64_tr_b16 v[24:25], v1 offset:10240\n"
"ds_read_b64_tr_b16 v[26:27], v1 offset:10752\n"
"s_waitcnt lgkmcnt(7)\n"
"v_mfma_f32_32x32x16_bf16 v[80:95], v[208:211], v[28:31], v[80:95]\n"
"ds_read_b64_tr_b16 v[28:29], v2 offset:10240\n"
"ds_read_b64_tr_b16 v[30:31], v2 offset:10752\n"
"s_waitcnt lgkmcnt(6)\n"
"v_mfma_f32_32x32x16_bf16 v[32:47], v[212:215], v[16:19], v[32:47]\n"
"ds_read_b128 v[216:219], v98 offset:4096\n"
"ds_read_b64_tr_b16 v[16:17], v1 offset:4096\n"
"ds_read_b64_tr_b16 v[18:19], v1 offset:4608\n"
"s_waitcnt lgkmcnt(7)\n"
"v_mfma_f32_32x32x16_bf16 v[48:63], v[212:215], v[20:23], v[48:63]\n"
"ds_read_b64_tr_b16 v[20:21], v2 offset:4096\n"
"ds_read_b64_tr_b16 v[22:23], v2 offset:4608\n"
"s_waitcnt lgkmcnt(7)\n"
"v_mfma_f32_32x32x16_bf16 v[64:79], v[212:215], v[24:27], v[64:79]\n"
"ds_read_b64_tr_b16 v[24:25], v1 offset:12288\n"
"ds_read_b64_tr_b16 v[26:27], v1 offset:12800\n"
"s_waitcnt lgkmcnt(7)\n"
"v_mfma_f32_32x32x16_bf16 v[80:95], v[212:215], v[28:31], v[80:95]\n"
"ds_read_b64_tr_b16 v[28:29], v2 offset:12288\n"
"LPAD4:\n"
"s_cbranch_execz LPAD7\n"
"ds_read_b64_tr_b16 v[30:31], v2 offset:12800\n"
"s_waitcnt lgkmcnt(6)\n"
"v_mfma_f32_32x32x16_bf16 v[32:47], v[216:219], v[16:19], v[32:47]\n"
"ds_read_b128 v[220:223], v99 offset:4096\n"
"ds_read_b64_tr_b16 v[16:17], v1 offset:6144\n"
"ds_read_b64_tr_b16 v[18:19], v1 offset:6656\n"
"s_waitcnt lgkmcnt(7)\n"
"v_mfma_f32_32x32x16_bf16 v[48:63], v[216:219], v[20:23], v[48:63]\n"
"ds_read_b64_tr_b16 v[20:21], v2 offset:6144\n"
"ds_read_b64_tr_b16 v[22:23], v2 offset:6656\n"
"s_waitcnt lgkmcnt(7)\n"
"v_mfma_f32_32x32x16_bf16 v[64:79], v[216:219], v[24:27], v[64:79]\n"
"ds_read_b64_tr_b16 v[24:25], v1 offset:14336\n"
"ds_read_b64_tr_b16 v[26:27], v1 offset:14848\n"
"s_waitcnt lgkmcnt(7)\n"
"v_mfma_f32_32x32x16_bf16 v[80:95], v[216:219], v[28:31], v[80:95]\n"
"ds_read_b64_tr_b16 v[28:29], v2 offset:14336\n"
"ds_read_b64_tr_b16 v[30:31], v2 offset:14848\n"
"s_waitcnt lgkmcnt(6)\n"
"v_mfma_f32_32x32x16_bf16 v[32:47], v[220:223], v[16:19], v[32:47]\n"
"s_barrier\n"
"ds_read_b128 v[224:227], v96 offset:8192\n"
"ds_read_b64_tr_b16 v[16:17], v1 offset:16384\n"
"ds_read_b64_tr_b16 v[18:19], v1 offset:16896\n"
"s_waitcnt lgkmcnt(7)\n"
"v_mfma_f32_32x32x16_bf16 v[48:63], v[220:223], v[20:23], v[48:63]\n"
"ds_read_b64_tr_b16 v[20:21], v2 offset:16384\n"
"ds_read_b64_tr_b16 v[22:23], v2 offset:16896\n"
"s_waitcnt lgkmcnt(7)\n"
"v_mfma_f32_32x32x16_bf16 v[64:79], v[220:223], v[24:27], v[64:79]\n"
"ds_read_b64_tr_b16 v[24:25], v1 offset:24576\n"
"ds_read_b64_tr_b16 v[26:27], v1 offset:25088\n"
"s_waitcnt lgkmcnt(7)\n"
"v_mfma_f32_32x32x16_bf16 v[80:95], v[220:223], v[28:31], v[80:95]\n"
"ds_read_b64_tr_b16 v[28:29], v2 offset:24576\n"
"ds_read_b64_tr_b16 v[30:31], v2 offset:25088\n"
"s_waitcnt lgkmcnt(6)\n"
"v_mfma_f32_32x32x16_bf16 v[32:47], v[224:227], v[16:19], v[32:47]\n"
"ds_read_b128 v[228:231], v97 offset:8192\n"
"ds_read_b64_tr_b16 v[16:17], v1 offset:18432\n"
"ds_read_b64_tr_b16 v[18:19], v1 offset:18944\n"
"s_waitcnt lgkmcnt(7)\n"
"v_mfma_f32_32x32x16_bf16 v[48:63], v[224:227], v[20:23], v[48:63]\n"
"ds_read_b64_tr_b16 v[20:21], v2 offset:18432\n"
"ds_read_b64_tr_b16 v[22:23], v2 offset:18944\n"
"s_waitcnt lgkmcnt(7)\n"
"v_mfma_f32_32x32x16_bf16 v[64:79], v[224:227], v[24:27], v[64:79]\n"
"ds_read_b64_tr_b16 v[24:25], v1 offset:26624\n"
"ds_read_b64_tr_b16 v[26:27], v1 offset:27136\n"
"s_waitcnt lgkmcnt(7)\n"
"v_mfma_f32_32x32x16_bf16 v[80:95], v[224:227], v[28:31], v[80:95]\n"
"ds_read_b64_tr_b16 v[28:29], v2 offset:26624\n"
"ds_read_b64_tr_b16 v[30:31], v2 offset:27136\n"
"s_waitcnt lgkmcnt(6)\n"
"v_mfma_f32_32x32x16_bf16 v[32:47], v[228:231], v[16:19], v[32:47]\n"
"ds_read_b128 v[232:235], v98 offset:8192\n"
"ds_read_b64_tr_b16 v[16:17], v1 offset:20480\n"
"ds_read_b64_tr_b16 v[18:19], v1 offset:20992\n"
"s_waitcnt lgkmcnt(7)\n"
"v_mfma_f32_32x32x16_bf16 v[48:63], v[228:231], v[20:23], v[48:63]\n"
"ds_read_b64_tr_b16 v[20:21], v2 offset:20480\n"
"ds_read_b64_tr_b16 v[22:23], v2 offset:20992\n"
"s_waitcnt lgkmcnt(7)\n"
"v_mfma_f32_32x32x16_bf16 v[64:79], v[228:231], v[24:27], v[64:79]\n"
"ds_read_b64_tr_b16 v[24:25], v1 offset:28672\n"
"ds_read_b64_tr_b16 v[26:27], v1 offset:29184\n"
"s_waitcnt lgkmcnt(7)\n"
"v_mfma_f32_32x32x16_bf16 v[80:95], v[228:231], v[28:31], v[80:95]\n"
"ds_read_b64_tr_b16 v[28:29], v2 offset:28672\n"
"ds_read_b64_tr_b16 v[30:31], v2 offset:29184\n"
"s_waitcnt lgkmcnt(6)\n"
"v_mfma_f32_32x32x16_bf16 v[32:47], v[232:235], v[16:19], v[32:47]\n"
"ds_read_b128 v[236:239], v99 offset:8192\n"
"ds_read_b64_tr_b16 v[16:17], v1 offset:22528\n"
"ds_read_b64_tr_b16 v[18:19], v1 offset:23040\n"
"s_waitcnt lgkmcnt(7)\n"
"v_mfma_f32_32x32x16_bf16 v[48:63], v[232:235], v[20:23], v[48:63]\n"
"ds_read_b64_tr_b16 v[20:21], v2 offset:22528\n"
"ds_read_b64_tr_b16 v[22:23], v2 offset:23040\n"
"s_waitcnt lgkmcnt(7)\n"
"v_mfma_f32_32x32x16_bf16 v[64:79], v[232:235], v[24:27], v[64:79]\n"
"ds_read_b64_tr_b16 v[24:25], v1 offset:30720\n"
"ds_read_b64_tr_b16 v[26:27], v1 offset:31232\n"
"s_waitcnt lgkmcnt(7)\n"
"v_mfma_f32_32x32x16_bf16 v[80:95], v[232:235], v[28:31], v[80:95]\n"
"ds_read_b64_tr_b16 v[28:29], v2 offset:30720\n"
"ds_read_b64_tr_b16 v[30:31], v2 offset:31232\n"
"s_waitcnt lgkmcnt(6)\n"
"v_mfma_f32_32x32x16_bf16 v[32:47], v[236:239], v[16:19], v[32:47]\n"
"s_barrier\n"
"ds_read_b128 v[240:243], v96 offset:12288\n"
"ds_read_b64_tr_b16 v[16:17], v1 offset:32768\n"
"ds_read_b64_tr_b16 v[18:19], v1 offset:33280\n"
"s_waitcnt lgkmcnt(7)\n"
"v_mfma_f32_32x32x16_bf16 v[48:63], v[236:239], v[20:23], v[48:63]\n"
"ds_read_b64_tr_b16 v[20:21], v2 offset:32768\n"
"ds_read_b64_tr_b16 v[22:23], v2 offset:33280\n"
"s_waitcnt lgkmcnt(7)\n"
"v_mfma_f32_32x32x16_bf16 v[64:79], v[236:239], v[24:27], v[64:79]\n"
"ds_read_b128 v[244:247], v97 offset:12288\n"
"ds_read_b64_tr_b16 v[24:25], v1 offset:34816\n"
"ds_read_b64_tr_b16 v[26:27], v1 offset:35328\n"
"s_waitcnt lgkmcnt(8)\n"
"v_mfma_f32_32x32x16_bf16 v[80:95], v[236:239], v[28:31], v[80:95]\n"
"ds_read_b64_tr_b16 v[28:29], v2 offset:34816\n"
"ds_read_b64_tr_b16 v[30:31], v2 offset:35328\n"
"s_waitcnt lgkmcnt(7)\n"
"v_mfma_f32_32x32x16_bf16 v[32:47], v[240:243], v[16:19], v[32:47]\n"
"ds_read_b128 v[248:251], v98 offset:12288\n"
"ds_read_b64_tr_b16 v[16:17], v1 offset:36864\n"
"ds_read_b64_tr_b16 v[18:19], v1 offset:37376\n"
"s_waitcnt lgkmcnt(8)\n"
"v_mfma_f32_32x32x16_bf16 v[48:63], v[240:243], v[20:23], v[48:63]\n"
"ds_read_b64_tr_b16 v[20:21], v2 offset:36864\n"
"ds_read_b64_tr_b16 v[22:23], v2 offset:37376\n"
"s_waitcnt lgkmcnt(7)\n"
"v_mfma_f32_32x32x16_bf16 v[32:47], v[244:247], v[24:27], v[32:47]\n"
"ds_read_b128 v[252:255], v99 offset:12288\n"
"ds_read_b64_tr_b16 v[24:25], v1 offset:38912\n"
"ds_read_b64_tr_b16 v[26:27], v1 offset:39424\n"
"s_waitcnt lgkmcnt(8)\n"
"v_mfma_f32_32x32x16_bf16 v[48:63], v[244:247], v[28:31], v[48:63]\n"
"ds_read_b64_tr_b16 v[28:29], v2 offset:38912\n"
"ds_read_b64_tr_b16 v[30:31], v2 offset:39424\n"
"s_waitcnt lgkmcnt(7)\n"
"v_mfma_f32_32x32x16_bf16 v[32:47], v[248:251], v[16:19], v[32:47]\n"
"ds_read_b64_tr_b16 v[16:17], v1 offset:40960\n"
"ds_read_b64_tr_b16 v[18:19], v1 offset:41472\n"
"s_waitcnt lgkmcnt(7)\n"
"v_mfma_f32_32x32x16_bf16 v[48:63], v[248:251], v[20:23], v[48:63]\n"
"ds_read_b64_tr_b16 v[20:21], v2 offset:40960\n"
"ds_read_b64_tr_b16 v[22:23], v2 offset:41472\n"
"s_waitcnt lgkmcnt(6)\n"
"v_mfma_f32_32x32x16_bf16 v[32:47], v[252:255], v[24:27], v[32:47]\n"
"ds_read_b64_tr_b16 v[24:25], v1 offset:43008\n"
"ds_read_b64_tr_b16 v[26:27], v1 offset:43520\n"
"s_waitcnt lgkmcnt(6)\n"
"v_mfma_f32_32x32x16_bf16 v[48:63], v[252:255], v[28:31], v[48:63]\n"
"ds_read_b64_tr_b16 v[28:29], v2 offset:43008\n"
"ds_read_b64_tr_b16 v[30:31], v2 offset:43520\n"
"s_waitcnt lgkmcnt(6)\n"
"v_mfma_f32_32x32x16_bf16 v[64:79], v[240:243], v[16:19], v[64:79]\n"
"ds_read_b64_tr_b16 v[16:17], v1 offset:45056\n"
"ds_read_b64_tr_b16 v[18:19], v1 offset:45568\n"
"s_waitcnt lgkmcnt(6)\n"
"v_mfma_f32_32x32x16_bf16 v[80:95], v[240:243], v[20:23], v[80:95]\n"
"ds_read_b64_tr_b16 v[20:21], v2 offset:45056\n"
"ds_read_b64_tr_b16 v[22:23], v2 offset:45568\n"
"v_cvt_pk_bf16_f32 v32, v32, v33\n"
"v_cvt_pk_bf16_f32 v33, v34, v35\n"
"v_cvt_pk_bf16_f32 v34, v36, v37\n"
"v_cvt_pk_bf16_f32 v35, v38, v39\n"
"s_waitcnt lgkmcnt(6)\n"
"v_mfma_f32_32x32x16_bf16 v[64:79], v[244:247], v[24:27], v[64:79]\n"
"ds_read_b64_tr_b16 v[24:25], v1 offset:47104\n"
"ds_read_b64_tr_b16 v[26:27], v1 offset:47616\n"
"ds_write_b128 v6, v[32:35] offset:8192\n"
"s_waitcnt lgkmcnt(7)\n"
"v_mfma_f32_32x32x16_bf16 v[80:95], v[244:247], v[28:31], v[80:95]\n"
"ds_read_b64_tr_b16 v[28:29], v2 offset:47104\n"
"ds_read_b64_tr_b16 v[30:31], v2 offset:47616\n"
"v_cvt_pk_bf16_f32 v36, v40, v41\n"
"v_cvt_pk_bf16_f32 v37, v42, v43\n"
"v_cvt_pk_bf16_f32 v38, v44, v45\n"
"v_cvt_pk_bf16_f32 v39, v46, v47\n"
"s_waitcnt lgkmcnt(7)\n"
"v_mfma_f32_32x32x16_bf16 v[64:79], v[248:251], v[16:19], v[64:79]\n"
"s_barrier\n"
"ds_read_b64_tr_b16 v[16:17], v1 offset:49152\n"
"ds_read_b64_tr_b16 v[18:19], v1 offset:49664\n"
"ds_write_b128 v6, v[36:39] offset:9216\n"
"s_waitcnt lgkmcnt(8)\n"
"v_mfma_f32_32x32x16_bf16 v[80:95], v[248:251], v[20:23], v[80:95]\n"
"ds_read_b64_tr_b16 v[20:21], v1 offset:57344\n"
"ds_read_b64_tr_b16 v[22:23], v1 offset:57856\n"
"v_cvt_pk_bf16_f32 v48, v48, v49\n"
"v_cvt_pk_bf16_f32 v49, v50, v51\n"
"v_cvt_pk_bf16_f32 v50, v52, v53\n"
"v_cvt_pk_bf16_f32 v51, v54, v55\n"
"s_waitcnt lgkmcnt(8)\n"
"v_mfma_f32_32x32x16_bf16 v[64:79], v[252:255], v[24:27], v[64:79]\n"
"ds_read_b64_tr_b16 v[24:25], v1 offset:51200\n"
"ds_read_b64_tr_b16 v[26:27], v1 offset:51712\n"
"ds_write_b128 v6, v[48:51] offset:10240\n"
"s_waitcnt lgkmcnt(8)\n"
"v_mfma_f32_32x32x16_bf16 v[80:95], v[252:255], v[28:31], v[80:95]\n"
"ds_read_b64_tr_b16 v[28:29], v1 offset:59392\n"
"ds_read_b64_tr_b16 v[30:31], v1 offset:59904\n"
"v_cvt_pk_bf16_f32 v52, v56, v57\n"
"v_cvt_pk_bf16_f32 v53, v58, v59\n"
"v_cvt_pk_bf16_f32 v54, v60, v61\n"
"v_cvt_pk_bf16_f32 v55, v62, v63\n"
"s_waitcnt lgkmcnt(8)\n"
"v_mfma_f32_32x32x16_bf16 v[96:111], v[16:19], v[128:131], 0\n"
"ds_read_b64_tr_b16 v[16:17], v1 offset:53248\n"
"ds_read_b64_tr_b16 v[18:19], v1 offset:53760\n"
"ds_write_b128 v6, v[52:55] offset:11264\n"
"s_waitcnt lgkmcnt(8)\n"
"v_mfma_f32_32x32x16_bf16 v[112:127], v[20:23], v[128:131], 0\n"
"ds_read_b64_tr_b16 v[20:21], v1 offset:61440\n"
"ds_read_b64_tr_b16 v[22:23], v1 offset:61952\n"
"v_cvt_pk_bf16_f32 v64, v64, v65\n"
"v_cvt_pk_bf16_f32 v65, v66, v67\n"
"v_cvt_pk_bf16_f32 v66, v68, v69\n"
"v_cvt_pk_bf16_f32 v67, v70, v71\n"
"s_waitcnt lgkmcnt(8)\n"
"v_mfma_f32_32x32x16_bf16 v[96:111], v[24:27], v[132:135], v[96:111]\n"
"ds_read_b64_tr_b16 v[24:25], v1 offset:55296\n"
"ds_read_b64_tr_b16 v[26:27], v1 offset:55808\n"
"ds_write_b128 v6, v[64:67] offset:12288\n"
"s_waitcnt lgkmcnt(8)\n"
"v_mfma_f32_32x32x16_bf16 v[112:127], v[28:31], v[132:135], v[112:127]\n"
"ds_read_b64_tr_b16 v[28:29], v1 offset:63488\n"
"ds_read_b64_tr_b16 v[30:31], v1 offset:64000\n"
"v_cvt_pk_bf16_f32 v68, v72, v73\n"
"v_cvt_pk_bf16_f32 v69, v74, v75\n"
"v_cvt_pk_bf16_f32 v70, v76, v77\n"
"v_cvt_pk_bf16_f32 v71, v78, v79\n"
"s_waitcnt lgkmcnt(8)\n"
"v_mfma_f32_32x32x16_bf16 v[96:111], v[16:19], v[136:139], v[96:111]\n"
"ds_read_b64_tr_b16 v[16:17], v2 offset:49152\n"
"ds_read_b64_tr_b16 v[18:19], v2 offset:49664\n"
"ds_write_b128 v6, v[68:71] offset:13312\n"
"s_waitcnt lgkmcnt(8)\n"
"v_mfma_f32_32x32x16_bf16 v[112:127], v[20:23], v[136:139], v[112:127]\n"
"ds_read_b64_tr_b16 v[20:21], v2 offset:57344\n"
"ds_read_b64_tr_b16 v[22:23], v2 offset:57856\n"
"v_cvt_pk_bf16_f32 v80, v80, v81\n"
"v_cvt_pk_bf16_f32 v81, v82, v83\n"
"v_cvt_pk_bf16_f32 v82, v84, v85\n"
"v_cvt_pk_bf16_f32 v83, v86, v87\n"
"s_waitcnt lgkmcnt(8)\n"
"v_mfma_f32_32x32x16_bf16 v[96:111], v[24:27], v[140:143], v[96:111]\n"
"ds_read_b64_tr_b16 v[24:25], v2 offset:51200\n"
"ds_read_b64_tr_b16 v[26:27], v2 offset:51712\n"
"ds_write_b128 v6, v[80:83] offset:14336\n"
"s_waitcnt lgkmcnt(8)\n"
"v_mfma_f32_32x32x16_bf16 v[112:127], v[28:31], v[140:143], v[112:127]\n"
"ds_read_b64_tr_b16 v[28:29], v2 offset:59392\n"
"ds_read_b64_tr_b16 v[30:31], v2 offset:59904\n"
"v_cvt_pk_bf16_f32 v84, v88, v89\n"
"v_cvt_pk_bf16_f32 v85, v90, v91\n"
"v_cvt_pk_bf16_f32 v86, v92, v93\n"
"v_cvt_pk_bf16_f32 v87, v94, v95\n"
"s_waitcnt lgkmcnt(8)\n"
"v_mfma_f32_32x32x16_bf16 v[32:47], v[16:19], v[128:131], 0\n"
"ds_read_b64_tr_b16 v[16:17], v2 offset:53248\n"
"ds_read_b64_tr_b16 v[18:19], v2 offset:53760\n"
"ds_write_b128 v6, v[84:87] offset:15360\n"
"s_waitcnt lgkmcnt(8)\n"
"v_mfma_f32_32x32x16_bf16 v[48:63], v[20:23], v[128:131], 0\n"
"ds_read_b64_tr_b16 v[20:21], v2 offset:61440\n"
"ds_read_b64_tr_b16 v[22:23], v2 offset:61952\n"
"s_waitcnt lgkmcnt(8)\n"
"v_mfma_f32_32x32x16_bf16 v[32:47], v[24:27], v[132:135], v[32:47]\n"
"ds_read_b64_tr_b16 v[24:25], v2 offset:55296\n"
"ds_read_b64_tr_b16 v[26:27], v2 offset:55808\n"
"s_waitcnt lgkmcnt(7)\n"
"v_mfma_f32_32x32x16_bf16 v[48:63], v[28:31], v[132:135], v[48:63]\n"
"ds_read_b64_tr_b16 v[28:29], v2 offset:63488\n"
"ds_read_b64_tr_b16 v[30:31], v2 offset:64000\n"
"s_waitcnt lgkmcnt(7)\n"
"v_mfma_f32_32x32x16_bf16 v[32:47], v[16:19], v[136:139], v[32:47]\n"
"s_waitcnt lgkmcnt(6)\n"
"s_barrier\n"
"ds_read_b64_tr_b16 v[16:17], v3 offset:0\n"
"ds_read_b64_tr_b16 v[18:19], v3 offset:512\n"
"s_waitcnt lgkmcnt(6)\n"
"v_mfma_f32_32x32x16_bf16 v[48:63], v[20:23], v[136:139], v[48:63]\n"
"ds_read_b64_tr_b16 v[20:21], v4 offset:0\n"
"ds_read_b64_tr_b16 v[22:23], v4 offset:512\n"
"s_waitcnt lgkmcnt(6)\n"
"v_mfma_f32_32x32x16_bf16 v[32:47], v[24:27], v[140:143], v[32:47]\n"
"ds_read_b64_tr_b16 v[24:25], v3 offset:8192\n"
"ds_read_b64_tr_b16 v[26:27], v3 offset:8704\n"
"s_waitcnt lgkmcnt(6)\n"
"v_mfma_f32_32x32x16_bf16 v[48:63], v[28:31], v[140:143], v[48:63]\n"
"ds_read_b64_tr_b16 v[28:29], v4 offset:8192\n"
"ds_read_b64_tr_b16 v[30:31], v4 offset:8704\n"
"s_waitcnt lgkmcnt(6)\n"
"v_mfma_f32_32x32x16_bf16 v[96:111], v[16:19], v[144:147], v[96:111]\n"
"ds_read_b64_tr_b16 v[16:17], v3 offset:2048\n"
"ds_read_b64_tr_b16 v[18:19], v3 offset:2560\n"
"s_waitcnt lgkmcnt(6)\n"
"v_mfma_f32_32x32x16_bf16 v[32:47], v[20:23], v[144:147], v[32:47]\n"
"ds_read_b64_tr_b16 v[20:21], v4 offset:2048\n"
"ds_read_b64_tr_b16 v[22:23], v4 offset:2560\n"
"s_waitcnt lgkmcnt(6)\n"
"v_mfma_f32_32x32x16_bf16 v[112:127], v[24:27], v[144:147], v[112:127]\n"
"ds_read_b64_tr_b16 v[24:25], v3 offset:10240\n"
"ds_read_b64_tr_b16 v[26:27], v3 offset:10752\n"
"s_waitcnt lgkmcnt(6)\n"
"v_mfma_f32_32x32x16_bf16 v[48:63], v[28:31], v[144:147], v[48:63]\n"
"ds_read_b64_tr_b16 v[28:29], v4 offset:10240\n"
"ds_read_b64_tr_b16 v[30:31], v4 offset:10752\n"
"s_waitcnt lgkmcnt(6)\n"
"v_mfma_f32_32x32x16_bf16 v[96:111], v[16:19], v[148:151], v[96:111]\n"
"ds_read_b64_tr_b16 v[16:17], v3 offset:4096\n"
"ds_read_b64_tr_b16 v[18:19], v3 offset:4608\n"
"s_waitcnt lgkmcnt(6)\n"
"v_mfma_f32_32x32x16_bf16 v[32:47], v[20:23], v[148:151], v[32:47]\n"
"ds_read_b64_tr_b16 v[20:21], v4 offset:4096\n"
"ds_read_b64_tr_b16 v[22:23], v4 offset:4608\n"
"s_waitcnt lgkmcnt(6)\n"
"v_mfma_f32_32x32x16_bf16 v[112:127], v[24:27], v[148:151], v[112:127]\n"
"ds_read_b64_tr_b16 v[24:25], v3 offset:12288\n"
"ds_read_b64_tr_b16 v[26:27], v3 offset:12800\n"
"s_waitcnt lgkmcnt(6)\n"
"v_mfma_f32_32x32x16_bf16 v[48:63], v[28:31], v[148:151], v[48:63]\n"
"ds_read_b64_tr_b16 v[28:29], v4 offset:12288\n"
"ds_read_b64_tr_b16 v[30:31], v4 offset:12800\n"
"s_waitcnt lgkmcnt(6)\n"
"v_mfma_f32_32x32x16_bf16 v[96:111], v[16:19], v[152:155], v[96:111]\n"
"ds_read_b64_tr_b16 v[16:17], v3 offset:6144\n"
"ds_read_b64_tr_b16 v[18:19], v3 offset:6656\n"
"s_waitcnt lgkmcnt(6)\n"
"v_mfma_f32_32x32x16_bf16 v[32:47], v[20:23], v[152:155], v[32:47]\n"
"ds_read_b64_tr_b16 v[20:21], v4 offset:6144\n"
"ds_read_b64_tr_b16 v[22:23], v4 offset:6656\n"
"s_waitcnt lgkmcnt(6)\n"
"v_mfma_f32_32x32x16_bf16 v[112:127], v[24:27], v[152:155], v[112:127]\n"
"ds_read_b64_tr_b16 v[24:25], v3 offset:14336\n"
"ds_read_b64_tr_b16 v[26:27], v3 offset:14848\n"
"s_waitcnt lgkmcnt(6)\n"
"v_mfma_f32_32x32x16_bf16 v[48:63], v[28:31], v[152:155], v[48:63]\n"
"ds_read_b64_tr_b16 v[28:29], v4 offset:14336\n"
"ds_read_b64_tr_b16 v[30:31], v4 offset:14848\n"
"s_waitcnt lgkmcnt(6)\n"
"v_mfma_f32_32x32x16_bf16 v[96:111], v[16:19], v[156:159], v[96:111]\n"
"s_barrier\n"
"ds_read_b64_tr_b16 v[16:17], v1 offset:0\n"
"ds_read_b64_tr_b16 v[18:19], v1 offset:512\n"
"s_waitcnt lgkmcnt(6)\n"
"v_mfma_f32_32x32x16_bf16 v[32:47], v[20:23], v[156:159], v[32:47]\n"
"ds_read_b64_tr_b16 v[20:21], v2 offset:0\n"
"ds_read_b64_tr_b16 v[22:23], v2 offset:512\n"
"s_waitcnt lgkmcnt(6)\n"
"v_mfma_f32_32x32x16_bf16 v[112:127], v[24:27], v[156:159], v[112:127]\n"
"ds_read_b64_tr_b16 v[24:25], v1 offset:8192\n"
"ds_read_b64_tr_b16 v[26:27], v1 offset:8704\n"
"s_waitcnt lgkmcnt(6)\n"
"v_mfma_f32_32x32x16_bf16 v[48:63], v[28:31], v[156:159], v[48:63]\n"
"ds_read_b64_tr_b16 v[28:29], v2 offset:8192\n"
"ds_read_b64_tr_b16 v[30:31], v2 offset:8704\n"
"s_waitcnt lgkmcnt(6)\n"
"v_mfma_f32_32x32x16_bf16 v[96:111], v[16:19], v[160:163], v[96:111]\n"
"ds_read_b64_tr_b16 v[16:17], v1 offset:2048\n"
"ds_read_b64_tr_b16 v[18:19], v1 offset:2560\n"
"s_waitcnt lgkmcnt(6)\n"
"v_mfma_f32_32x32x16_bf16 v[32:47], v[20:23], v[160:163], v[32:47]\n"
"ds_read_b64_tr_b16 v[20:21], v2 offset:2048\n"
"ds_read_b64_tr_b16 v[22:23], v2 offset:2560\n"
"s_waitcnt lgkmcnt(6)\n"
"v_mfma_f32_32x32x16_bf16 v[112:127], v[24:27], v[160:163], v[112:127]\n"
"ds_read_b64_tr_b16 v[24:25], v1 offset:10240\n"
"ds_read_b64_tr_b16 v[26:27], v1 offset:10752\n"
"s_waitcnt lgkmcnt(6)\n"
"v_mfma_f32_32x32x16_bf16 v[48:63], v[28:31], v[160:163], v[48:63]\n"
"ds_read_b64_tr_b16 v[28:29], v2 offset:10240\n"
"ds_read_b64_tr_b16 v[30:31], v2 offset:10752\n"
"s_waitcnt lgkmcnt(6)\n"
"v_mfma_f32_32x32x16_bf16 v[96:111], v[16:19], v[164:167], v[96:111]\n"
"ds_read_b64_tr_b16 v[16:17], v1 offset:4096\n"
"ds_read_b64_tr_b16 v[18:19], v1 offset:4608\n"
"s_waitcnt lgkmcnt(6)\n"
"v_mfma_f32_32x32x16_bf16 v[32:47], v[20:23], v[164:167], v[32:47]\n"
"ds_read_b64_tr_b16 v[20:21], v2 offset:4096\n"
"ds_read_b64_tr_b16 v[22:23], v2 offset:4608\n"
"s_waitcnt lgkmcnt(6)\n"
"v_mfma_f32_32x32x16_bf16 v[112:127], v[24:27], v[164:167], v[112:127]\n"
"ds_read_b64_tr_b16 v[24:25], v1 offset:12288\n"
"ds_read_b64_tr_b16 v[26:27], v1 offset:12800\n"
"s_waitcnt lgkmcnt(6)\n"
"v_mfma_f32_32x32x16_bf16 v[48:63], v[28:31], v[164:167], v[48:63]\n"
"ds_read_b64_tr_b16 v[28:29], v2 offset:12288\n"
"ds_read_b64_tr_b16 v[30:31], v2 offset:12800\n"
"s_waitcnt lgkmcnt(6)\n"
"v_mfma_f32_32x32x16_bf16 v[96:111], v[16:19], v[168:171], v[96:111]\n"
"ds_read_b64_tr_b16 v[16:17], v1 offset:6144\n"
"ds_read_b64_tr_b16 v[18:19], v1 offset:6656\n"
"s_waitcnt lgkmcnt(6)\n"
"v_mfma_f32_32x32x16_bf16 v[32:47], v[20:23], v[168:171], v[32:47]\n"
"ds_read_b64_tr_b16 v[20:21], v2 offset:6144\n"
"ds_read_b64_tr_b16 v[22:23], v2 offset:6656\n"
"s_waitcnt lgkmcnt(6)\n"
"v_mfma_f32_32x32x16_bf16 v[112:127], v[24:27], v[168:171], v[112:127]\n"
"ds_read_b64_tr_b16 v[24:25], v1 offset:14336\n"
"ds_read_b64_tr_b16 v[26:27], v1 offset:14848\n"
"s_waitcnt lgkmcnt(6)\n"
"v_mfma_f32_32x32x16_bf16 v[48:63], v[28:31], v[168:171], v[48:63]\n"
"ds_read_b64_tr_b16 v[28:29], v2 offset:14336\n"
"ds_read_b64_tr_b16 v[30:31], v2 offset:14848\n"
"s_waitcnt lgkmcnt(6)\n"
"v_mfma_f32_32x32x16_bf16 v[96:111], v[16:19], v[172:175], v[96:111]\n"
"s_barrier\n"
"ds_read_b64_tr_b16 v[16:17], v1 offset:16384\n"
"ds_read_b64_tr_b16 v[18:19], v1 offset:16896\n"
"s_waitcnt lgkmcnt(6)\n"
"v_mfma_f32_32x32x16_bf16 v[32:47], v[20:23], v[172:175], v[32:47]\n"
"ds_read_b64_tr_b16 v[20:21], v2 offset:16384\n"
"ds_read_b64_tr_b16 v[22:23], v2 offset:16896\n"
"s_waitcnt lgkmcnt(6)\n"
"v_mfma_f32_32x32x16_bf16 v[112:127], v[24:27], v[172:175], v[112:127]\n"
"ds_read_b64_tr_b16 v[24:25], v1 offset:24576\n"
"ds_read_b64_tr_b16 v[26:27], v1 offset:25088\n"
"s_waitcnt lgkmcnt(6)\n"
"v_mfma_f32_32x32x16_bf16 v[48:63], v[28:31], v[172:175], v[48:63]\n"
"ds_read_b64_tr_b16 v[28:29], v2 offset:24576\n"
"ds_read_b64_tr_b16 v[30:31], v2 offset:25088\n"
"s_waitcnt lgkmcnt(6)\n"
"v_mfma_f32_32x32x16_bf16 v[96:111], v[16:19], v[176:179], v[96:111]\n"
"ds_read_b64_tr_b16 v[16:17], v1 offset:18432\n"
"ds_read_b64_tr_b16 v[18:19], v1 offset:18944\n"
"s_waitcnt lgkmcnt(6)\n"
"v_mfma_f32_32x32x16_bf16 v[32:47], v[20:23], v[176:179], v[32:47]\n"
"ds_read_b64_tr_b16 v[20:21], v2 offset:18432\n"
"ds_read_b64_tr_b16 v[22:23], v2 offset:18944\n"
"s_waitcnt lgkmcnt(6)\n"
"v_mfma_f32_32x32x16_bf16 v[112:127], v[24:27], v[176:179], v[112:127]\n"
"ds_read_b64_tr_b16 v[24:25], v1 offset:26624\n"
"ds_read_b64_tr_b16 v[26:27], v1 offset:27136\n"
"s_waitcnt lgkmcnt(6)\n"
"v_mfma_f32_32x32x16_bf16 v[48:63], v[28:31], v[176:179], v[48:63]\n"
"ds_read_b64_tr_b16 v[28:29], v2 offset:26624\n"
"ds_read_b64_tr_b16 v[30:31], v2 offset:27136\n"
"s_waitcnt lgkmcnt(6)\n"
"v_mfma_f32_32x32x16_bf16 v[96:111], v[16:19], v[180:183], v[96:111]\n"
"ds_read_b64_tr_b16 v[16:17], v1 offset:20480\n"
"ds_read_b64_tr_b16 v[18:19], v1 offset:20992\n"
"s_waitcnt lgkmcnt(6)\n"
"v_mfma_f32_32x32x16_bf16 v[32:47], v[20:23], v[180:183], v[32:47]\n"
"ds_read_b64_tr_b16 v[20:21], v2 offset:20480\n"
"ds_read_b64_tr_b16 v[22:23], v2 offset:20992\n"
"s_waitcnt lgkmcnt(6)\n"
"v_mfma_f32_32x32x16_bf16 v[112:127], v[24:27], v[180:183], v[112:127]\n"
"ds_read_b64_tr_b16 v[24:25], v1 offset:28672\n"
"ds_read_b64_tr_b16 v[26:27], v1 offset:29184\n"
"s_waitcnt lgkmcnt(6)\n"
"v_mfma_f32_32x32x16_bf16 v[48:63], v[28:31], v[180:183], v[48:63]\n"
"ds_read_b64_tr_b16 v[28:29], v2 offset:28672\n"
"ds_read_b64_tr_b16 v[30:31], v2 offset:29184\n"
"s_waitcnt lgkmcnt(6)\n"
"v_mfma_f32_32x32x16_bf16 v[96:111], v[16:19], v[184:187], v[96:111]\n"
"ds_read_b64_tr_b16 v[16:17], v1 offset:22528\n"
"ds_read_b64_tr_b16 v[18:19], v1 offset:23040\n"
"s_waitcnt lgkmcnt(6)\n"
"v_mfma_f32_32x32x16_bf16 v[32:47], v[20:23], v[184:187], v[32:47]\n"
"ds_read_b64_tr_b16 v[20:21], v2 offset:22528\n"
"ds_read_b64_tr_b16 v[22:23], v2 offset:23040\n"
"s_waitcnt lgkmcnt(6)\n"
"v_mfma_f32_32x32x16_bf16 v[112:127], v[24:27], v[184:187], v[112:127]\n"
"ds_read_b64_tr_b16 v[24:25], v1 offset:30720\n"
"ds_read_b64_tr_b16 v[26:27], v1 offset:31232\n"
"s_waitcnt lgkmcnt(6)\n"
"v_mfma_f32_32x32x16_bf16 v[48:63], v[28:31], v[184:187], v[48:63]\n"
"ds_read_b64_tr_b16 v[28:29], v2 offset:30720\n"
"ds_read_b64_tr_b16 v[30:31], v2 offset:31232\n"
"s_waitcnt lgkmcnt(6)\n"
"v_mfma_f32_32x32x16_bf16 v[96:111], v[16:19], v[188:191], v[96:111]\n"
"s_barrier\n"
"ds_read_b64_tr_b16 v[16:17], v1 offset:32768\n"
"ds_read_b64_tr_b16 v[18:19], v1 offset:33280\n"
"s_waitcnt lgkmcnt(6)\n"
"v_mfma_f32_32x32x16_bf16 v[32:47], v[20:23], v[188:191], v[32:47]\n"
"ds_read_b64_tr_b16 v[20:21], v2 offset:32768\n"
"ds_read_b64_tr_b16 v[22:23], v2 offset:33280\n"
"s_waitcnt lgkmcnt(6)\n"
"v_mfma_f32_32x32x16_bf16 v[112:127], v[24:27], v[188:191], v[112:127]\n"
"ds_read_b64_tr_b16 v[24:25], v1 offset:40960\n"
"ds_read_b64_tr_b16 v[26:27], v1 offset:41472\n"
"s_waitcnt lgkmcnt(6)\n"
"v_mfma_f32_32x32x16_bf16 v[48:63], v[28:31], v[188:191], v[48:63]\n"
"ds_read_b64_tr_b16 v[28:29], v2 offset:40960\n"
"ds_read_b64_tr_b16 v[30:31], v2 offset:41472\n"
"s_waitcnt lgkmcnt(6)\n"
"v_mfma_f32_32x32x16_bf16 v[96:111], v[16:19], v[192:195], v[96:111]\n"
"ds_read_b64_tr_b16 v[16:17], v1 offset:34816\n"
"ds_read_b64_tr_b16 v[18:19], v1 offset:35328\n"
"s_waitcnt lgkmcnt(6)\n"
"v_mfma_f32_32x32x16_bf16 v[32:47], v[20:23], v[192:195], v[32:47]\n"
"ds_read_b64_tr_b16 v[20:21], v2 offset:34816\n"
"ds_read_b64_tr_b16 v[22:23], v2 offset:35328\n"
"s_waitcnt lgkmcnt(6)\n"
"v_mfma_f32_32x32x16_bf16 v[112:127], v[24:27], v[192:195], v[112:127]\n"
"ds_read_b64_tr_b16 v[24:25], v1 offset:43008\n"
"ds_read_b64_tr_b16 v[26:27], v1 offset:43520\n"
"s_waitcnt lgkmcnt(6)\n"
"v_mfma_f32_32x32x16_bf16 v[48:63], v[28:31], v[192:195], v[48:63]\n"
"ds_read_b64_tr_b16 v[28:29], v2 offset:43008\n"
"ds_read_b64_tr_b16 v[30:31], v2 offset:43520\n"
"s_waitcnt lgkmcnt(6)\n"
"v_mfma_f32_32x32x16_bf16 v[96:111], v[16:19], v[196:199], v[96:111]\n"
"ds_read_b64_tr_b16 v[16:17], v1 offset:36864\n"
"ds_read_b64_tr_b16 v[18:19], v1 offset:37376\n"
"s_waitcnt lgkmcnt(6)\n"
"v_mfma_f32_32x32x16_bf16 v[32:47], v[20:23], v[196:199], v[32:47]\n"
"ds_read_b64_tr_b16 v[20:21], v2 offset:36864\n"
"ds_read_b64_tr_b16 v[22:23], v2 offset:37376\n"
"s_waitcnt lgkmcnt(6)\n"
"v_mfma_f32_32x32x16_bf16 v[112:127], v[24:27], v[196:199], v[112:127]\n"
"ds_read_b64_tr_b16 v[24:25], v1 offset:45056\n"
"ds_read_b64_tr_b16 v[26:27], v1 offset:45568\n"
"s_waitcnt lgkmcnt(6)\n"
"v_mfma_f32_32x32x16_bf16 v[48:63], v[28:31], v[196:199], v[48:63]\n"
"ds_read_b64_tr_b16 v[28:29], v2 offset:45056\n"
"ds_read_b64_tr_b16 v[30:31], v2 offset:45568\n"
"s_waitcnt lgkmcnt(6)\n"
"v_mfma_f32_32x32x16_bf16 v[96:111], v[16:19], v[200:203], v[96:111]\n"
"ds_read_b64_tr_b16 v[16:17], v1 offset:38912\n"
"ds_read_b64_tr_b16 v[18:19], v1 offset:39424\n"
"s_waitcnt lgkmcnt(6)\n"
"v_mfma_f32_32x32x16_bf16 v[32:47], v[20:23], v[200:203], v[32:47]\n"
"ds_read_b64_tr_b16 v[20:21], v2 offset:38912\n"
"ds_read_b64_tr_b16 v[22:23], v2 offset:39424\n"
"s_waitcnt lgkmcnt(6)\n"
"v_mfma_f32_32x32x16_bf16 v[112:127], v[24:27], v[200:203], v[112:127]\n"
"ds_read_b64_tr_b16 v[24:25], v1 offset:47104\n"
"ds_read_b64_tr_b16 v[26:27], v1 offset:47616\n"
"s_waitcnt lgkmcnt(6)\n"
"v_mfma_f32_32x32x16_bf16 v[48:63], v[28:31], v[200:203], v[48:63]\n"
"ds_read_b64_tr_b16 v[28:29], v2 offset:47104\n"
"ds_read_b64_tr_b16 v[30:31], v2 offset:47616\n"
"s_waitcnt lgkmcnt(6)\n"
"v_mfma_f32_32x32x16_bf16 v[96:111], v[16:19], v[204:207], v[96:111]\n"
"s_waitcnt vmcnt(0)\n"
"s_barrier\n"
"ds_read_b64_tr_b16 v[16:17], v1 offset:49152\n"
"ds_read_b64_tr_b16 v[18:19], v1 offset:49664\n"
"s_waitcnt lgkmcnt(6)\n"
"v_mfma_f32_32x32x16_bf16 v[32:47], v[20:23], v[204:207], v[32:47]\n"
"ds_read_b64_tr_b16 v[20:21], v2 offset:49152\n"
"ds_read_b64_tr_b16 v[22:23], v2 offset:49664\n"
"s_waitcnt lgkmcnt(6)\n"
"v_mfma_f32_32x32x16_bf16 v[112:127], v[24:27], v[204:207], v[112:127]\n"
"ds_read_b64_tr_b16 v[24:25], v1 offset:57344\n"
"ds_read_b64_tr_b16 v[26:27], v1 offset:57856\n"
"s_waitcnt lgkmcnt(6)\n"
"v_mfma_f32_32x32x16_bf16 v[48:63], v[28:31], v[204:207], v[48:63]\n"
"ds_read_b64_tr_b16 v[28:29], v2 offset:57344\n"
"ds_read_b64_tr_b16 v[30:31], v2 offset:57856\n"
"s_waitcnt lgkmcnt(6)\n"
"v_mfma_f32_32x32x16_bf16 v[96:111], v[16:19], v[208:211], v[96:111]\n"
"ds_read_b64_tr_b16 v[16:17], v1 offset:51200\n"
"ds_read_b64_tr_b16 v[18:19], v1 offset:51712\n"
"s_waitcnt lgkmcnt(6)\n"
"v_mfma_f32_32x32x16_bf16 v[32:47], v[20:23], v[208:211], v[32:47]\n"
"ds_read_b64_tr_b16 v[20:21], v2 offset:51200\n"
"ds_read_b64_tr_b16 v[22:23], v2 offset:51712\n"
"s_waitcnt lgkmcnt(6)\n"
"v_mfma_f32_32x32x16_bf16 v[112:127], v[24:27], v[208:211], v[112:127]\n"
"ds_read_b64_tr_b16 v[24:25], v1 offset:59392\n"
"ds_read_b64_tr_b16 v[26:27], v1 offset:59904\n"
"s_waitcnt lgkmcnt(6)\n"
"v_mfma_f32_32x32x16_bf16 v[48:63], v[28:31], v[208:211], v[48:63]\n"
"ds_read_b64_tr_b16 v[28:29], v2 offset:59392\n"
"ds_read_b64_tr_b16 v[30:31], v2 offset:59904\n"
"s_waitcnt lgkmcnt(6)\n"
"v_mfma_f32_32x32x16_bf16 v[96:111], v[16:19], v[212:215], v[96:111]\n"
"ds_read_b64_tr_b16 v[16:17], v1 offset:53248\n"
"ds_read_b64_tr_b16 v[18:19], v1 offset:53760\n"
"s_waitcnt lgkmcnt(6)\n"
"v_mfma_f32_32x32x16_bf16 v[32:47], v[20:23], v[212:215], v[32:47]\n"
"ds_read_b64_tr_b16 v[20:21], v2 offset:53248\n"
"ds_read_b64_tr_b16 v[22:23], v2 offset:53760\n"
"s_waitcnt lgkmcnt(6)\n"
"v_mfma_f32_32x32x16_bf16 v[112:127], v[24:27], v[212:215], v[112:127]\n"
"ds_read_b64_tr_b16 v[24:25], v1 offset:61440\n"
"ds_read_b64_tr_b16 v[26:27], v1 offset:61952\n"
"s_waitcnt lgkmcnt(6)\n"
"v_mfma_f32_32x32x16_bf16 v[48:63], v[28:31], v[212:215], v[48:63]\n"
"ds_read_b64_tr_b16 v[28:29], v2 offset:61440\n"
"ds_read_b64_tr_b16 v[30:31], v2 offset:61952\n"
"s_waitcnt lgkmcnt(6)\n"
"v_mfma_f32_32x32x16_bf16 v[96:111], v[16:19], v[216:219], v[96:111]\n"
"ds_read_b64_tr_b16 v[16:17], v1 offset:55296\n"
"ds_read_b64_tr_b16 v[18:19], v1 offset:55808\n"
"s_waitcnt lgkmcnt(6)\n"
"v_mfma_f32_32x32x16_bf16 v[32:47], v[20:23], v[216:219], v[32:47]\n"
"ds_read_b64_tr_b16 v[20:21], v2 offset:55296\n"
"ds_read_b64_tr_b16 v[22:23], v2 offset:55808\n"
"s_waitcnt lgkmcnt(6)\n"
"v_mfma_f32_32x32x16_bf16 v[112:127], v[24:27], v[216:219], v[112:127]\n"
"LPAD5:\n"
"s_cbranch_execz LBKA\n"
"ds_read_b64_tr_b16 v[24:25], v1 offset:63488\n"
"ds_read_b64_tr_b16 v[26:27], v1 offset:64000\n"
"s_waitcnt lgkmcnt(6)\n"
"v_mfma_f32_32x32x16_bf16 v[48:63], v[28:31], v[216:219], v[48:63]\n"
"ds_read_b64_tr_b16 v[28:29], v2 offset:63488\n"
"ds_read_b64_tr_b16 v[30:31], v2 offset:64000\n"
"s_waitcnt lgkmcnt(6)\n"
"v_mfma_f32_32x32x16_bf16 v[96:111], v[16:19], v[220:223], v[96:111]\n"
"s_barrier\n"
"ds_read_b64_tr_b16 v[16:17], v3 offset:0\n"
"ds_read_b64_tr_b16 v[18:19], v3 offset:512\n"
"s_waitcnt lgkmcnt(6)\n"
"v_mfma_f32_32x32x16_bf16 v[32:47], v[20:23], v[220:223], v[32:47]\n"
"ds_read_b64_tr_b16 v[20:21], v4 offset:0\n"
"ds_read_b64_tr_b16 v[22:23], v4 offset:512\n"
"s_waitcnt lgkmcnt(6)\n"
"v_mfma_f32_32x32x16_bf16 v[112:127], v[24:27], v[220:223], v[112:127]\n"
"ds_read_b64_tr_b16 v[24:25], v3 offset:8192\n"
"ds_read_b64_tr_b16 v[26:27], v3 offset:8704\n"
"s_waitcnt lgkmcnt(6)\n"
"v_mfma_f32_32x32x16_bf16 v[48:63], v[28:31], v[220:223], v[48:63]\n"
"ds_read_b64_tr_b16 v[28:29], v4 offset:8192\n"
"ds_read_b64_tr_b16 v[30:31], v4 offset:8704\n"
"s_waitcnt lgkmcnt(6)\n"
"v_mfma_f32_32x32x16_bf16 v[96:111], v[16:19], v[224:227], v[96:111]\n"
"ds_read_b64_tr_b16 v[16:17], v3 offset:2048\n"
"ds_read_b64_tr_b16 v[18:19], v3 offset:2560\n"
"s_waitcnt lgkmcnt(6)\n"
"v_mfma_f32_32x32x16_bf16 v[32:47], v[20:23], v[224:227], v[32:47]\n"
"ds_read_b64_tr_b16 v[20:21], v4 offset:2048\n"
"ds_read_b64_tr_b16 v[22:23], v4 offset:2560\n"
"s_waitcnt lgkmcnt(6)\n"
"v_mfma_f32_32x32x16_bf16 v[112:127], v[24:27], v[224:227], v[112:127]\n"
"ds_read_b64_tr_b16 v[24:25], v3 offset:10240\n"
"ds_read_b64_tr_b16 v[26:27], v3 offset:10752\n"
"s_waitcnt lgkmcnt(6)\n"
"v_mfma_f32_32x32x16_bf16 v[48:63], v[28:31], v[224:227], v[48:63]\n"
"ds_read_b64_tr_b16 v[28:29], v4 offset:10240\n"
"ds_read_b64_tr_b16 v[30:31], v4 offset:10752\n"
"s_waitcnt lgkmcnt(6)\n"
"v_mfma_f32_32x32x16_bf16 v[96:111], v[16:19], v[228:231], v[96:111]\n"
"ds_read_b64_tr_b16 v[16:17], v3 offset:4096\n"
"ds_read_b64_tr_b16 v[18:19], v3 offset:4608\n"
"s_waitcnt lgkmcnt(6)\n"
"v_mfma_f32_32x32x16_bf16 v[32:47], v[20:23], v[228:231], v[32:47]\n"
"ds_read_b64_tr_b16 v[20:21], v4 offset:4096\n"
"ds_read_b64_tr_b16 v[22:23], v4 offset:4608\n"
"s_waitcnt lgkmcnt(6)\n"
"v_mfma_f32_32x32x16_bf16 v[112:127], v[24:27], v[228:231], v[112:127]\n"
"ds_read_b64_tr_b16 v[24:25], v3 offset:12288\n"
"ds_read_b64_tr_b16 v[26:27], v3 offset:12800\n"
"s_waitcnt lgkmcnt(6)\n"
"v_mfma_f32_32x32x16_bf16 v[48:63], v[28:31], v[228:231], v[48:63]\n"
"ds_read_b64_tr_b16 v[28:29], v4 offset:12288\n"
"ds_read_b64_tr_b16 v[30:31], v4 offset:12800\n"
"s_waitcnt lgkmcnt(6)\n"
"v_mfma_f32_32x32x16_bf16 v[96:111], v[16:19], v[232:235], v[96:111]\n"
"ds_read_b64_tr_b16 v[16:17], v3 offset:6144\n"
"ds_read_b64_tr_b16 v[18:19], v3 offset:6656\n"
"s_waitcnt lgkmcnt(6)\n"
"v_mfma_f32_32x32x16_bf16 v[32:47], v[20:23], v[232:235], v[32:47]\n"
"ds_read_b64_tr_b16 v[20:21], v4 offset:6144\n"
"ds_read_b64_tr_b16 v[22:23], v4 offset:6656\n"
"s_waitcnt lgkmcnt(6)\n"
"v_mfma_f32_32x32x16_bf16 v[112:127], v[24:27], v[232:235], v[112:127]\n"
"ds_read_b64_tr_b16 v[24:25], v3 offset:14336\n"
"ds_read_b64_tr_b16 v[26:27], v3 offset:14848\n"
"s_waitcnt lgkmcnt(6)\n"
"v_mfma_f32_32x32x16_bf16 v[48:63], v[28:31], v[232:235], v[48:63]\n"
"ds_read_b64_tr_b16 v[28:29], v4 offset:14336\n"
"ds_read_b64_tr_b16 v[30:31], v4 offset:14848\n"
"s_waitcnt lgkmcnt(6)\n"
"v_mfma_f32_32x32x16_bf16 v[96:111], v[16:19], v[236:239], v[96:111]\n"
"s_barrier\n"
"ds_read_b64_tr_b16 v[16:17], v1 offset:0\n"
"ds_read_b64_tr_b16 v[18:19], v1 offset:512\n"
"s_waitcnt lgkmcnt(6)\n"
"v_mfma_f32_32x32x16_bf16 v[32:47], v[20:23], v[236:239], v[32:47]\n"
"ds_read_b64_tr_b16 v[20:21], v1 offset:8192\n"
"ds_read_b64_tr_b16 v[22:23], v1 offset:8704\n"
"s_waitcnt lgkmcnt(6)\n"
"v_mfma_f32_32x32x16_bf16 v[112:127], v[24:27], v[236:239], v[112:127]\n"
"ds_read_b64_tr_b16 v[24:25], v1 offset:2048\n"
"ds_read_b64_tr_b16 v[26:27], v1 offset:2560\n"
"s_waitcnt lgkmcnt(6)\n"
"v_mfma_f32_32x32x16_bf16 v[48:63], v[28:31], v[236:239], v[48:63]\n"
"ds_read_b64_tr_b16 v[28:29], v1 offset:10240\n"
"ds_read_b64_tr_b16 v[30:31], v1 offset:10752\n"
"s_waitcnt lgkmcnt(6)\n"
"v_mfma_f32_32x32x16_bf16 v[96:111], v[16:19], v[240:243], v[96:111]\n"
"ds_read_b64_tr_b16 v[16:17], v1 offset:4096\n"
"ds_read_b64_tr_b16 v[18:19], v1 offset:4608\n"
"s_waitcnt lgkmcnt(6)\n"
"v_mfma_f32_32x32x16_bf16 v[112:127], v[20:23], v[240:243], v[112:127]\n"
"ds_read_b64_tr_b16 v[20:21], v1 offset:12288\n"
"ds_read_b64_tr_b16 v[22:23], v1 offset:12800\n"
"s_waitcnt lgkmcnt(6)\n"
"v_mfma_f32_32x32x16_bf16 v[96:111], v[24:27], v[244:247], v[96:111]\n"
"ds_read_b64_tr_b16 v[24:25], v1 offset:6144\n"
"ds_read_b64_tr_b16 v[26:27], v1 offset:6656\n"
"s_waitcnt lgkmcnt(6)\n"
"v_mfma_f32_32x32x16_bf16 v[112:127], v[28:31], v[244:247], v[112:127]\n"
"ds_read_b64_tr_b16 v[28:29], v1 offset:14336\n"
"ds_read_b64_tr_b16 v[30:31], v1 offset:14848\n"
"s_waitcnt lgkmcnt(6)\n"
"v_mfma_f32_32x32x16_bf16 v[96:111], v[16:19], v[248:251], v[96:111]\n"
"ds_read_b64_tr_b16 v[16:17], v2 offset:0\n"
"ds_read_b64_tr_b16 v[18:19], v2 offset:512\n"
"s_waitcnt lgkmcnt(6)\n"
"v_mfma_f32_32x32x16_bf16 v[112:127], v[20:23], v[248:251], v[112:127]\n"
"ds_read_b64_tr_b16 v[20:21], v2 offset:8192\n"
"ds_read_b64_tr_b16 v[22:23], v2 offset:8704\n"
"s_waitcnt lgkmcnt(6)\n"
"v_mfma_f32_32x32x16_bf16 v[96:111], v[24:27], v[252:255], v[96:111]\n"
"ds_read_b64_tr_b16 v[24:25], v2 offset:2048\n"
"ds_read_b64_tr_b16 v[26:27], v2 offset:2560\n"
"s_waitcnt lgkmcnt(6)\n"
"v_mfma_f32_32x32x16_bf16 v[112:127], v[28:31], v[252:255], v[112:127]\n"
"ds_read_b64_tr_b16 v[28:29], v2 offset:10240\n"
"ds_read_b64_tr_b16 v[30:31], v2 offset:10752\n"
"s_waitcnt lgkmcnt(6)\n"
"v_mfma_f32_32x32x16_bf16 v[32:47], v[16:19], v[240:243], v[32:47]\n"
"ds_read_b64_tr_b16 v[16:17], v2 offset:4096\n"
"ds_read_b64_tr_b16 v[18:19], v2 offset:4608\n"
"s_waitcnt lgkmcnt(6)\n"
"v_mfma_f32_32x32x16_bf16 v[48:63], v[20:23], v[240:243], v[48:63]\n"
"ds_read_b64_tr_b16 v[20:21], v2 offset:12288\n"
"ds_read_b64_tr_b16 v[22:23], v2 offset:12800\n"
"s_waitcnt lgkmcnt(6)\n"
"v_mfma_f32_32x32x16_bf16 v[32:47], v[24:27], v[244:247], v[32:47]\n"
"ds_read_b64_tr_b16 v[24:25], v2 offset:6144\n"
"ds_read_b64_tr_b16 v[26:27], v2 offset:6656\n"
"v_mul_f32 v96, v96, v112\n"
"v_mul_f32 v97, v97, v113\n"
"v_mul_f32 v98, v98, v114\n"
"v_mul_f32 v99, v99, v115\n"
"s_waitcnt lgkmcnt(6)\n"
"v_mfma_f32_32x32x16_bf16 v[48:63], v[28:31], v[244:247], v[48:63]\n"
"ds_read_b64_tr_b16 v[28:29], v2 offset:14336\n"
"ds_read_b64_tr_b16 v[30:31], v2 offset:14848\n"
"v_mul_f32 v100, v100, v116\n"
"v_mul_f32 v101, v101, v117\n"
"v_mul_f32 v102, v102, v118\n"
"v_mul_f32 v103, v103, v119\n"
"s_waitcnt lgkmcnt(6)\n"
"v_mfma_f32_32x32x16_bf16 v[32:47], v[16:19], v[248:251], v[32:47]\n"
"s_barrier\n"
"ds_read_b64_tr_b16 v[16:17], v1 offset:16384\n"
"ds_read_b64_tr_b16 v[18:19], v1 offset:16896\n"
"v_cvt_pk_bf16_f32 v96, v96, v97\n"
"v_cvt_pk_bf16_f32 v97, v98, v99\n"
"v_cvt_pk_bf16_f32 v98, v100, v101\n"
"v_cvt_pk_bf16_f32 v99, v102, v103\n"
"s_waitcnt lgkmcnt(6)\n"
"v_mfma_f32_32x32x16_bf16 v[48:63], v[20:23], v[248:251], v[48:63]\n"
"ds_read_b64_tr_b16 v[20:21], v2 offset:16384\n"
"ds_read_b64_tr_b16 v[22:23], v2 offset:16896\n"
"ds_write_b128 v6, v[96:99] offset:8192\n"
"s_waitcnt lgkmcnt(7)\n"
"v_mfma_f32_32x32x16_bf16 v[32:47], v[24:27], v[252:255], v[32:47]\n"
"ds_read_b64_tr_b16 v[24:25], v1 offset:18432\n"
"ds_read_b64_tr_b16 v[26:27], v1 offset:18944\n"
"v_mul_f32 v104, v104, v120\n"
"v_mul_f32 v105, v105, v121\n"
"v_mul_f32 v106, v106, v122\n"
"v_mul_f32 v107, v107, v123\n"
"s_waitcnt lgkmcnt(7)\n"
"v_mfma_f32_32x32x16_bf16 v[48:63], v[28:31], v[252:255], v[48:63]\n"
"ds_read_b64_tr_b16 v[28:29], v2 offset:18432\n"
"ds_read_b64_tr_b16 v[30:31], v2 offset:18944\n"
"v_mul_f32 v108, v108, v124\n"
"v_mul_f32 v109, v109, v125\n"
"v_mul_f32 v110, v110, v126\n"
"v_mul_f32 v111, v111, v127\n"
"s_waitcnt lgkmcnt(7)\n"
"v_mfma_f32_32x32x16_bf16 v[64:79], v[16:19], v[128:131], 0\n"
"ds_read_b64_tr_b16 v[16:17], v1 offset:20480\n"
"ds_read_b64_tr_b16 v[18:19], v1 offset:20992\n"
"v_cvt_pk_bf16_f32 v104, v104, v105\n"
"v_cvt_pk_bf16_f32 v105, v106, v107\n"
"v_cvt_pk_bf16_f32 v106, v108, v109\n"
"v_cvt_pk_bf16_f32 v107, v110, v111\n"
"s_waitcnt lgkmcnt(7)\n"
"v_mfma_f32_32x32x16_bf16 v[80:95], v[20:23], v[128:131], 0\n"
"ds_read_b64_tr_b16 v[20:21], v2 offset:20480\n"
"ds_read_b64_tr_b16 v[22:23], v2 offset:20992\n"
"ds_write_b128 v6, v[104:107] offset:9216\n"
"s_waitcnt lgkmcnt(7)\n"
"v_mfma_f32_32x32x16_bf16 v[64:79], v[24:27], v[132:135], v[64:79]\n"
"ds_read_b64_tr_b16 v[24:25], v1 offset:22528\n"
"ds_read_b64_tr_b16 v[26:27], v1 offset:23040\n"
"v_mul_f32 v32, v32, v48\n"
"v_mul_f32 v33, v33, v49\n"
"v_mul_f32 v34, v34, v50\n"
"v_mul_f32 v35, v35, v51\n"
"s_waitcnt lgkmcnt(7)\n"
"v_mfma_f32_32x32x16_bf16 v[80:95], v[28:31], v[132:135], v[80:95]\n"
"ds_read_b64_tr_b16 v[28:29], v2 offset:22528\n"
"ds_read_b64_tr_b16 v[30:31], v2 offset:23040\n"
"v_mul_f32 v36, v36, v52\n"
"v_mul_f32 v37, v37, v53\n"
"v_mul_f32 v38, v38, v54\n"
"v_mul_f32 v39, v39, v55\n"
"s_waitcnt lgkmcnt(7)\n"
"v_mfma_f32_32x32x16_bf16 v[64:79], v[16:19], v[136:139], v[64:79]\n"
"ds_read_b64_tr_b16 v[16:17], v1 offset:24576\n"
"ds_read_b64_tr_b16 v[18:19], v1 offset:25088\n"
"v_cvt_pk_bf16_f32 v32, v32, v33\n"
"v_cvt_pk_bf16_f32 v33, v34, v35\n"
"v_cvt_pk_bf16_f32 v34, v36, v37\n"
"v_cvt_pk_bf16_f32 v35, v38, v39\n"
"s_waitcnt lgkmcnt(7)\n"
"v_mfma_f32_32x32x16_bf16 v[80:95], v[20:23], v[136:139], v[80:95]\n"
"ds_read_b64_tr_b16 v[20:21], v2 offset:24576\n"
"ds_read_b64_tr_b16 v[22:23], v2 offset:25088\n"
"ds_write_b128 v6, v[32:35] offset:10240\n"
"s_waitcnt lgkmcnt(7)\n"
"v_mfma_f32_32x32x16_bf16 v[64:79], v[24:27], v[140:143], v[64:79]\n"
"ds_read_b64_tr_b16 v[24:25], v1 offset:26624\n"
"ds_read_b64_tr_b16 v[26:27], v1 offset:27136\n"
"v_mul_f32 v40, v40, v56\n"
"v_mul_f32 v41, v41, v57\n"
"v_mul_f32 v42, v42, v58\n"
"v_mul_f32 v43, v43, v59\n"
"s_waitcnt lgkmcnt(7)\n"
"v_mfma_f32_32x32x16_bf16 v[80:95], v[28:31], v[140:143], v[80:95]\n"
"ds_read_b64_tr_b16 v[28:29], v2 offset:26624\n"
"ds_read_b64_tr_b16 v[30:31], v2 offset:27136\n"
"v_mul_f32 v44, v44, v60\n"
"v_mul_f32 v45, v45, v61\n"
"v_mul_f32 v46, v46, v62\n"
"v_mul_f32 v47, v47, v63\n"
"s_waitcnt lgkmcnt(7)\n"
"v_mfma_f32_32x32x16_bf16 v[64:79], v[16:19], v[144:147], v[64:79]\n"
"ds_read_b64_tr_b16 v[16:17], v1 offset:28672\n"
"ds_read_b64_tr_b16 v[18:19], v1 offset:29184\n"
"v_cvt_pk_bf16_f32 v40, v40, v41\n"
"v_cvt_pk_bf16_f32 v41, v42, v43\n"
"v_cvt_pk_bf16_f32 v42, v44, v45\n"
"v_cvt_pk_bf16_f32 v43, v46, v47\n"
"s_waitcnt lgkmcnt(7)\n"
"v_mfma_f32_32x32x16_bf16 v[80:95], v[20:23], v[144:147], v[80:95]\n"
"ds_read_b64_tr_b16 v[20:21], v2 offset:28672\n"
"ds_read_b64_tr_b16 v[22:23], v2 offset:29184\n"
"ds_write_b128 v6, v[40:43] offset:11264\n"
"s_waitcnt lgkmcnt(7)\n"
"v_mfma_f32_32x32x16_bf16 v[64:79], v[24:27], v[148:151], v[64:79]\n"
"ds_read_b64_tr_b16 v[24:25], v1 offset:30720\n"
"ds_read_b64_tr_b16 v[26:27], v1 offset:31232\n"
"s_waitcnt lgkmcnt(7)\n"
"v_mfma_f32_32x32x16_bf16 v[80:95], v[28:31], v[148:151], v[80:95]\n"
"ds_read_b64_tr_b16 v[28:29], v2 offset:30720\n"
"ds_read_b64_tr_b16 v[30:31], v2 offset:31232\n"
"s_waitcnt lgkmcnt(7)\n"
"v_mfma_f32_32x32x16_bf16 v[64:79], v[16:19], v[152:155], v[64:79]\n"
"s_waitcnt lgkmcnt(4)\n"
"s_barrier\n"
"ds_read_b64_tr_b16 v[16:17], v1 offset:32768\n"
"ds_read_b64_tr_b16 v[18:19], v1 offset:33280\n"
"v_mfma_f32_32x32x16_bf16 v[80:95], v[20:23], v[152:155], v[80:95]\n"
"ds_read_b64_tr_b16 v[20:21], v2 offset:32768\n"
"ds_read_b64_tr_b16 v[22:23], v2 offset:33280\n"
"s_waitcnt lgkmcnt(6)\n"
"v_mfma_f32_32x32x16_bf16 v[64:79], v[24:27], v[156:159], v[64:79]\n"
"ds_read_b64_tr_b16 v[24:25], v1 offset:34816\n"
"ds_read_b64_tr_b16 v[26:27], v1 offset:35328\n"
"s_waitcnt lgkmcnt(6)\n"
"v_mfma_f32_32x32x16_bf16 v[80:95], v[28:31], v[156:159], v[80:95]\n"
"ds_read_b64_tr_b16 v[28:29], v2 offset:34816\n"
"ds_read_b64_tr_b16 v[30:31], v2 offset:35328\n"
"s_waitcnt lgkmcnt(6)\n"
"v_mfma_f32_32x32x16_bf16 v[64:79], v[16:19], v[160:163], v[64:79]\n"
"ds_read_b64_tr_b16 v[16:17], v1 offset:36864\n"
"ds_read_b64_tr_b16 v[18:19], v1 offset:37376\n"
"s_waitcnt lgkmcnt(6)\n"
"v_mfma_f32_32x32x16_bf16 v[80:95], v[20:23], v[160:163], v[80:95]\n"
"ds_read_b64_tr_b16 v[20:21], v2 offset:36864\n"
"ds_read_b64_tr_b16 v[22:23], v2 offset:37376\n"
"s_waitcnt lgkmcnt(6)\n"
"v_mfma_f32_32x32x16_bf16 v[64:79], v[24:27], v[164:167], v[64:79]\n"
"ds_read_b64_tr_b16 v[24:25], v1 offset:38912\n"
"ds_read_b64_tr_b16 v[26:27], v1 offset:39424\n"
"s_waitcnt lgkmcnt(6)\n"
"v_mfma_f32_32x32x16_bf16 v[80:95], v[28:31], v[164:167], v[80:95]\n"
"ds_read_b64_tr_b16 v[28:29], v2 offset:38912\n"
"ds_read_b64_tr_b16 v[30:31], v2 offset:39424\n"
"s_waitcnt lgkmcnt(6)\n"
"v_mfma_f32_32x32x16_bf16 v[64:79], v[16:19], v[168:171], v[64:79]\n"
"ds_read_b64_tr_b16 v[16:17], v1 offset:40960\n"
"ds_read_b64_tr_b16 v[18:19], v1 offset:41472\n"
"s_waitcnt lgkmcnt(6)\n"
"v_mfma_f32_32x32x16_bf16 v[80:95], v[20:23], v[168:171], v[80:95]\n"
"ds_read_b64_tr_b16 v[20:21], v2 offset:40960\n"
"ds_read_b64_tr_b16 v[22:23], v2 offset:41472\n"
"s_waitcnt lgkmcnt(6)\n"
"v_mfma_f32_32x32x16_bf16 v[64:79], v[24:27], v[172:175], v[64:79]\n"
"ds_read_b64_tr_b16 v[24:25], v1 offset:43008\n"
"ds_read_b64_tr_b16 v[26:27], v1 offset:43520\n"
"s_waitcnt lgkmcnt(6)\n"
"v_mfma_f32_32x32x16_bf16 v[80:95], v[28:31], v[172:175], v[80:95]\n"
"ds_read_b64_tr_b16 v[28:29], v2 offset:43008\n"
"ds_read_b64_tr_b16 v[30:31], v2 offset:43520\n"
"s_waitcnt lgkmcnt(6)\n"
"v_mfma_f32_32x32x16_bf16 v[64:79], v[16:19], v[176:179], v[64:79]\n"
"ds_read_b64_tr_b16 v[16:17], v1 offset:45056\n"
"ds_read_b64_tr_b16 v[18:19], v1 offset:45568\n"
"s_waitcnt lgkmcnt(6)\n"
"v_mfma_f32_32x32x16_bf16 v[80:95], v[20:23], v[176:179], v[80:95]\n"
"ds_read_b64_tr_b16 v[20:21], v2 offset:45056\n"
"ds_read_b64_tr_b16 v[22:23], v2 offset:45568\n"
"s_waitcnt lgkmcnt(6)\n"
"v_mfma_f32_32x32x16_bf16 v[64:79], v[24:27], v[180:183], v[64:79]\n"
"ds_read_b64_tr_b16 v[24:25], v1 offset:47104\n"
"ds_read_b64_tr_b16 v[26:27], v1 offset:47616\n"
"s_waitcnt lgkmcnt(6)\n"
"v_mfma_f32_32x32x16_bf16 v[80:95], v[28:31], v[180:183], v[80:95]\n"
"ds_read_b64_tr_b16 v[28:29], v2 offset:47104\n"
"ds_read_b64_tr_b16 v[30:31], v2 offset:47616\n"
"s_waitcnt lgkmcnt(6)\n"
"v_mfma_f32_32x32x16_bf16 v[64:79], v[16:19], v[184:187], v[64:79]\n"
"s_barrier\n"
"ds_read_b64_tr_b16 v[16:17], v1 offset:49152\n"
"ds_read_b64_tr_b16 v[18:19], v1 offset:49664\n"
"s_waitcnt lgkmcnt(6)\n"
"v_mfma_f32_32x32x16_bf16 v[80:95], v[20:23], v[184:187], v[80:95]\n"
"ds_read_b64_tr_b16 v[20:21], v2 offset:49152\n"
"ds_read_b64_tr_b16 v[22:23], v2 offset:49664\n"
"s_waitcnt lgkmcnt(6)\n"
"v_mfma_f32_32x32x16_bf16 v[64:79], v[24:27], v[188:191], v[64:79]\n"
"ds_read_b64_tr_b16 v[24:25], v1 offset:51200\n"
"ds_read_b64_tr_b16 v[26:27], v1 offset:51712\n"
"s_waitcnt lgkmcnt(6)\n"
"v_mfma_f32_32x32x16_bf16 v[80:95], v[28:31], v[188:191], v[80:95]\n"
"ds_read_b64_tr_b16 v[28:29], v2 offset:51200\n"
"ds_read_b64_tr_b16 v[30:31], v2 offset:51712\n"
"s_waitcnt lgkmcnt(6)\n"
"v_mfma_f32_32x32x16_bf16 v[64:79], v[16:19], v[192:195], v[64:79]\n"
"ds_read_b64_tr_b16 v[16:17], v1 offset:53248\n"
"ds_read_b64_tr_b16 v[18:19], v1 offset:53760\n"
"s_waitcnt lgkmcnt(6)\n"
"v_mfma_f32_32x32x16_bf16 v[80:95], v[20:23], v[192:195], v[80:95]\n"
"ds_read_b64_tr_b16 v[20:21], v2 offset:53248\n"
"ds_read_b64_tr_b16 v[22:23], v2 offset:53760\n"
"s_waitcnt lgkmcnt(6)\n"
"v_mfma_f32_32x32x16_bf16 v[64:79], v[24:27], v[196:199], v[64:79]\n"
"ds_read_b64_tr_b16 v[24:25], v1 offset:55296\n"
"ds_read_b64_tr_b16 v[26:27], v1 offset:55808\n"
"s_waitcnt lgkmcnt(6)\n"
"v_mfma_f32_32x32x16_bf16 v[80:95], v[28:31], v[196:199], v[80:95]\n"
"ds_read_b64_tr_b16 v[28:29], v2 offset:55296\n"
"ds_read_b64_tr_b16 v[30:31], v2 offset:55808\n"
"s_waitcnt lgkmcnt(6)\n"
"v_mfma_f32_32x32x16_bf16 v[64:79], v[16:19], v[200:203], v[64:79]\n"
"ds_read_b64_tr_b16 v[16:17], v1 offset:57344\n"
"ds_read_b64_tr_b16 v[18:19], v1 offset:57856\n"
"s_waitcnt lgkmcnt(6)\n"
"v_mfma_f32_32x32x16_bf16 v[80:95], v[20:23], v[200:203], v[80:95]\n"
"ds_read_b64_tr_b16 v[20:21], v2 offset:57344\n"
"ds_read_b64_tr_b16 v[22:23], v2 offset:57856\n"
"s_waitcnt lgkmcnt(6)\n"
"v_mfma_f32_32x32x16_bf16 v[64:79], v[24:27], v[204:207], v[64:79]\n"
"ds_read_b64_tr_b16 v[24:25], v1 offset:59392\n"
"ds_read_b64_tr_b16 v[26:27], v1 offset:59904\n"
"s_waitcnt lgkmcnt(6)\n"
"v_mfma_f32_32x32x16_bf16 v[80:95], v[28:31], v[204:207], v[80:95]\n"
"ds_read_b64_tr_b16 v[28:29], v2 offset:59392\n"
"ds_read_b64_tr_b16 v[30:31], v2 offset:59904\n"
"s_waitcnt lgkmcnt(6)\n"
"v_mfma_f32_32x32x16_bf16 v[64:79], v[16:19], v[208:211], v[64:79]\n"
"ds_read_b64_tr_b16 v[16:17], v1 offset:61440\n"
"ds_read_b64_tr_b16 v[18:19], v1 offset:61952\n"
"s_waitcnt lgkmcnt(6)\n"
"v_mfma_f32_32x32x16_bf16 v[80:95], v[20:23], v[208:211], v[80:95]\n"
"ds_read_b64_tr_b16 v[20:21], v2 offset:61440\n"
"ds_read_b64_tr_b16 v[22:23], v2 offset:61952\n"
"s_waitcnt lgkmcnt(6)\n"
"v_mfma_f32_32x32x16_bf16 v[64:79], v[24:27], v[212:215], v[64:79]\n"
"ds_read_b64_tr_b16 v[24:25], v1 offset:63488\n"
"ds_read_b64_tr_b16 v[26:27], v1 offset:64000\n"
"s_waitcnt lgkmcnt(6)\n"
"v_mfma_f32_32x32x16_bf16 v[80:95], v[28:31], v[212:215], v[80:95]\n"
"ds_read_b64_tr_b16 v[28:29], v2 offset:63488\n"
"ds_read_b64_tr_b16 v[30:31], v2 offset:64000\n"
"s_waitcnt lgkmcnt(6)\n"
"v_mfma_f32_32x32x16_bf16 v[64:79], v[16:19], v[216:219], v[64:79]\n"
"s_barrier\n"
"ds_read_b64_tr_b16 v[16:17], v3 offset:0\n"
"ds_read_b64_tr_b16 v[18:19], v3 offset:512\n"
"s_waitcnt lgkmcnt(6)\n"
"v_mfma_f32_32x32x16_bf16 v[80:95], v[20:23], v[216:219], v[80:95]\n"
"ds_read_b64_tr_b16 v[20:21], v3 offset:2048\n"
"ds_read_b64_tr_b16 v[22:23], v3 offset:2560\n"
"s_waitcnt lgkmcnt(6)\n"
"v_mfma_f32_32x32x16_bf16 v[64:79], v[24:27], v[220:223], v[64:79]\n"
"ds_read_b64_tr_b16 v[24:25], v3 offset:4096\n"
"ds_read_b64_tr_b16 v[26:27], v3 offset:4608\n"
"s_waitcnt lgkmcnt(6)\n"
"v_mfma_f32_32x32x16_bf16 v[80:95], v[28:31], v[220:223], v[80:95]\n"
"ds_read_b64_tr_b16 v[28:29], v3 offset:6144\n"
"ds_read_b64_tr_b16 v[30:31], v3 offset:6656\n"
"s_waitcnt lgkmcnt(6)\n"
"v_mfma_f32_32x32x16_bf16 v[64:79], v[16:19], v[224:227], v[64:79]\n"
"ds_read_b64_tr_b16 v[16:17], v3 offset:8192\n"
"ds_read_b64_tr_b16 v[18:19], v3 offset:8704\n"
"s_waitcnt lgkmcnt(6)\n"
"v_mfma_f32_32x32x16_bf16 v[64:79], v[20:23], v[228:231], v[64:79]\n"
"ds_read_b64_tr_b16 v[20:21], v3 offset:10240\n"
"ds_read_b64_tr_b16 v[22:23], v3 offset:10752\n"
"s_waitcnt lgkmcnt(6)\n"
"v_mfma_f32_32x32x16_bf16 v[64:79], v[24:27], v[232:235], v[64:79]\n"
"ds_read_b64_tr_b16 v[24:25], v3 offset:12288\n"
"ds_read_b64_tr_b16 v[26:27], v3 offset:12800\n"
"s_waitcnt lgkmcnt(6)\n"
"v_mfma_f32_32x32x16_bf16 v[64:79], v[28:31], v[236:239], v[64:79]\n"
"ds_read_b64_tr_b16 v[28:29], v3 offset:14336\n"
"ds_read_b64_tr_b16 v[30:31], v3 offset:14848\n"
"s_waitcnt lgkmcnt(6)\n"
"v_mfma_f32_32x32x16_bf16 v[64:79], v[16:19], v[240:243], v[64:79]\n"
"ds_read_b64_tr_b16 v[16:17], v4 offset:0\n"
"ds_read_b64_tr_b16 v[18:19], v4 offset:512\n"
"s_waitcnt lgkmcnt(6)\n"
"v_mfma_f32_32x32x16_bf16 v[64:79], v[20:23], v[244:247], v[64:79]\n"
"ds_read_b64_tr_b16 v[20:21], v4 offset:2048\n"
"ds_read_b64_tr_b16 v[22:23], v4 offset:2560\n"
"s_waitcnt lgkmcnt(6)\n"
"v_mfma_f32_32x32x16_bf16 v[64:79], v[24:27], v[248:251], v[64:79]\n"
"ds_read_b64_tr_b16 v[24:25], v4 offset:4096\n"
"ds_read_b64_tr_b16 v[26:27], v4 offset:4608\n"
"s_waitcnt lgkmcnt(6)\n"
"v_mfma_f32_32x32x16_bf16 v[64:79], v[28:31], v[252:255], v[64:79]\n"
"ds_read_b64_tr_b16 v[28:29], v4 offset:6144\n"
"ds_read_b64_tr_b16 v[30:31], v4 offset:6656\n"
"s_waitcnt lgkmcnt(6)\n"
"v_mfma_f32_32x32x16_bf16 v[80:95], v[16:19], v[224:227], v[80:95]\n"
"ds_read_b64_tr_b16 v[16:17], v4 offset:8192\n"
"ds_read_b64_tr_b16 v[18:19], v4 offset:8704\n"
"s_waitcnt lgkmcnt(6)\n"
"v_mfma_f32_32x32x16_bf16 v[80:95], v[20:23], v[228:231], v[80:95]\n"
"ds_read_b64_tr_b16 v[20:21], v4 offset:10240\n"
"ds_read_b64_tr_b16 v[22:23], v4 offset:10752\n"
"s_waitcnt lgkmcnt(6)\n"
"v_mfma_f32_32x32x16_bf16 v[80:95], v[24:27], v[232:235], v[80:95]\n"
"ds_read_b64_tr_b16 v[24:25], v4 offset:12288\n"
"ds_read_b64_tr_b16 v[26:27], v4 offset:12800\n"
"v_mul_f32 v128, 0x3fb8aa3b, v64\n"
"v_mul_f32 v129, 0x3fb8aa3b, v65\n"
"v_mul_f32 v130, 0x3fb8aa3b, v66\n"
"v_mul_f32 v131, 0x3fb8aa3b, v67\n"
"s_waitcnt lgkmcnt(6)\n"
"v_mfma_f32_32x32x16_bf16 v[80:95], v[28:31], v[236:239], v[80:95]\n"
"ds_read_b64_tr_b16 v[28:29], v4 offset:14336\n"
"ds_read_b64_tr_b16 v[30:31], v4 offset:14848\n"
"v_mul_f32 v132, 0x3fb8aa3b, v68\n"
"v_mul_f32 v133, 0x3fb8aa3b, v69\n"
"v_mul_f32 v134, 0x3fb8aa3b, v70\n"
"v_mul_f32 v135, 0x3fb8aa3b, v71\n"
"s_waitcnt lgkmcnt(6)\n"
"v_mfma_f32_32x32x16_bf16 v[80:95], v[16:19], v[240:243], v[80:95]\n"
"s_barrier\n"
"v_exp_f32 v128, v128 clamp\n"
"v_max_f32 v64, 0, v64\n"
"v_exp_f32 v129, v129 clamp\n"
"v_max_f32 v65, 0, v65\n"
"s_waitcnt lgkmcnt(4)\n"
"v_mfma_f32_32x32x16_bf16 v[80:95], v[20:23], v[244:247], v[80:95]\n"
"v_exp_f32 v130, v130 clamp\n"
"v_max_f32 v66, 0, v66\n"
"v_exp_f32 v131, v131 clamp\n"
"v_max_f32 v67, 0, v67\n"
"s_waitcnt lgkmcnt(2)\n"
"v_mfma_f32_32x32x16_bf16 v[80:95], v[24:27], v[248:251], v[80:95]\n"
"v_exp_f32 v132, v132 clamp\n"
"v_max_f32 v68, 0, v68\n"
"v_exp_f32 v133, v133 clamp\n"
"v_max_f32 v69, 0, v69\n"
"s_waitcnt lgkmcnt(0)\n"
"v_mfma_f32_32x32x16_bf16 v[80:95], v[28:31], v[252:255], v[80:95]\n"
"v_exp_f32 v134, v134 clamp\n"
"v_max_f32 v70, 0, v70\n"
"v_exp_f32 v135, v135 clamp\n"
"v_max_f32 v71, 0, v71\n"
"v_add_f32 v64, v64, v128\n"
"v_add_f32 v65, v65, v129\n"
"v_add_f32 v66, v66, v130\n"
"v_add_f32 v67, v67, v131\n"
"v_add_f32 v68, v68, v132\n"
"v_add_f32 v69, v69, v133\n"
"v_add_f32 v70, v70, v134\n"
"v_add_f32 v71, v71, v135\n"
"v_add_f32 v64, 0x358637bd, v64\n"
"v_add_f32 v65, 0x358637bd, v65\n"
"v_add_f32 v66, 0x358637bd, v66\n"
"v_add_f32 v67, 0x358637bd, v67\n"
"v_add_f32 v68, 0x358637bd, v68\n"
"v_add_f32 v69, 0x358637bd, v69\n"
"v_add_f32 v70, 0x358637bd, v70\n"
"v_add_f32 v71, 0x358637bd, v71\n"
"v_cvt_pk_bf16_f32 v64, v64, v65\n"
"v_cvt_pk_bf16_f32 v65, v66, v67\n"
"v_cvt_pk_bf16_f32 v66, v68, v69\n"
"v_cvt_pk_bf16_f32 v67, v70, v71\n"
"ds_read_b128 v[160:163], v13 offset:0\n"
"ds_read_b128 v[164:167], v13 offset:1024\n"
"ds_read_b128 v[168:171], v13 offset:2048\n"
"ds_read_b128 v[172:175], v13 offset:3072\n"
"v_mul_f32 v136, 0x3fb8aa3b, v72\n"
"v_mul_f32 v137, 0x3fb8aa3b, v73\n"
"v_mul_f32 v138, 0x3fb8aa3b, v74\n"
"v_mul_f32 v139, 0x3fb8aa3b, v75\n"
"v_mul_f32 v140, 0x3fb8aa3b, v76\n"
"v_mul_f32 v141, 0x3fb8aa3b, v77\n"
"v_mul_f32 v142, 0x3fb8aa3b, v78\n"
"v_mul_f32 v143, 0x3fb8aa3b, v79\n"
"v_exp_f32 v136, v136 clamp\n"
"v_max_f32 v72, 0, v72\n"
"v_exp_f32 v137, v137 clamp\n"
"v_max_f32 v73, 0, v73\n"
"v_exp_f32 v138, v138 clamp\n"
"v_max_f32 v74, 0, v74\n"
"v_exp_f32 v139, v139 clamp\n"
"v_max_f32 v75, 0, v75\n"
"v_exp_f32 v140, v140 clamp\n"
"v_max_f32 v76, 0, v76\n"
"v_exp_f32 v141, v141 clamp\n"
"v_max_f32 v77, 0, v77\n"
"v_exp_f32 v142, v142 clamp\n"
"v_max_f32 v78, 0, v78\n"
"v_exp_f32 v143, v143 clamp\n"
"v_max_f32 v79, 0, v79\n"
"v_add_f32 v72, v72, v136\n"
"v_add_f32 v73, v73, v137\n"
"v_add_f32 v74, v74, v138\n"
"v_add_f32 v75, v75, v139\n"
"v_add_f32 v76, v76, v140\n"
"v_add_f32 v77, v77, v141\n"
"v_add_f32 v78, v78, v142\n"
"v_add_f32 v79, v79, v143\n"
"v_add_f32 v72, 0x358637bd, v72\n"
"v_add_f32 v73, 0x358637bd, v73\n"
"v_add_f32 v74, 0x358637bd, v74\n"
"v_add_f32 v75, 0x358637bd, v75\n"
"v_add_f32 v76, 0x358637bd, v76\n"
"v_add_f32 v77, 0x358637bd, v77\n"
"v_add_f32 v78, 0x358637bd, v78\n"
"v_add_f32 v79, 0x358637bd, v79\n"
"v_cvt_pk_bf16_f32 v72, v72, v73\n"
"v_cvt_pk_bf16_f32 v73, v74, v75\n"
"v_cvt_pk_bf16_f32 v74, v76, v77\n"
"v_cvt_pk_bf16_f32 v75, v78, v79\n"
"s_waitcnt lgkmcnt(3)\n"
"v_mfma_f32_32x32x16_bf16 v[32:47], v[160:163], v[64:67], 0\n"
"s_waitcnt lgkmcnt(2)\n"
"v_mfma_f32_32x32x16_bf16 v[48:63], v[164:167], v[72:75], 0\n"
"v_mul_f32 v144, 0x3fb8aa3b, v80\n"
"v_mul_f32 v145, 0x3fb8aa3b, v81\n"
"v_mul_f32 v146, 0x3fb8aa3b, v82\n"
"v_mul_f32 v147, 0x3fb8aa3b, v83\n"
"v_mul_f32 v148, 0x3fb8aa3b, v84\n"
"v_mul_f32 v149, 0x3fb8aa3b, v85\n"
"v_mul_f32 v150, 0x3fb8aa3b, v86\n"
"v_mul_f32 v151, 0x3fb8aa3b, v87\n"
"v_exp_f32 v144, v144 clamp\n"
"v_max_f32 v80, 0, v80\n"
"v_exp_f32 v145, v145 clamp\n"
"v_max_f32 v81, 0, v81\n"
"v_exp_f32 v146, v146 clamp\n"
"v_max_f32 v82, 0, v82\n"
"v_exp_f32 v147, v147 clamp\n"
"v_max_f32 v83, 0, v83\n"
"v_exp_f32 v148, v148 clamp\n"
"v_max_f32 v84, 0, v84\n"
"v_exp_f32 v149, v149 clamp\n"
"v_max_f32 v85, 0, v85\n"
"v_exp_f32 v150, v150 clamp\n"
"v_max_f32 v86, 0, v86\n"
"v_exp_f32 v151, v151 clamp\n"
"v_max_f32 v87, 0, v87\n"
"v_add_f32 v80, v80, v144\n"
"v_add_f32 v81, v81, v145\n"
"v_add_f32 v82, v82, v146\n"
"v_add_f32 v83, v83, v147\n"
"v_add_f32 v84, v84, v148\n"
"v_add_f32 v85, v85, v149\n"
"v_add_f32 v86, v86, v150\n"
"v_add_f32 v87, v87, v151\n"
"v_add_f32 v80, 0x358637bd, v80\n"
"v_add_f32 v81, 0x358637bd, v81\n"
"v_add_f32 v82, 0x358637bd, v82\n"
"v_add_f32 v83, 0x358637bd, v83\n"
"v_add_f32 v84, 0x358637bd, v84\n"
"v_add_f32 v85, 0x358637bd, v85\n"
"v_add_f32 v86, 0x358637bd, v86\n"
"v_add_f32 v87, 0x358637bd, v87\n"
"v_cvt_pk_bf16_f32 v80, v80, v81\n"
"v_cvt_pk_bf16_f32 v81, v82, v83\n"
"v_cvt_pk_bf16_f32 v82, v84, v85\n"
"v_cvt_pk_bf16_f32 v83, v86, v87\n"
"s_waitcnt lgkmcnt(1)\n"
"s_nop 0\n"
"v_mfma_f32_32x32x16_bf16 v[96:111], v[168:171], v[80:83], 0\n"
"v_add_f32 v40, 0x358637bd, v40\n"
"v_rcp_f32 v40, v40\n"
"s_nop 0\n"
"v_mul_f32 v32, v32, v40\n"
"v_mul_f32 v33, v33, v40\n"
"v_mul_f32 v34, v34, v40\n"
"v_mul_f32 v35, v35, v40\n"
"v_mul_f32 v36, v36, v40\n"
"v_mul_f32 v37, v37, v40\n"
"v_mul_f32 v38, v38, v40\n"
"v_mul_f32 v39, v39, v40\n"
"v_cvt_pk_bf16_f32 v32, v32, v33\n"
"LPAD6:\n"
"s_cbranch_execz LBKA\n"
"v_cvt_pk_bf16_f32 v33, v34, v35\n"
"v_cvt_pk_bf16_f32 v34, v36, v37\n"
"v_cvt_pk_bf16_f32 v35, v38, v39\n"
"buffer_store_dwordx4 v[32:35], v6, s[32:35], s57 offen sc1\n"
"v_add_f32 v56, 0x358637bd, v56\n"
"v_rcp_f32 v56, v56\n"
"s_nop 0\n"
"v_mul_f32 v48, v48, v56\n"
"v_mul_f32 v49, v49, v56\n"
"v_mul_f32 v50, v50, v56\n"
"v_mul_f32 v51, v51, v56\n"
"v_mul_f32 v52, v52, v56\n"
"v_mul_f32 v53, v53, v56\n"
"v_mul_f32 v54, v54, v56\n"
"v_mul_f32 v55, v55, v56\n"
"v_cvt_pk_bf16_f32 v48, v48, v49\n"
"v_cvt_pk_bf16_f32 v49, v50, v51\n"
"v_cvt_pk_bf16_f32 v50, v52, v53\n"
"v_cvt_pk_bf16_f32 v51, v54, v55\n"
"buffer_store_dwordx4 v[48:51], v6, s[32:35], s57 offen offset:1024 sc1\n"
"v_mul_f32 v152, 0x3fb8aa3b, v88\n"
"v_mul_f32 v153, 0x3fb8aa3b, v89\n"
"v_mul_f32 v154, 0x3fb8aa3b, v90\n"
"v_mul_f32 v155, 0x3fb8aa3b, v91\n"
"v_mul_f32 v156, 0x3fb8aa3b, v92\n"
"v_mul_f32 v157, 0x3fb8aa3b, v93\n"
"v_mul_f32 v158, 0x3fb8aa3b, v94\n"
"v_mul_f32 v159, 0x3fb8aa3b, v95\n"
"v_exp_f32 v152, v152 clamp\n"
"v_max_f32 v88, 0, v88\n"
"v_exp_f32 v153, v153 clamp\n"
"v_max_f32 v89, 0, v89\n"
"v_exp_f32 v154, v154 clamp\n"
"v_max_f32 v90, 0, v90\n"
"v_exp_f32 v155, v155 clamp\n"
"v_max_f32 v91, 0, v91\n"
"v_exp_f32 v156, v156 clamp\n"
"v_max_f32 v92, 0, v92\n"
"v_exp_f32 v157, v157 clamp\n"
"v_max_f32 v93, 0, v93\n"
"v_exp_f32 v158, v158 clamp\n"
"v_max_f32 v94, 0, v94\n"
"v_exp_f32 v159, v159 clamp\n"
"v_max_f32 v95, 0, v95\n"
"v_add_f32 v88, v88, v152\n"
"v_add_f32 v89, v89, v153\n"
"v_add_f32 v90, v90, v154\n"
"v_add_f32 v91, v91, v155\n"
"v_add_f32 v92, v92, v156\n"
"v_add_f32 v93, v93, v157\n"
"v_add_f32 v94, v94, v158\n"
"v_add_f32 v95, v95, v159\n"
"v_add_f32 v88, 0x358637bd, v88\n"
"v_add_f32 v89, 0x358637bd, v89\n"
"v_add_f32 v90, 0x358637bd, v90\n"
"v_add_f32 v91, 0x358637bd, v91\n"
"v_add_f32 v92, 0x358637bd, v92\n"
"v_add_f32 v93, 0x358637bd, v93\n"
"v_add_f32 v94, 0x358637bd, v94\n"
"v_add_f32 v95, 0x358637bd, v95\n"
"v_cvt_pk_bf16_f32 v88, v88, v89\n"
"v_cvt_pk_bf16_f32 v89, v90, v91\n"
"v_cvt_pk_bf16_f32 v90, v92, v93\n"
"v_cvt_pk_bf16_f32 v91, v94, v95\n"
"s_waitcnt lgkmcnt(0)\n"
"s_nop 0\n"
"v_mfma_f32_32x32x16_bf16 v[112:127], v[172:175], v[88:91], 0\n"
"v_add_f32 v104, 0x358637bd, v104\n"
"v_rcp_f32 v104, v104\n"
"s_nop 0\n"
"v_mul_f32 v96, v96, v104\n"
"v_mul_f32 v97, v97, v104\n"
"v_mul_f32 v98, v98, v104\n"
"v_mul_f32 v99, v99, v104\n"
"v_mul_f32 v100, v100, v104\n"
"v_mul_f32 v101, v101, v104\n"
"v_mul_f32 v102, v102, v104\n"
"v_mul_f32 v103, v103, v104\n"
"v_cvt_pk_bf16_f32 v96, v96, v97\n"
"v_cvt_pk_bf16_f32 v97, v98, v99\n"
"v_cvt_pk_bf16_f32 v98, v100, v101\n"
"v_cvt_pk_bf16_f32 v99, v102, v103\n"
"buffer_store_dwordx4 v[96:99], v6, s[32:35], s57 offen offset:2048 sc1\n"
"v_add_f32 v120, 0x358637bd, v120\n"
"v_rcp_f32 v120, v120\n"
"s_nop 0\n"
"v_mul_f32 v112, v112, v120\n"
"v_mul_f32 v113, v113, v120\n"
"v_mul_f32 v114, v114, v120\n"
"v_mul_f32 v115, v115, v120\n"
"v_mul_f32 v116, v116, v120\n"
"v_mul_f32 v117, v117, v120\n"
"v_mul_f32 v118, v118, v120\n"
"v_mul_f32 v119, v119, v120\n"
"v_cvt_pk_bf16_f32 v112, v112, v113\n"
"v_cvt_pk_bf16_f32 v113, v114, v115\n"
"v_cvt_pk_bf16_f32 v114, v116, v117\n"
"v_cvt_pk_bf16_f32 v115, v118, v119\n"
"buffer_store_dwordx4 v[112:115], v6, s[32:35], s57 offen offset:3072 sc1\n"
"v_mov_b32 v14, s40\n"
"v_mov_b32 v15, s41\n"
"v_lshrrev_b32 v1, 5, v0\n"
"v_lshlrev_b32 v1, 9, v1\n"
"v_bfe_u32 v16, v0, 2, 2\n"
"v_lshl_add_u32 v1, v16, 7, v1\n"
"v_bfe_u32 v16, v0, 3, 1\n"
"v_lshl_add_u32 v1, v16, 6, v1\n"
"v_bfe_u32 v16, v0, 4, 1\n"
"v_lshl_add_u32 v1, v16, 5, v1\n"
"v_and_b32 v16, 3, v0\n"
"v_lshl_add_u32 v1, v16, 3, v1\n"
"s_and_b32 s51, s42, 1\n"
"s_mul_i32 s49, s51, 8256\n"
"v_xor_b32 v1, s49, v1\n"
"v_xor_b32 v2, 64, v1\n"
"s_lshr_b32 s50, s42, 1\n"
"s_mul_i32 s49, s50, 147456\n"
"v_lshlrev_b32 v16, 4, v0\n"
"v_add_u32 v3, s49, v16\n"
"s_lshl_b32 s49, s46, 9\n"
"s_lshl_b32 s52, s42, 5\n"
"s_add_u32 s49, s49, s52\n"
"s_add_u32 s49, s49, 655744\n"
"v_and_b32 v4, 3, v0\n"
"v_lshlrev_b32 v4, 3, v4\n"
"v_add_u32 v4, s49, v4\n"
"s_mul_i32 s49, s46, 294912\n"
"s_lshl_b32 s52, s51, 14\n"
"s_add_u32 s49, s49, s52\n"
"s_add_u32 s49, s49, 1163264\n"
"s_add_u32 s52, s3, 0\n"
"s_and_b32 s52, s52, 3\n"
"s_lshl_b32 s52, s52, 10\n"
"s_add_u32 s68, s52, s49\n"
"s_add_u32 s52, s3, 1\n"
"s_and_b32 s52, s52, 3\n"
"s_lshl_b32 s52, s52, 10\n"
"s_add_u32 s69, s52, s49\n"
"s_add_u32 s52, s3, 2\n"
"s_and_b32 s52, s52, 3\n"
"s_lshl_b32 s52, s52, 10\n"
"s_add_u32 s70, s52, s49\n"
"s_add_u32 s52, s3, 3\n"
"s_and_b32 s52, s52, 3\n"
"s_lshl_b32 s52, s52, 10\n"
"s_add_u32 s71, s52, s49\n"
"s_lshl_b32 s49, s42, 12\n"
"s_add_u32 s49, s49, 65536\n"
"v_add_u32 v10, s49, v16\n"
"s_lshl_b32 s49, s42, 11\n"
"s_add_u32 s49, s49, 155648\n"
"v_add_u32 v11, s49, v16\n"
"v_and_b32 v12, 31, v0\n"
"v_lshlrev_b32 v12, 10, v12\n"
"v_lshrrev_b32 v17, 5, v0\n"
"v_lshl_add_u32 v12, v17, 4, v12\n"
"s_lshl_b32 s72, s46, 16\n"
"s_lshr_b32 s50, s42, 1\n"
"s_lshl_b32 s50, s50, 15\n"
"s_add_u32 s72, s72, s50\n"
"s_lshl_b32 s50, s47, 7\n"
"s_add_u32 s72, s72, s50\n"
"s_and_b32 s50, s42, 1\n"
"s_lshl_b32 s50, s50, 6\n"
"s_add_u32 s72, s72, s50\n"
"s_xor_b32 s49, s42, 1\n"
"s_mul_i32 s49, s49, 18432\n"
"s_add_u32 s49, s49, 83968\n"
"v_add_u32 v18, s49, v16\n"
"s_xor_b32 s49, s42, 2\n"
"s_mul_i32 s49, s49, 18432\n"
"s_add_u32 s49, s49, 88064\n"
"v_add_u32 v19, s49, v16\n"
"s_xor_b32 s49, s42, 3\n"
"s_mul_i32 s49, s49, 18432\n"
"s_add_u32 s49, s49, 92160\n"
"v_add_u32 v20, s49, v16\n"
"s_xor_b32 s49, s42, 1\n"
"s_lshl_b32 s49, s49, 12\n"
"s_add_u32 s49, s49, 67584\n"
"v_add_u32 v21, s49, v16\n"
"s_mul_i32 s49, s42, 18432\n"
"s_add_u32 s49, s49, 94208\n"
"v_add_u32 v22, s49, v16\n"
"s_lshr_b32 s49, s42, 1\n"
"s_lshl_b32 s49, s49, 7\n"
"s_add_u32 s49, s49, 96256\n"
"v_and_b32 v17, 31, v0\n"
"v_lshl_add_u32 v17, v17, 2, s49\n"
"s_barrier\n"
"ds_read_b64_tr_b16 v[192:193], v1 offset:0\n"
"ds_read_b64_tr_b16 v[194:195], v1 offset:1024\n"
"ds_read_b64_tr_b16 v[196:197], v2 offset:0\n"
"ds_read_b64_tr_b16 v[198:199], v2 offset:1024\n"
"ds_read_b64_tr_b16 v[200:201], v1 offset:2048\n"
"ds_read_b64_tr_b16 v[202:203], v1 offset:3072\n"
"ds_read_b64_tr_b16 v[204:205], v2 offset:2048\n"
"ds_read_b64_tr_b16 v[206:207], v2 offset:3072\n"
"ds_read_b64_tr_b16 v[208:209], v1 offset:4096\n"
"ds_read_b64_tr_b16 v[210:211], v1 offset:5120\n"
"ds_read_b64_tr_b16 v[212:213], v2 offset:4096\n"
"ds_read_b64_tr_b16 v[214:215], v2 offset:5120\n"
"s_waitcnt vmcnt(0)\n"
"s_mov_b64 exec, 1\n"
"buffer_store_dwordx2 v[14:15], off, s[32:35], s58 sc1\n"
"s_mov_b64 exec, -1\n"
"buffer_load_dwordx2 v[8:9], v4, s[32:35], 0 offen sc1\n"
"s_waitcnt vmcnt(0)\n"
"v_cmp_eq_u64 vcc, s[40:41], v[8:9]\n"
"s_nop 0\n"
"s_and_b32 s49, vcc_lo, 0xf\n"
"s_cmp_eq_u32 s49, 0xf\n"
"s_cbranch_scc0 LCA_slow_1\n"
"LCA_back_2:\n"
"buffer_load_dwordx4 v[128:131], v3, s[32:35], s68 offen sc1\n"
"s_add_u32 s68, s68, 4096\n"
"buffer_load_dwordx4 v[132:135], v3, s[32:35], s68 offen sc1\n"
"s_add_u32 s68, s68, 4096\n"
"buffer_load_dwordx4 v[136:139], v3, s[32:35], s68 offen sc1\n"
"s_add_u32 s68, s68, 4096\n"
"buffer_load_dwordx4 v[140:143], v3, s[32:35], s68 offen sc1\n"
"buffer_load_dwordx4 v[144:147], v3, s[32:35], s69 offen sc1\n"
"s_add_u32 s69, s69, 4096\n"
"buffer_load_dwordx4 v[148:151], v3, s[32:35], s69 offen sc1\n"
"s_add_u32 s69, s69, 4096\n"
"buffer_load_dwordx4 v[152:155], v3, s[32:35], s69 offen sc1\n"
"s_add_u32 s69, s69, 4096\n"
"buffer_load_dwordx4 v[156:159], v3, s[32:35], s69 offen sc1\n"
"buffer_load_dwordx4 v[160:163], v3, s[32:35], s70 offen sc1\n"
"s_add_u32 s70, s70, 4096\n"
"buffer_load_dwordx4 v[164:167], v3, s[32:35], s70 offen sc1\n"
"s_add_u32 s70, s70, 4096\n"
"buffer_load_dwordx4 v[168:171], v3, s[32:35], s70 offen sc1\n"
"s_add_u32 s70, s70, 4096\n"
"buffer_load_dwordx4 v[172:175], v3, s[32:35], s70 offen sc1\n"
"buffer_load_dwordx4 v[176:179], v3, s[32:35], s71 offen sc1\n"
"s_add_u32 s71, s71, 4096\n"
"buffer_load_dwordx4 v[180:183], v3, s[32:35], s71 offen sc1\n"
"s_add_u32 s71, s71, 4096\n"
"buffer_load_dwordx4 v[184:187], v3, s[32:35], s71 offen sc1\n"
"s_add_u32 s71, s71, 4096\n"
"buffer_load_dwordx4 v[188:191], v3, s[32:35], s71 offen sc1\n"
"s_waitcnt vmcnt(15) lgkmcnt(8)\n"
"v_mfma_f32_32x32x16_bf16 v[112:127], v[196:199], v[128:131], 0\n"
"v_mfma_f32_32x32x16_bf16 v[96:111], v[192:195], v[128:131], 0\n"
"ds_read_b64_tr_b16 v[216:217], v1 offset:6144\n"
"ds_read_b64_tr_b16 v[218:219], v1 offset:7168\n"
"ds_read_b64_tr_b16 v[220:221], v2 offset:6144\n"
"ds_read_b64_tr_b16 v[222:223], v2 offset:7168\n"
"s_waitcnt vmcnt(14) lgkmcnt(8)\n"
"v_mfma_f32_32x32x16_bf16 v[112:127], v[204:207], v[132:135], v[112:127]\n"
"v_mfma_f32_32x32x16_bf16 v[96:111], v[200:203], v[132:135], v[96:111]\n"
"ds_read_b64_tr_b16 v[192:193], v1 offset:16384\n"
"ds_read_b64_tr_b16 v[194:195], v1 offset:17408\n"
"ds_read_b64_tr_b16 v[196:197], v2 offset:16384\n"
"ds_read_b64_tr_b16 v[198:199], v2 offset:17408\n"
"s_waitcnt vmcnt(13) lgkmcnt(8)\n"
"v_mfma_f32_32x32x16_bf16 v[112:127], v[212:215], v[136:139], v[112:127]\n"
"v_mfma_f32_32x32x16_bf16 v[96:111], v[208:211], v[136:139], v[96:111]\n"
"ds_read_b64_tr_b16 v[200:201], v1 offset:18432\n"
"ds_read_b64_tr_b16 v[202:203], v1 offset:19456\n"
"ds_read_b64_tr_b16 v[204:205], v2 offset:18432\n"
"ds_read_b64_tr_b16 v[206:207], v2 offset:19456\n"
"s_waitcnt vmcnt(12) lgkmcnt(8)\n"
"v_mfma_f32_32x32x16_bf16 v[112:127], v[220:223], v[140:143], v[112:127]\n"
"v_mfma_f32_32x32x16_bf16 v[96:111], v[216:219], v[140:143], v[96:111]\n"
"ds_read_b64_tr_b16 v[208:209], v1 offset:20480\n"
"ds_read_b64_tr_b16 v[210:211], v1 offset:21504\n"
"ds_read_b64_tr_b16 v[212:213], v2 offset:20480\n"
"ds_read_b64_tr_b16 v[214:215], v2 offset:21504\n"
"s_waitcnt vmcnt(11) lgkmcnt(8)\n"
"v_mfma_f32_32x32x16_bf16 v[112:127], v[196:199], v[144:147], v[112:127]\n"
"v_mfma_f32_32x32x16_bf16 v[96:111], v[192:195], v[144:147], v[96:111]\n"
"ds_read_b64_tr_b16 v[216:217], v1 offset:22528\n"
"ds_read_b64_tr_b16 v[218:219], v1 offset:23552\n"
"ds_read_b64_tr_b16 v[220:221], v2 offset:22528\n"
"ds_read_b64_tr_b16 v[222:223], v2 offset:23552\n"
"s_waitcnt vmcnt(10) lgkmcnt(8)\n"
"v_mfma_f32_32x32x16_bf16 v[112:127], v[204:207], v[148:151], v[112:127]\n"
"v_mfma_f32_32x32x16_bf16 v[96:111], v[200:203], v[148:151], v[96:111]\n"
"ds_read_b64_tr_b16 v[192:193], v1 offset:32768\n"
"ds_read_b64_tr_b16 v[194:195], v1 offset:33792\n"
"ds_read_b64_tr_b16 v[196:197], v2 offset:32768\n"
"ds_read_b64_tr_b16 v[198:199], v2 offset:33792\n"
"s_waitcnt vmcnt(9) lgkmcnt(8)\n"
"v_mfma_f32_32x32x16_bf16 v[112:127], v[212:215], v[152:155], v[112:127]\n"
"v_mfma_f32_32x32x16_bf16 v[96:111], v[208:211], v[152:155], v[96:111]\n"
"ds_read_b64_tr_b16 v[200:201], v1 offset:34816\n"
"ds_read_b64_tr_b16 v[202:203], v1 offset:35840\n"
"ds_read_b64_tr_b16 v[204:205], v2 offset:34816\n"
"ds_read_b64_tr_b16 v[206:207], v2 offset:35840\n"
"s_waitcnt vmcnt(8) lgkmcnt(8)\n"
"v_mfma_f32_32x32x16_bf16 v[112:127], v[220:223], v[156:159], v[112:127]\n"
"v_mfma_f32_32x32x16_bf16 v[96:111], v[216:219], v[156:159], v[96:111]\n"
"ds_read_b64_tr_b16 v[208:209], v1 offset:36864\n"
"ds_read_b64_tr_b16 v[210:211], v1 offset:37888\n"
"ds_read_b64_tr_b16 v[212:213], v2 offset:36864\n"
"ds_read_b64_tr_b16 v[214:215], v2 offset:37888\n"
"s_waitcnt vmcnt(7) lgkmcnt(8)\n"
"v_mfma_f32_32x32x16_bf16 v[112:127], v[196:199], v[160:163], v[112:127]\n"
"v_mfma_f32_32x32x16_bf16 v[96:111], v[192:195], v[160:163], v[96:111]\n"
"ds_read_b64_tr_b16 v[216:217], v1 offset:38912\n"
"ds_read_b64_tr_b16 v[218:219], v1 offset:39936\n"
"ds_read_b64_tr_b16 v[220:221], v2 offset:38912\n"
"ds_read_b64_tr_b16 v[222:223], v2 offset:39936\n"
"s_waitcnt vmcnt(6) lgkmcnt(8)\n"
"v_mfma_f32_32x32x16_bf16 v[112:127], v[204:207], v[164:167], v[112:127]\n"
"v_mfma_f32_32x32x16_bf16 v[96:111], v[200:203], v[164:167], v[96:111]\n"
"ds_read_b64_tr_b16 v[192:193], v1 offset:49152\n"
"ds_read_b64_tr_b16 v[194:195], v1 offset:50176\n"
"ds_read_b64_tr_b16 v[196:197], v2 offset:49152\n"
"ds_read_b64_tr_b16 v[198:199], v2 offset:50176\n"
"s_waitcnt vmcnt(5) lgkmcnt(8)\n"
"v_mfma_f32_32x32x16_bf16 v[112:127], v[212:215], v[168:171], v[112:127]\n"
"v_mfma_f32_32x32x16_bf16 v[96:111], v[208:211], v[168:171], v[96:111]\n"
"ds_read_b64_tr_b16 v[200:201], v1 offset:51200\n"
"ds_read_b64_tr_b16 v[202:203], v1 offset:52224\n"
"ds_read_b64_tr_b16 v[204:205], v2 offset:51200\n"
"ds_read_b64_tr_b16 v[206:207], v2 offset:52224\n"
"s_waitcnt vmcnt(4) lgkmcnt(8)\n"
"v_mfma_f32_32x32x16_bf16 v[112:127], v[220:223], v[172:175], v[112:127]\n"
"v_mfma_f32_32x32x16_bf16 v[96:111], v[216:219], v[172:175], v[96:111]\n"
"ds_read_b64_tr_b16 v[208:209], v1 offset:53248\n"
"ds_read_b64_tr_b16 v[210:211], v1 offset:54272\n"
"ds_read_b64_tr_b16 v[212:213], v2 offset:53248\n"
"ds_read_b64_tr_b16 v[214:215], v2 offset:54272\n"
"ds_read_b64_tr_b16 v[216:217], v1 offset:55296\n"
"ds_read_b64_tr_b16 v[218:219], v1 offset:56320\n"
"ds_read_b64_tr_b16 v[220:221], v2 offset:55296\n"
"s_waitcnt lgkmcnt(14)\n"
"ds_read_b64_tr_b16 v[222:223], v2 offset:56320\n"
"s_waitcnt vmcnt(3) lgkmcnt(12)\n"
"v_mfma_f32_32x32x16_bf16 v[112:127], v[196:199], v[176:179], v[112:127]\n"
"s_waitcnt vmcnt(2) lgkmcnt(8)\n"
"v_mfma_f32_32x32x16_bf16 v[112:127], v[204:207], v[180:183], v[112:127]\n"
"s_waitcnt vmcnt(1) lgkmcnt(4)\n"
"v_mfma_f32_32x32x16_bf16 v[112:127], v[212:215], v[184:187], v[112:127]\n"
"s_waitcnt vmcnt(0) lgkmcnt(0)\n"
"v_mfma_f32_32x32x16_bf16 v[112:127], v[220:223], v[188:191], v[112:127]\n"
"v_mfma_f32_32x32x16_bf16 v[96:111], v[192:195], v[176:179], v[96:111]\n"
"v_mfma_f32_32x32x16_bf16 v[96:111], v[200:203], v[180:183], v[96:111]\n"
"v_mfma_f32_32x32x16_bf16 v[96:111], v[208:211], v[184:187], v[96:111]\n"
"v_mfma_f32_32x32x16_bf16 v[96:111], v[216:219], v[188:191], v[96:111]\n"
"s_nop 7\n"
"ds_write_b128 v10, v[112:115] offset:0\n"
"ds_write_b128 v10, v[116:119] offset:1024\n"
"ds_write_b128 v10, v[120:123] offset:2048\n"
"ds_write_b128 v10, v[124:127] offset:3072\n"
"ds_write_b128 v11, v[96:99] offset:0\n"
"ds_write_b128 v11, v[100:103] offset:1024\n"
"s_waitcnt lgkmcnt(0)\n"
"s_barrier\n"
"ds_read_b32 v72, v17 offset:0\n"
"ds_read_b32 v73, v17 offset:18432\n"
"ds_read_b32 v74, v17 offset:36864\n"
"ds_read_b32 v75, v17 offset:55296\n"
"ds_read_b128 v[32:35], v18 offset:0\n"
"ds_read_b128 v[36:39], v18 offset:1024\n"
"ds_read_b128 v[40:43], v19 offset:0\n"
"ds_read_b128 v[44:47], v19 offset:1024\n"
"ds_read_b128 v[48:51], v20 offset:0\n"
"ds_read_b128 v[52:55], v20 offset:1024\n"
"ds_read_b128 v[64:67], v22 offset:0\n"
"ds_read_b128 v[68:71], v22 offset:1024\n"
"ds_read_b128 v[56:59], v21 offset:0\n"
"ds_read_b128 v[60:63], v21 offset:1024\n"
"s_waitcnt lgkmcnt(12)\n"
"v_add_f32 v72, v72, v73\n"
"s_waitcnt lgkmcnt(10)\n"
"v_add_f32 v74, v74, v75\n"
"v_add_f32 v72, v72, v74\n"
"v_mul_f32 v72, 0xbfb8aa3b, v72\n"
"v_exp_f32 v72, v72\n"
"s_nop 0\n"
"v_add_f32 v72, 1.0, v72\n"
"v_rcp_f32 v72, v72\n"
"s_waitcnt lgkmcnt(7)\n"
"v_add_f32 v32, v32, v40\n"
"v_add_f32 v33, v33, v41\n"
"v_add_f32 v34, v34, v42\n"
"v_add_f32 v35, v35, v43\n"
"s_waitcnt lgkmcnt(6)\n"
"v_add_f32 v36, v36, v44\n"
"v_add_f32 v37, v37, v45\n"
"v_add_f32 v38, v38, v46\n"
"v_add_f32 v39, v39, v47\n"
"s_waitcnt lgkmcnt(5)\n"
"v_add_f32 v32, v32, v48\n"
"v_add_f32 v33, v33, v49\n"
"v_add_f32 v34, v34, v50\n"
"v_add_f32 v35, v35, v51\n"
"s_waitcnt lgkmcnt(4)\n"
"v_add_f32 v36, v36, v52\n"
"v_add_f32 v37, v37, v53\n"
"v_add_f32 v38, v38, v54\n"
"v_add_f32 v39, v39, v55\n"
"s_waitcnt lgkmcnt(3)\n"
"v_add_f32 v32, v32, v64\n"
"v_add_f32 v33, v33, v65\n"
"v_add_f32 v34, v34, v66\n"
"v_add_f32 v35, v35, v67\n"
"s_waitcnt lgkmcnt(2)\n"
"v_add_f32 v36, v36, v68\n"
"v_add_f32 v37, v37, v69\n"
"v_add_f32 v38, v38, v70\n"
"v_add_f32 v39, v39, v71\n"
"s_waitcnt lgkmcnt(1)\n"
"v_add_f32 v104, v104, v56\n"
"v_add_f32 v105, v105, v57\n"
"v_add_f32 v106, v106, v58\n"
"v_add_f32 v107, v107, v59\n"
"s_waitcnt lgkmcnt(0)\n"
"v_add_f32 v108, v108, v60\n"
"v_add_f32 v109, v109, v61\n"
"v_add_f32 v110, v110, v62\n"
"v_add_f32 v111, v111, v63\n"
"v_sub_f32 v104, v104, v32\n"
"v_sub_f32 v105, v105, v33\n"
"v_sub_f32 v106, v106, v34\n"
"v_sub_f32 v107, v107, v35\n"
"v_sub_f32 v108, v108, v36\n"
"v_sub_f32 v109, v109, v37\n"
"v_sub_f32 v110, v110, v38\n"
"v_sub_f32 v111, v111, v39\n"
"v_fma_f32 v104, v72, v104, v32\n"
"v_fma_f32 v105, v72, v105, v33\n"
"v_fma_f32 v106, v72, v106, v34\n"
"v_fma_f32 v107, v72, v107, v35\n"
"v_fma_f32 v108, v72, v108, v36\n"
"v_fma_f32 v109, v72, v109, v37\n"
"v_fma_f32 v110, v72, v110, v38\n"
"v_fma_f32 v111, v72, v111, v39\n"
"v_cvt_pk_bf16_f32 v104, v104, v105\n"
"v_cvt_pk_bf16_f32 v105, v106, v107\n"
"v_cvt_pk_bf16_f32 v106, v108, v109\n"
"v_cvt_pk_bf16_f32 v107, v110, v111\n"
"s_nop 0\n"
"v_permlane32_swap_b32 v104, v106\n"
"v_permlane32_swap_b32 v105, v107\n"
"buffer_store_dwordx4 v[104:107], v12, s[36:39], s72 offen offset:32\n"
"s_endpgm\n"
"LPAD7:\n"
"s_cbranch_execz LBKA\n"
"LTCA:\n"
"LCA_slow_1:\n"
"s_mov_b32 s50, 131072\n"
"LCA_slow_1l:\n"
"buffer_load_dwordx2 v[8:9], v4, s[32:35], 0 offen sc1\n"
"s_waitcnt vmcnt(0)\n"
"v_cmp_eq_u64 vcc, s[40:41], v[8:9]\n"
"s_nop 1\n"
"s_and_b32 s49, vcc_lo, 0xf\n"
"s_cmp_eq_u32 s49, 0xf\n"
"s_cbranch_scc1 LCA_back_2\n"
"s_sleep 1\n"
"s_sub_u32 s50, s50, 1\n"
"s_cmp_lg_u32 s50, 0\n"
"s_cbranch_scc1 LCA_slow_1l\n"
"s_branch LCA_back_2\n"
"LBHB:\n"
"s_waitcnt vmcnt(8)\n"
"s_barrier\n"
"s_add_u32 s74, s45, 2\n"
"s_and_b32 s74, s74, 7\n"
"s_lshl_b32 s74, s74, 7\n"
"s_add_u32 s74, s74, s57\n"
"s_add_u32 m0, s59, 8192\n"
"s_nop 0\n"
"buffer_load_dwordx4 v16, s[8:11], s74 offen lds\n"
"buffer_load_dwordx4 v17, s[8:11], s74 offen offset:1024 lds\n"
"buffer_load_dwordx4 v18, s[8:11], s74 offen offset:2048 lds\n"
"s_nop 1\n"
"buffer_load_dwordx4 v19, s[8:11], s74 offen offset:3072 lds\n"
"s_sleep 1\n"
"s_add_u32 s74, s45, 2\n"
"s_and_b32 s74, s74, 7\n"
"s_mul_i32 s74, s74, 229376\n"
"s_add_u32 s74, s74, s58\n"
"s_add_u32 m0, s60, 32768\n"
"s_nop 0\n"
"buffer_load_dwordx4 v1, s[16:19], s74 offen lds\n"
"s_nop 4\n"
"buffer_load_dwordx4 v2, s[16:19], s74 offen offset:1024 lds\n"
"s_sleep 1\n"
"s_add_u32 s74, s45, 2\n"
"s_and_b32 s74, s74, 7\n"
"s_mul_i32 s74, s74, 229376\n"
"s_add_u32 s74, s74, s58\n"
"s_add_u32 m0, s60, 40960\n"
"s_nop 0\n"
"buffer_load_dwordx4 v1, s[20:23], s74 offen lds\n"
"s_nop 4\n"
"buffer_load_dwordx4 v2, s[20:23], s74 offen offset:1024 lds\n"
"s_sleep 1\n"
"s_lshl_b32 s74, s2, 3\n"
"s_add_u32 s74, s74, 679936\n"
"s_cmp_eq_u32 s42, 0\n"
"s_cselect_b64 exec, 1, 0\n"
"buffer_store_dwordx2 v[14:15], off, s[32:35], s74 sc1\n"
"s_mov_b64 exec, -1\n"
"s_waitcnt vmcnt(9)\n"
"s_barrier\n"
"s_add_u32 s74, s45, 3\n"
"s_and_b32 s74, s74, 7\n"
"s_lshl_b32 s74, s74, 7\n"
"s_add_u32 s74, s74, s57\n"
"s_add_u32 m0, s59, 12288\n"
"s_nop 0\n"
"buffer_load_dwordx4 v16, s[8:11], s74 offen lds\n"
"buffer_load_dwordx4 v17, s[8:11], s74 offen offset:1024 lds\n"
"buffer_load_dwordx4 v18, s[8:11], s74 offen offset:2048 lds\n"
"s_nop 5\n"
"buffer_load_dwordx4 v19, s[8:11], s74 offen offset:3072 lds\n"
"s_sleep 1\n"
"s_add_u32 s74, s45, 3\n"
"s_and_b32 s74, s74, 7\n"
"s_mul_i32 s74, s74, 229376\n"
"s_add_u32 s74, s74, s58\n"
"s_add_u32 m0, s60, 49152\n"
"s_nop 0\n"
"buffer_load_dwordx4 v1, s[16:19], s74 offen lds\n"
"s_nop 4\n"
"buffer_load_dwordx4 v2, s[16:19], s74 offen offset:1024 lds\n"
"s_sleep 1\n"
"s_add_u32 s74, s45, 3\n"
"s_and_b32 s74, s74, 7\n"
"s_mul_i32 s74, s74, 229376\n"
"s_add_u32 s74, s74, s58\n"
"s_add_u32 m0, s60, 57344\n"
"s_nop 0\n"
"buffer_load_dwordx4 v1, s[20:23], s74 offen lds\n"
"s_nop 4\n"
"buffer_load_dwordx4 v2, s[20:23], s74 offen offset:1024 lds\n"
"s_sleep 1\n"
"s_add_u32 s74, s45, 4\n"
"s_and_b32 s74, s74, 7\n"
"s_lshl_b32 s74, s74, 7\n"
"s_add_u32 s74, s74, s57\n"
"s_add_u32 m0, s59, 0\n"
"s_nop 0\n"
"buffer_load_dwordx4 v16, s[8:11], s74 offen lds\n"
"s_nop 4\n"
"buffer_load_dwordx4 v17, s[8:11], s74 offen offset:1024 lds\n"
"s_sleep 1\n"
"buffer_load_dwordx4 v18, s[8:11], s74 offen offset:2048 lds\n"
"s_nop 10\n"
"buffer_load_dwordx4 v19, s[8:11], s74 offen offset:3072 lds\n"
"s_sleep 1\n"
"s_add_u32 s74, s45, 4\n"
"s_and_b32 s74, s74, 7\n"
"s_mul_i32 s74, s74, 229376\n"
"s_add_u32 s74, s74, s58\n"
"s_add_u32 m0, s60, 65536\n"
"s_nop 0\n"
"buffer_load_dwordx4 v1, s[16:19], s74 offen lds\n"
"s_nop 4\n"
"buffer_load_dwordx4 v2, s[16:19], s74 offen offset:1024 lds\n"
"s_sleep 1\n"
"s_add_u32 s74, s45, 4\n"
"s_and_b32 s74, s74, 7\n"
"s_mul_i32 s74, s74, 229376\n"
"s_add_u32 s74, s74, s58\n"
"s_add_u32 m0, s60, 73728\n"
"s_nop 0\n"
"buffer_load_dwordx4 v1, s[20:23], s74 offen lds\n"
"s_nop 4\n"
"buffer_load_dwordx4 v2, s[20:23], s74 offen offset:1024 lds\n"
"s_waitcnt vmcnt(17)\n"
"s_barrier\n"
"s_sleep 1\n"
"s_add_u32 s74, s45, 5\n"
"s_and_b32 s74, s74, 7\n"
"s_lshl_b32 s74, s74, 7\n"
"s_add_u32 s74, s74, s57\n"
"s_add_u32 m0, s59, 4096\n"
"s_nop 0\n"
"buffer_load_dwordx4 v16, s[8:11], s74 offen lds\n"
"s_nop 4\n"
"buffer_load_dwordx4 v17, s[8:11], s74 offen offset:1024 lds\n"
"s_sleep 1\n"
"buffer_load_dwordx4 v18, s[8:11], s74 offen offset:2048 lds\n"
"s_nop 10\n"
"buffer_load_dwordx4 v19, s[8:11], s74 offen offset:3072 lds\n"
"s_sleep 1\n"
"s_add_u32 s74, s45, 5\n"
"s_and_b32 s74, s74, 7\n"
"s_mul_i32 s74, s74, 229376\n"
"s_add_u32 s74, s74, s58\n"
"s_add_u32 m0, s60, 0\n"
"s_nop 0\n"
"buffer_load_dwordx4 v1, s[16:19], s74 offen lds\n"
"s_nop 4\n"
"buffer_load_dwordx4 v2, s[16:19], s74 offen offset:1024 lds\n"
"s_sleep 1\n"
"s_add_u32 s74, s45, 5\n"
"s_and_b32 s74, s74, 7\n"
"s_mul_i32 s74, s74, 229376\n"
"s_add_u32 s74, s74, s58\n"
"s_add_u32 m0, s60, 8192\n"
"s_nop 0\n"
"buffer_load_dwordx4 v1, s[20:23], s74 offen lds\n"
"s_nop 4\n"
"buffer_load_dwordx4 v2, s[20:23], s74 offen offset:1024 lds\n"
"s_waitcnt vmcnt(16)\n"
"s_barrier\n"
"s_nop 1\n"
"s_add_u32 s74, s45, 6\n"
"s_and_b32 s74, s74, 7\n"
"s_lshl_b32 s74, s74, 7\n"
"s_add_u32 s74, s74, s57\n"
"s_add_u32 m0, s59, 8192\n"
"s_nop 0\n"
"buffer_load_dwordx4 v16, s[8:11], s74 offen lds\n"
"s_nop 7\n"
"buffer_load_dwordx4 v17, s[8:11], s74 offen offset:1024 lds\n"
"s_sleep 1\n"
"buffer_load_dwordx4 v18, s[8:11], s74 offen offset:2048 lds\n"
"s_nop 10\n"
"buffer_load_dwordx4 v19, s[8:11], s74 offen offset:3072 lds\n"
"s_sleep 1\n"
"s_add_u32 s74, s45, 6\n"
"s_and_b32 s74, s74, 7\n"
"s_mul_i32 s74, s74, 229376\n"
"s_add_u32 s74, s74, s58\n"
"s_add_u32 m0, s60, 16384\n"
"s_nop 0\n"
"buffer_load_dwordx4 v1, s[16:19], s74 offen lds\n"
"s_nop 4\n"
"buffer_load_dwordx4 v2, s[16:19], s74 offen offset:1024 lds\n"
"s_sleep 1\n"
"s_add_u32 s74, s45, 6\n"
"s_and_b32 s74, s74, 7\n"
"s_mul_i32 s74, s74, 229376\n"
"s_add_u32 s74, s74, s58\n"
"s_add_u32 m0, s60, 24576\n"
"s_nop 0\n"
"buffer_load_dwordx4 v1, s[20:23], s74 offen lds\n"
"s_nop 4\n"
"buffer_load_dwordx4 v2, s[20:23], s74 offen offset:1024 lds\n"
"s_waitcnt vmcnt(16)\n"
"s_barrier\n"
"s_add_u32 s74, s45, 7\n"
"s_and_b32 s74, s74, 7\n"
"s_lshl_b32 s74, s74, 7\n"
"s_add_u32 s74, s74, s57\n"
"s_add_u32 m0, s59, 12288\n"
"s_nop 0\n"
"buffer_load_dwordx4 v16, s[8:11], s74 offen lds\n"
"buffer_load_dwordx4 v17, s[8:11], s74 offen offset:1024 lds\n"
"s_nop 11\n"
"buffer_load_dwordx4 v18, s[8:11], s74 offen offset:2048 lds\n"
"s_sleep 1\n"
"buffer_load_dwordx4 v19, s[8:11], s74 offen offset:3072 lds\n"
"s_nop 10\n"
"s_add_u32 s74, s45, 7\n"
"s_and_b32 s74, s74, 7\n"
"s_mul_i32 s74, s74, 229376\n"
"s_add_u32 s74, s74, s58\n"
"s_add_u32 m0, s60, 32768\n"
"s_nop 0\n"
"buffer_load_dwordx4 v1, s[16:19], s74 offen lds\n"
"s_nop 7\n"
"buffer_load_dwordx4 v2, s[16:19], s74 offen offset:1024 lds\n"
"s_sleep 1\n"
"s_add_u32 s74, s45, 7\n"
"s_and_b32 s74, s74, 7\n"
"s_mul_i32 s74, s74, 229376\n"
"s_add_u32 s74, s74, s58\n"
"s_add_u32 m0, s60, 40960\n"
"s_nop 0\n"
"buffer_load_dwordx4 v1, s[20:23], s74 offen lds\n"
"s_nop 4\n"
"buffer_load_dwordx4 v2, s[20:23], s74 offen offset:1024 lds\n"
"s_waitcnt vmcnt(16)\n"
"s_barrier\n"
"s_add_u32 s74, s45, 0\n"
"s_and_b32 s74, s74, 7\n"
"s_mul_i32 s74, s74, 229376\n"
"s_add_u32 s74, s74, s58\n"
"s_add_u32 s74, s74, 1024\n"
"s_add_u32 m0, s60, 49152\n"
"s_nop 0\n"
"buffer_load_dwordx4 v1, s[16:19], s74 offen lds\n"
"buffer_load_dwordx4 v2, s[16:19], s74 offen offset:1024 lds\n"
"s_add_u32 s74, s45, 0\n"
"s_and_b32 s74, s74, 7\n"
"s_mul_i32 s74, s74, 229376\n"
"s_add_u32 s74, s74, s58\n"
"s_add_u32 s74, s74, 1024\n"
"s_add_u32 m0, s60, 57344\n"
"s_nop 0\n"
"buffer_load_dwordx4 v1, s[20:23], s74 offen lds\n"
"s_nop 5\n"
"buffer_load_dwordx4 v2, s[20:23], s74 offen offset:1024 lds\n"
"s_waitcnt vmcnt(12)\n"
"s_barrier\n"
"s_add_u32 s74, s45, 1\n"
"s_and_b32 s74, s74, 7\n"
"s_mul_i32 s74, s74, 229376\n"
"s_add_u32 s74, s74, s58\n"
"s_add_u32 s74, s74, 1024\n"
"LPAD8:\n"
"s_cbranch_execz LPAD9\n"
"s_add_u32 m0, s60, 65536\n"
"s_nop 0\n"
"buffer_load_dwordx4 v1, s[16:19], s74 offen lds\n"
"buffer_load_dwordx4 v2, s[16:19], s74 offen offset:1024 lds\n"
"s_add_u32 s74, s45, 1\n"
"s_and_b32 s74, s74, 7\n"
"s_mul_i32 s74, s74, 229376\n"
"s_add_u32 s74, s74, s58\n"
"s_add_u32 s74, s74, 1024\n"
"s_add_u32 m0, s60, 73728\n"
"s_nop 0\n"
"buffer_load_dwordx4 v1, s[20:23], s74 offen lds\n"
"buffer_load_dwordx4 v2, s[20:23], s74 offen offset:1024 lds\n"
"s_nop 8\n"
"s_lshl_b32 s74, s2, 2\n"
"s_add_u32 s74, s74, s42\n"
"s_cmp_eq_u32 s74, 256\n"
"s_cselect_b64 s[72:73], -1, 0\n"
"v_lshlrev_b32 v40, 5, v0\n"
"v_add_u32 v40, 679936, v40\n"
"s_mov_b64 exec, s[72:73]\n"
"buffer_load_dwordx4 v[32:35], v40, s[32:35], 0 offen sc1\n"
"buffer_load_dwordx4 v[36:39], v40, s[32:35], 0 offen offset:16 sc1\n"
"s_mov_b64 exec, -1\n"
"s_waitcnt vmcnt(10)\n"
"s_barrier\n"
"s_add_u32 s74, s45, 2\n"
"s_and_b32 s74, s74, 7\n"
"s_mul_i32 s74, s74, 229376\n"
"s_add_u32 s74, s74, s58\n"
"s_add_u32 s74, s74, 1024\n"
"s_add_u32 m0, s60, 0\n"
"s_nop 0\n"
"buffer_load_dwordx4 v1, s[16:19], s74 offen lds\n"
"buffer_load_dwordx4 v2, s[16:19], s74 offen offset:1024 lds\n"
"s_add_u32 s74, s45, 2\n"
"s_and_b32 s74, s74, 7\n"
"s_mul_i32 s74, s74, 229376\n"
"s_add_u32 s74, s74, s58\n"
"s_add_u32 s74, s74, 1024\n"
"s_add_u32 m0, s60, 8192\n"
"s_nop 0\n"
"buffer_load_dwordx4 v1, s[20:23], s74 offen lds\n"
"buffer_load_dwordx4 v2, s[20:23], s74 offen offset:1024 lds\n"
"s_nop 8\n"
"s_add_u32 s74, s66, 0\n"
"s_add_u32 m0, s59, 16384\n"
"s_nop 0\n"
"buffer_load_dwordx4 v5, s[24:27], s74 offen lds\n"
"s_nop 10\n"
"buffer_load_dwordx4 v6, s[24:27], s74 offen offset:1024 lds\n"
"s_sleep 1\n"
"s_add_u32 s74, s66, 65536\n"
"s_add_u32 m0, s59, 6144\n"
"s_nop 0\n"
"buffer_load_dwordx4 v5, s[24:27], s74 offen lds\n"
"s_nop 7\n"
"buffer_load_dwordx4 v6, s[24:27], s74 offen offset:1024 lds\n"
"s_sleep 1\n"
"s_add_u32 s74, s67, 0\n"
"s_add_u32 m0, s59, 4096\n"
"s_nop 0\n"
"buffer_load_dwordx4 v5, s[24:27], s74 offen lds\n"
"s_nop 7\n"
"buffer_load_dwordx4 v6, s[24:27], s74 offen offset:1024 lds\n"
"s_waitcnt vmcnt(16)\n"
"s_barrier\n"
"s_add_u32 s74, s45, 3\n"
"s_and_b32 s74, s74, 7\n"
"s_mul_i32 s74, s74, 229376\n"
"s_add_u32 s74, s74, s58\n"
"s_add_u32 s74, s74, 1024\n"
"s_add_u32 m0, s60, 16384\n"
"s_nop 0\n"
"buffer_load_dwordx4 v1, s[16:19], s74 offen lds\n"
"buffer_load_dwordx4 v2, s[16:19], s74 offen offset:1024 lds\n"
"s_nop 7\n"
"s_add_u32 s74, s45, 3\n"
"s_and_b32 s74, s74, 7\n"
"s_mul_i32 s74, s74, 229376\n"
"s_add_u32 s74, s74, s58\n"
"s_add_u32 s74, s74, 1024\n"
"s_add_u32 m0, s60, 24576\n"
"s_nop 0\n"
"buffer_load_dwordx4 v1, s[20:23], s74 offen lds\n"
"s_nop 6\n"
"buffer_load_dwordx4 v2, s[20:23], s74 offen offset:1024 lds\n"
"s_sleep 1\n"
"s_add_u32 s74, s67, 65536\n"
"s_add_u32 m0, s59, 12288\n"
"s_nop 0\n"
"buffer_load_dwordx4 v5, s[24:27], s74 offen lds\n"
"s_nop 7\n"
"buffer_load_dwordx4 v6, s[24:27], s74 offen offset:1024 lds\n"
"s_sleep 1\n"
"s_add_u32 s74, s68, 0\n"
"s_add_u32 m0, s59, 14336\n"
"s_nop 0\n"
"buffer_load_dwordx4 v5, s[24:27], s74 offen lds\n"
"s_nop 7\n"
"buffer_load_dwordx4 v6, s[24:27], s74 offen offset:1024 lds\n"
"s_sleep 1\n"
"s_add_u32 s74, s68, 65536\n"
"s_add_u32 m0, s59, 2048\n"
"s_nop 0\n"
"buffer_load_dwordx4 v5, s[24:27], s74 offen lds\n"
"s_nop 7\n"
"buffer_load_dwordx4 v6, s[24:27], s74 offen offset:1024 lds\n"
"s_waitcnt vmcnt(20)\n"
"s_cmp_lg_u64 s[72:73], 0\n"
"s_cbranch_scc1 LHB_eb_1\n"
"LHB_ebk_2:\n"
"s_barrier\n"
"v_lshlrev_b32 v28, 4, v0\n"
"v_add_u32 v28, s59, v28\n"
"ds_read_b128 v[16:19], v28 offset:8192\n"
"ds_read_b128 v[20:23], v28 offset:9216\n"
"ds_read_b128 v[24:27], v28 offset:10240\n"
"ds_read_b128 v[28:31], v28 offset:11264\n"
"s_add_u32 s74, s45, 4\n"
"s_and_b32 s74, s74, 7\n"
"s_mul_i32 s74, s74, 229376\n"
"s_add_u32 s74, s74, s58\n"
"s_add_u32 s74, s74, 1024\n"
"s_add_u32 m0, s60, 32768\n"
"s_nop 0\n"
"buffer_load_dwordx4 v1, s[16:19], s74 offen lds\n"
"s_nop 4\n"
"buffer_load_dwordx4 v2, s[16:19], s74 offen offset:1024 lds\n"
"s_sleep 1\n"
"s_add_u32 s74, s45, 4\n"
"s_and_b32 s74, s74, 7\n"
"s_mul_i32 s74, s74, 229376\n"
"s_add_u32 s74, s74, s58\n"
"s_add_u32 s74, s74, 1024\n"
"s_add_u32 m0, s60, 40960\n"
"s_nop 0\n"
"buffer_load_dwordx4 v1, s[20:23], s74 offen lds\n"
"s_nop 3\n"
"buffer_load_dwordx4 v2, s[20:23], s74 offen offset:1024 lds\n"
"s_sleep 1\n"
"s_add_u32 s74, s70, 0\n"
"s_waitcnt lgkmcnt(3)\n"
"buffer_store_dwordx4 v[16:19], v9, s[32:35], s74 offen sc1\n"
"s_nop 8\n"
"s_waitcnt lgkmcnt(2)\n"
"buffer_store_dwordx4 v[20:23], v9, s[32:35], s74 offen offset:1024 sc1\n"
"s_nop 12\n"
"s_waitcnt lgkmcnt(1)\n"
"buffer_store_dwordx4 v[24:27], v9, s[32:35], s74 offen offset:2048 sc1\n"
"s_nop 12\n"
"s_waitcnt lgkmcnt(0)\n"
"buffer_store_dwordx4 v[28:31], v9, s[32:35], s74 offen offset:3072 sc1\n"
"s_waitcnt vmcnt(24)\n"
"s_barrier\n"
"s_add_u32 s74, s45, 5\n"
"s_and_b32 s74, s74, 7\n"
"s_mul_i32 s74, s74, 229376\n"
"s_add_u32 s74, s74, s58\n"
"s_add_u32 s74, s74, 1024\n"
"s_add_u32 m0, s60, 49152\n"
"s_nop 0\n"
"buffer_load_dwordx4 v1, s[16:19], s74 offen lds\n"
"s_nop 1\n"
"buffer_load_dwordx4 v2, s[16:19], s74 offen offset:1024 lds\n"
"s_sleep 1\n"
"s_add_u32 s74, s45, 5\n"
"s_and_b32 s74, s74, 7\n"
"s_mul_i32 s74, s74, 229376\n"
"s_add_u32 s74, s74, s58\n"
"s_add_u32 s74, s74, 1024\n"
"s_add_u32 m0, s60, 57344\n"
"s_nop 0\n"
"buffer_load_dwordx4 v1, s[20:23], s74 offen lds\n"
"s_nop 3\n"
"buffer_load_dwordx4 v2, s[20:23], s74 offen offset:1024 lds\n"
"s_waitcnt vmcnt(26)\n"
"ds_read_b64_tr_b16 v[80:81], v7 offset:16384\n"
"ds_read_b64_tr_b16 v[82:83], v7 offset:17408\n"
"ds_read_b64_tr_b16 v[84:85], v8 offset:16384\n"
"ds_read_b64_tr_b16 v[86:87], v8 offset:17408\n"
"s_nop 6\n"
"s_add_u32 s74, s69, 0\n"
"s_add_u32 m0, s59, 0\n"
"s_nop 0\n"
"buffer_load_dwordx4 v5, s[24:27], s74 offen lds\n"
"s_nop 10\n"
"buffer_load_dwordx4 v6, s[24:27], s74 offen offset:1024 lds\n"
"s_waitcnt vmcnt(26)\n"
"ds_read_b64_tr_b16 v[88:89], v7 offset:6144\n"
"ds_read_b64_tr_b16 v[90:91], v7 offset:7168\n"
"ds_read_b64_tr_b16 v[92:93], v8 offset:6144\n"
"ds_read_b64_tr_b16 v[94:95], v8 offset:7168\n"
"s_nop 6\n"
"s_waitcnt lgkmcnt(4)\n"
"s_add_u32 s74, s69, 65536\n"
"s_add_u32 m0, s59, 16384\n"
"s_nop 0\n"
"buffer_load_dwordx4 v5, s[24:27], s74 offen lds\n"
"s_nop 9\n"
"buffer_load_dwordx4 v6, s[24:27], s74 offen offset:1024 lds\n"
"s_waitcnt vmcnt(22)\n"
"s_barrier\n"
"s_add_u32 s74, s45, 6\n"
"s_and_b32 s74, s74, 7\n"
"s_mul_i32 s74, s74, 229376\n"
"s_add_u32 s74, s74, s58\n"
"s_add_u32 s74, s74, 1024\n"
"s_add_u32 m0, s60, 65536\n"
"s_nop 0\n"
"buffer_load_dwordx4 v1, s[16:19], s74 offen lds\n"
"buffer_load_dwordx4 v2, s[16:19], s74 offen offset:1024 lds\n"
"s_nop 3\n"
"s_add_u32 s74, s45, 6\n"
"s_and_b32 s74, s74, 7\n"
"s_mul_i32 s74, s74, 229376\n"
"s_add_u32 s74, s74, s58\n"
"s_add_u32 s74, s74, 1024\n"
"s_add_u32 m0, s60, 73728\n"
"s_nop 0\n"
"buffer_load_dwordx4 v1, s[20:23], s74 offen lds\n"
"s_nop 6\n"
"buffer_load_dwordx4 v2, s[20:23], s74 offen offset:1024 lds\n"
"ds_read_b64_tr_b16 v[96:97], v7 offset:4096\n"
"ds_read_b64_tr_b16 v[98:99], v7 offset:5120\n"
"ds_read_b64_tr_b16 v[100:101], v8 offset:4096\n"
"ds_read_b64_tr_b16 v[102:103], v8 offset:5120\n"
"s_nop 7\n"
"s_waitcnt lgkmcnt(4)\n"
"s_add_u32 s74, s66, 524288\n"
"s_add_u32 m0, s59, 6144\n"
"s_nop 0\n"
"buffer_load_dwordx4 v5, s[24:27], s74 offen lds\n"
"s_nop 9\n"
"buffer_load_dwordx4 v6, s[24:27], s74 offen offset:1024 lds\n"
"s_waitcnt vmcnt(26)\n"
"ds_read_b64_tr_b16 v[104:105], v7 offset:12288\n"
"ds_read_b64_tr_b16 v[106:107], v7 offset:13312\n"
"ds_read_b64_tr_b16 v[108:109], v8 offset:12288\n"
"ds_read_b64_tr_b16 v[110:111], v8 offset:13312\n"
"s_nop 6\n"
"s_waitcnt lgkmcnt(4)\n"
"s_add_u32 s74, s66, 589824\n"
"s_add_u32 m0, s59, 4096\n"
"s_nop 0\n"
"buffer_load_dwordx4 v5, s[24:27], s74 offen lds\n"
"s_nop 9\n"
"buffer_load_dwordx4 v6, s[24:27], s74 offen offset:1024 lds\n"
"s_waitcnt vmcnt(26)\n"
"ds_read_b64_tr_b16 v[112:113], v7 offset:14336\n"
"ds_read_b64_tr_b16 v[114:115], v7 offset:15360\n"
"ds_read_b64_tr_b16 v[116:117], v8 offset:14336\n"
"ds_read_b64_tr_b16 v[118:119], v8 offset:15360\n"
"s_nop 6\n"
"s_waitcnt lgkmcnt(4)\n"
"s_add_u32 s74, s67, 524288\n"
"s_add_u32 m0, s59, 12288\n"
"s_nop 0\n"
"buffer_load_dwordx4 v5, s[24:27], s74 offen lds\n"
"s_nop 9\n"
"buffer_load_dwordx4 v6, s[24:27], s74 offen offset:1024 lds\n"
"s_waitcnt vmcnt(22)\n"
"s_barrier\n"
"s_nop 2\n"
"s_add_u32 s74, s45, 7\n"
"s_and_b32 s74, s74, 7\n"
"s_mul_i32 s74, s74, 229376\n"
"s_add_u32 s74, s74, s58\n"
"s_add_u32 s74, s74, 1024\n"
"s_add_u32 m0, s60, 0\n"
"s_nop 0\n"
"buffer_load_dwordx4 v1, s[16:19], s74 offen lds\n"
"s_nop 6\n"
"buffer_load_dwordx4 v2, s[16:19], s74 offen offset:1024 lds\n"
"s_sleep 1\n"
"s_add_u32 s74, s45, 7\n"
"s_and_b32 s74, s74, 7\n"
"s_mul_i32 s74, s74, 229376\n"
"s_add_u32 s74, s74, s58\n"
"s_add_u32 s74, s74, 1024\n"
"s_add_u32 m0, s60, 8192\n"
"s_nop 0\n"
"buffer_load_dwordx4 v1, s[20:23], s74 offen lds\n"
"s_nop 3\n"
"buffer_load_dwordx4 v2, s[20:23], s74 offen offset:1024 lds\n"
"ds_read_b64_tr_b16 v[120:121], v7 offset:2048\n"
"ds_read_b64_tr_b16 v[122:123], v7 offset:3072\n"
"ds_read_b64_tr_b16 v[124:125], v8 offset:2048\n"
"ds_read_b64_tr_b16 v[126:127], v8 offset:3072\n"
"s_nop 7\n"
"s_waitcnt lgkmcnt(4)\n"
"s_add_u32 s74, s67, 589824\n"
"s_add_u32 m0, s59, 14336\n"
"s_nop 0\n"
"buffer_load_dwordx4 v5, s[24:27], s74 offen lds\n"
"s_nop 9\n"
"buffer_load_dwordx4 v6, s[24:27], s74 offen offset:1024 lds\n"
"s_waitcnt vmcnt(18)\n"
"ds_read_b64_tr_b16 v[128:129], v7 offset:0\n"
"ds_read_b64_tr_b16 v[130:131], v7 offset:1024\n"
"ds_read_b64_tr_b16 v[132:133], v8 offset:0\n"
"ds_read_b64_tr_b16 v[134:135], v8 offset:1024\n"
"s_nop 6\n"
"s_waitcnt lgkmcnt(4)\n"
"s_add_u32 s74, s68, 524288\n"
"s_add_u32 m0, s59, 2048\n"
"s_nop 0\n"
"buffer_load_dwordx4 v5, s[24:27], s74 offen lds\n"
"s_nop 9\n"
"buffer_load_dwordx4 v6, s[24:27], s74 offen offset:1024 lds\n"
"s_lshl_b32 s49, s43, 2\n"
"s_add_u32 s49, s49, s42\n"
"s_lshr_b32 s49, s49, 1\n"
"s_lshl_b32 s49, s49, 9\n"
"s_lshl_b32 s50, s44, 4\n"
"s_add_u32 s49, s49, s50\n"
"s_and_b32 s50, s42, 1\n"
"s_lshl_b32 s50, s50, 3\n"
"s_add_u32 s49, s49, s50\n"
"s_add_u32 s49, s49, 655360\n"
"s_mov_b64 exec, 1\n"
"buffer_store_dwordx2 v[14:15], off, s[32:35], s49 sc1\n"
"s_mov_b64 exec, -1\n"
"s_barrier\n"
"s_nop 6\n"
"s_lshl_b32 s74, s44, 6\n"
"s_sub_u32 s58, s58, s74\n"
"s_add_u32 s58, s58, 3072\n"
"s_add_u32 s74, s45, 0\n"
"s_and_b32 s74, s74, 7\n"
"s_mul_i32 s74, s74, 229376\n"
"s_add_u32 s74, s74, s58\n"
"s_add_u32 m0, s61, 16384\n"
"s_nop 0\n"
"buffer_load_dwordx4 v3, s[16:19], s74 offen lds\n"
"s_nop 4\n"
"s_add_u32 s74, s45, 1\n"
"s_and_b32 s74, s74, 7\n"
"s_mul_i32 s74, s74, 229376\n"
"s_add_u32 s74, s74, s58\n"
"s_add_u32 m0, s61, 20480\n"
"s_nop 0\n"
"buffer_load_dwordx4 v3, s[16:19], s74 offen lds\n"
"s_nop 7\n"
"s_add_u32 s74, s45, 0\n"
"s_and_b32 s74, s74, 7\n"
"s_mul_i32 s74, s74, 229376\n"
"s_add_u32 s74, s74, s58\n"
"s_add_u32 m0, s61, 24576\n"
"s_nop 0\n"
"buffer_load_dwordx4 v3, s[20:23], s74 offen lds\n"
"s_nop 7\n"
"s_add_u32 s74, s45, 1\n"
"s_and_b32 s74, s74, 7\n"
"s_mul_i32 s74, s74, 229376\n"
"s_add_u32 s74, s74, s58\n"
"s_add_u32 m0, s61, 28672\n"
"s_nop 0\n"
"buffer_load_dwordx4 v3, s[20:23], s74 offen lds\n"
"s_waitcnt vmcnt(23)\n"
"ds_read_b64_tr_b16 v[136:137], v7 offset:16384\n"
"ds_read_b64_tr_b16 v[138:139], v7 offset:17408\n"
"ds_read_b64_tr_b16 v[140:141], v8 offset:16384\n"
"ds_read_b64_tr_b16 v[142:143], v8 offset:17408\n"
"s_waitcnt lgkmcnt(4)\n"
"s_add_u32 s74, s68, 589824\n"
"s_add_u32 m0, s59, 0\n"
"s_nop 0\n"
"buffer_load_dwordx4 v5, s[24:27], s74 offen lds\n"
"s_nop 9\n"
"buffer_load_dwordx4 v6, s[24:27], s74 offen offset:1024 lds\n"
"s_waitcnt vmcnt(19)\n"
"ds_read_b64_tr_b16 v[144:145], v7 offset:6144\n"
"ds_read_b64_tr_b16 v[146:147], v7 offset:7168\n"
"ds_read_b64_tr_b16 v[148:149], v8 offset:6144\n"
"ds_read_b64_tr_b16 v[150:151], v8 offset:7168\n"
"s_nop 6\n"
"s_waitcnt lgkmcnt(4)\n"
"s_add_u32 s74, s69, 524288\n"
"s_add_u32 m0, s59, 16384\n"
"s_nop 0\n"
"buffer_load_dwordx4 v5, s[24:27], s74 offen lds\n"
"s_nop 9\n"
"buffer_load_dwordx4 v6, s[24:27], s74 offen offset:1024 lds\n"
"s_barrier\n"
"s_add_u32 s74, s45, 2\n"
"s_and_b32 s74, s74, 7\n"
"s_mul_i32 s74, s74, 229376\n"
"s_add_u32 s74, s74, s58\n"
"s_add_u32 m0, s61, 32768\n"
"s_nop 0\n"
"buffer_load_dwordx4 v3, s[16:19], s74 offen lds\n"
"s_nop 2\n"
"s_add_u32 s74, s45, 3\n"
"s_and_b32 s74, s74, 7\n"
"s_mul_i32 s74, s74, 229376\n"
"s_add_u32 s74, s74, s58\n"
"s_add_u32 m0, s61, 36864\n"
"s_nop 0\n"
"buffer_load_dwordx4 v3, s[16:19], s74 offen lds\n"
"s_nop 7\n"
"s_add_u32 s74, s45, 2\n"
"s_and_b32 s74, s74, 7\n"
"s_mul_i32 s74, s74, 229376\n"
"s_add_u32 s74, s74, s58\n"
"s_add_u32 m0, s61, 40960\n"
"s_nop 0\n"
"buffer_load_dwordx4 v3, s[20:23], s74 offen lds\n"
"s_nop 7\n"
"s_add_u32 s74, s45, 3\n"
"s_and_b32 s74, s74, 7\n"
"s_mul_i32 s74, s74, 229376\n"
"s_add_u32 s74, s74, s58\n"
"s_add_u32 m0, s61, 45056\n"
"s_nop 0\n"
"buffer_load_dwordx4 v3, s[20:23], s74 offen lds\n"
"s_waitcnt vmcnt(23)\n"
"ds_read_b64_tr_b16 v[152:153], v7 offset:4096\n"
"ds_read_b64_tr_b16 v[154:155], v7 offset:5120\n"
"ds_read_b64_tr_b16 v[156:157], v8 offset:4096\n"
"ds_read_b64_tr_b16 v[158:159], v8 offset:5120\n"
"s_waitcnt lgkmcnt(4)\n"
"s_add_u32 s74, s69, 589824\n"
"s_add_u32 m0, s59, 6144\n"
"s_nop 0\n"
"buffer_load_dwordx4 v5, s[24:27], s74 offen lds\n"
"s_nop 9\n"
"buffer_load_dwordx4 v6, s[24:27], s74 offen offset:1024 lds\n"
"s_waitcnt vmcnt(23)\n"
"ds_read_b64_tr_b16 v[160:161], v7 offset:12288\n"
"ds_read_b64_tr_b16 v[162:163], v7 offset:13312\n"
"ds_read_b64_tr_b16 v[164:165], v8 offset:12288\n"
"ds_read_b64_tr_b16 v[166:167], v8 offset:13312\n"
"s_nop 6\n"
"s_waitcnt lgkmcnt(4)\n"
"s_add_u32 s74, s66, 1048576\n"
"s_add_u32 m0, s59, 4096\n"
"s_nop 0\n"
"buffer_load_dwordx4 v5, s[24:27], s74 offen lds\n"
"s_nop 9\n"
"buffer_load_dwordx4 v6, s[24:27], s74 offen offset:1024 lds\n"
"s_waitcnt vmcnt(21)\n"
"s_barrier\n"
"s_sleep 1\n"
"s_nop 5\n"
"buffer_load_dwordx2 v[12:13], v11, s[32:35], 0 offen sc1\n"
"s_sleep 1\n"
"s_add_u32 s74, s45, 4\n"
"s_and_b32 s74, s74, 7\n"
"s_mul_i32 s74, s74, 229376\n"
"s_add_u32 s74, s74, s58\n"
"s_add_u32 m0, s61, 49152\n"
"s_nop 0\n"
"buffer_load_dwordx4 v3, s[16:19], s74 offen lds\n"
"s_sleep 1\n"
"s_add_u32 s74, s45, 5\n"
"s_and_b32 s74, s74, 7\n"
"s_mul_i32 s74, s74, 229376\n"
"s_add_u32 s74, s74, s58\n"
"s_add_u32 m0, s61, 53248\n"
"s_nop 0\n"
"buffer_load_dwordx4 v3, s[16:19], s74 offen lds\n"
"s_sleep 1\n"
"s_add_u32 s74, s45, 4\n"
"s_and_b32 s74, s74, 7\n"
"s_mul_i32 s74, s74, 229376\n"
"s_add_u32 s74, s74, s58\n"
"s_add_u32 m0, s61, 57344\n"
"s_nop 0\n"
"buffer_load_dwordx4 v3, s[20:23], s74 offen lds\n"
"s_sleep 1\n"
"s_add_u32 s74, s45, 5\n"
"s_and_b32 s74, s74, 7\n"
"s_mul_i32 s74, s74, 229376\n"
"s_add_u32 s74, s74, s58\n"
"s_add_u32 m0, s61, 61440\n"
"s_nop 0\n"
"buffer_load_dwordx4 v3, s[20:23], s74 offen lds\n"
"s_waitcnt vmcnt(17)\n"
"s_barrier\n"
"s_waitcnt vmcnt(4)\n"
"v_cmp_eq_u64 vcc, s[40:41], v[12:13]\n"
"s_nop 0\n"
"s_and_b32 s74, vcc_lo, 0xf\n"
"s_cmp_eq_u32 s74, 0xf\n"
"s_cbranch_scc0 LHB_slow_3\n"
"LHB_back_4:\n"
"s_add_u32 s74, s45, 6\n"
"s_and_b32 s74, s74, 7\n"
"s_mul_i32 s74, s74, 229376\n"
"s_add_u32 s74, s74, s58\n"
"s_add_u32 m0, s61, 65536\n"
"s_nop 0\n"
"buffer_load_dwordx4 v3, s[16:19], s74 offen lds\n"
"s_sleep 1\n"
"s_add_u32 s74, s45, 7\n"
"s_and_b32 s74, s74, 7\n"
"s_mul_i32 s74, s74, 229376\n"
"s_add_u32 s74, s74, s58\n"
"s_add_u32 m0, s61, 69632\n"
"s_nop 0\n"
"buffer_load_dwordx4 v3, s[16:19], s74 offen lds\n"
"s_sleep 1\n"
"s_add_u32 s74, s45, 6\n"
"s_and_b32 s74, s74, 7\n"
"s_mul_i32 s74, s74, 229376\n"
"s_add_u32 s74, s74, s58\n"
"s_add_u32 m0, s61, 73728\n"
"s_nop 0\n"
"buffer_load_dwordx4 v3, s[20:23], s74 offen lds\n"
"s_sleep 1\n"
"s_add_u32 s74, s45, 7\n"
"s_and_b32 s74, s74, 7\n"
"s_mul_i32 s74, s74, 229376\n"
"s_add_u32 s74, s74, s58\n"
"s_add_u32 m0, s61, 77824\n"
"s_nop 0\n"
"buffer_load_dwordx4 v3, s[20:23], s74 offen lds\n"
"s_sleep 1\n"
"s_add_u32 s74, s62, 0\n"
"buffer_load_dwordx4 v[168:171], v9, s[32:35], s74 offen sc1\n"
"s_sleep 1\n"
"buffer_load_dwordx4 v[172:175], v10, s[32:35], s74 offen sc1\n"
"s_barrier\n"
"v_lshlrev_b32 v188, 4, v0\n"
"v_add_u32 v188, s59, v188\n"
"ds_read_b128 v[176:179], v188 offset:8192\n"
"ds_read_b128 v[180:183], v188 offset:9216\n"
"ds_read_b128 v[184:187], v188 offset:10240\n"
"ds_read_b128 v[188:191], v188 offset:11264\n"
"s_add_u32 s74, s66, 0\n"
"s_lshl_b32 s75, s42, 12\n"
"s_add_u32 m0, s75, 0\n"
"s_nop 0\n"
"buffer_load_dwordx4 v5, s[28:31], s74 offen lds\n"
"buffer_load_dwordx4 v6, s[28:31], s74 offen offset:1024 lds\n"
"s_add_u32 s74, s66, 65536\n"
"s_lshl_b32 s75, s42, 12\n"
"s_add_u32 m0, s75, 2048\n"
"s_nop 0\n"
"buffer_load_dwordx4 v5, s[28:31], s74 offen lds\n"
"buffer_load_dwordx4 v6, s[28:31], s74 offen offset:1024 lds\n"
"s_nop 11\n"
"s_add_u32 s74, s70, 32768\n"
"s_waitcnt lgkmcnt(3)\n"
"buffer_store_dwordx4 v[176:179], v9, s[32:35], s74 offen sc1\n"
"s_nop 11\n"
"s_waitcnt lgkmcnt(2)\n"
"buffer_store_dwordx4 v[180:183], v9, s[32:35], s74 offen offset:1024 sc1\n"
"s_nop 12\n"
"s_waitcnt lgkmcnt(1)\n"
"buffer_store_dwordx4 v[184:187], v9, s[32:35], s74 offen offset:2048 sc1\n"
"s_nop 12\n"
"s_waitcnt lgkmcnt(0)\n"
"buffer_store_dwordx4 v[188:191], v9, s[32:35], s74 offen offset:3072 sc1\n"
"s_waitcnt vmcnt(14)\n"
"s_barrier\n"
"s_add_u32 s74, s67, 0\n"
"s_lshl_b32 s75, s42, 12\n"
"s_add_u32 m0, s75, 16384\n"
"s_nop 0\n"
"buffer_load_dwordx4 v5, s[28:31], s74 offen lds\n"
"buffer_load_dwordx4 v6, s[28:31], s74 offen offset:1024 lds\n"
"s_add_u32 s74, s67, 65536\n"
"s_lshl_b32 s75, s42, 12\n"
"s_add_u32 m0, s75, 18432\n"
"s_nop 0\n"
"buffer_load_dwordx4 v5, s[28:31], s74 offen lds\n"
"buffer_load_dwordx4 v6, s[28:31], s74 offen offset:1024 lds\n"
"s_nop 11\n"
"s_add_u32 s74, s62, 4096\n"
"buffer_load_dwordx4 v[192:195], v9, s[32:35], s74 offen sc1\n"
"s_nop 12\n"
"buffer_load_dwordx4 v[196:199], v10, s[32:35], s74 offen sc1\n"
"s_sleep 1\n"
"s_add_u32 s74, s63, 0\n"
"buffer_load_dwordx4 v[200:203], v9, s[32:35], s74 offen sc1\n"
"s_nop 9\n"
"buffer_load_dwordx4 v[204:207], v10, s[32:35], s74 offen sc1\n"
"s_waitcnt vmcnt(18)\n"
"s_barrier\n"
"s_add_u32 s74, s68, 0\n"
"s_lshl_b32 s75, s42, 12\n"
"s_add_u32 m0, s75, 32768\n"
"s_nop 0\n"
"buffer_load_dwordx4 v5, s[28:31], s74 offen lds\n"
"buffer_load_dwordx4 v6, s[28:31], s74 offen offset:1024 lds\n"
"s_add_u32 s74, s68, 65536\n"
"s_lshl_b32 s75, s42, 12\n"
"s_add_u32 m0, s75, 34816\n"
"s_nop 0\n"
"buffer_load_dwordx4 v5, s[28:31], s74 offen lds\n"
"buffer_load_dwordx4 v6, s[28:31], s74 offen offset:1024 lds\n"
"s_waitcnt vmcnt(21)\n"
"v_mfma_f32_32x32x16_bf16 v[32:47], v[84:87], v[168:171], 0\n"
"s_waitcnt vmcnt(20)\n"
"v_mfma_f32_32x32x16_bf16 v[48:63], v[80:83], v[172:175], 0\n"
"s_add_u32 s74, s63, 4096\n"
"buffer_load_dwordx4 v[208:211], v9, s[32:35], s74 offen sc1\n"
"v_mfma_f32_32x32x16_bf16 v[64:79], v[84:87], v[172:175], 0\n"
"buffer_load_dwordx4 v[212:215], v10, s[32:35], s74 offen sc1\n"
"v_mfma_f32_32x32x16_bf16 v[16:31], v[80:83], v[168:171], 0\n"
"s_add_u32 s74, s64, 0\n"
"buffer_load_dwordx4 v[216:219], v9, s[32:35], s74 offen sc1\n"
"s_nop 5\n"
"buffer_load_dwordx4 v[220:223], v10, s[32:35], s74 offen sc1\n"
"s_waitcnt vmcnt(16)\n"
"s_lshl_b32 s49, s43, 2\n"
"s_add_u32 s49, s49, s42\n"
"s_lshr_b32 s49, s49, 1\n"
"s_lshl_b32 s49, s49, 9\n"
"s_lshl_b32 s50, s44, 4\n"
"s_add_u32 s49, s49, s50\n"
"s_and_b32 s50, s42, 1\n"
"s_lshl_b32 s50, s50, 3\n"
"s_add_u32 s49, s49, s50\n"
"s_add_u32 s49, s49, 655488\n"
"s_mov_b64 exec, 1\n"
"buffer_store_dwordx2 v[14:15], off, s[32:35], s49 sc1\n"
"s_mov_b64 exec, -1\n"
"s_barrier\n"
"s_add_u32 s74, s69, 0\n"
"s_lshl_b32 s75, s42, 12\n"
"s_add_u32 m0, s75, 49152\n"
"s_nop 0\n"
"buffer_load_dwordx4 v5, s[28:31], s74 offen lds\n"
"buffer_load_dwordx4 v6, s[28:31], s74 offen offset:1024 lds\n"
"s_add_u32 s74, s69, 65536\n"
"s_lshl_b32 s75, s42, 12\n"
"s_add_u32 m0, s75, 51200\n"
"s_nop 0\n"
"buffer_load_dwordx4 v5, s[28:31], s74 offen lds\n"
"s_nop 1\n"
"buffer_load_dwordx4 v6, s[28:31], s74 offen offset:1024 lds\n"
"s_sleep 1\n"
"s_add_u32 s74, s64, 4096\n"
"buffer_load_dwordx4 v[224:227], v9, s[32:35], s74 offen sc1\n"
"s_nop 9\n"
"buffer_load_dwordx4 v[228:231], v10, s[32:35], s74 offen sc1\n"
"s_sleep 1\n"
"s_add_u32 s74, s65, 0\n"
"buffer_load_dwordx4 v[232:235], v9, s[32:35], s74 offen sc1\n"
"s_nop 9\n"
"buffer_load_dwordx4 v[236:239], v10, s[32:35], s74 offen sc1\n"
"s_sleep 1\n"
"s_add_u32 s74, s65, 4096\n"
"buffer_load_dwordx4 v[240:243], v9, s[32:35], s74 offen sc1\n"
"s_nop 9\n"
"buffer_load_dwordx4 v[244:247], v10, s[32:35], s74 offen sc1\n"
"s_waitcnt vmcnt(22)\n"
"v_mfma_f32_32x32x16_bf16 v[32:47], v[92:95], v[192:195], v[32:47]\n"
"s_waitcnt vmcnt(21)\n"
"v_mfma_f32_32x32x16_bf16 v[48:63], v[88:91], v[196:199], v[48:63]\n"
"v_mfma_f32_32x32x16_bf16 v[64:79], v[92:95], v[196:199], v[64:79]\n"
"s_add_u32 s74, s66, 1114112\n"
"s_add_u32 m0, s59, 12288\n"
"s_nop 0\n"
"buffer_load_dwordx4 v5, s[24:27], s74 offen lds\n"
"v_mfma_f32_32x32x16_bf16 v[16:31], v[88:91], v[192:195], v[16:31]\n"
"s_waitcnt vmcnt(21)\n"
"v_mfma_f32_32x32x16_bf16 v[32:47], v[100:103], v[200:203], v[32:47]\n"
"buffer_load_dwordx4 v6, s[24:27], s74 offen offset:1024 lds\n"
"s_waitcnt vmcnt(21)\n"
"v_mfma_f32_32x32x16_bf16 v[48:63], v[96:99], v[204:207], v[48:63]\n"
"v_mfma_f32_32x32x16_bf16 v[64:79], v[100:103], v[204:207], v[64:79]\n"
"s_add_u32 s74, s67, 1048576\n"
"s_add_u32 m0, s59, 8192\n"
"s_nop 0\n"
"buffer_load_dwordx4 v5, s[24:27], s74 offen lds\n"
"v_mfma_f32_32x32x16_bf16 v[16:31], v[96:99], v[200:203], v[16:31]\n"
"s_nop 2\n"
"buffer_load_dwordx4 v6, s[24:27], s74 offen offset:1024 lds\n"
"ds_read_b64_tr_b16 v[248:249], v7 offset:14336\n"
"ds_read_b64_tr_b16 v[250:251], v7 offset:15360\n"
"ds_read_b64_tr_b16 v[252:253], v8 offset:14336\n"
"ds_read_b64_tr_b16 v[254:255], v8 offset:15360\n"
"s_nop 7\n"
"s_add_u32 s74, s67, 1114112\n"
"s_add_u32 m0, s59, 10240\n"
"s_nop 0\n"
"buffer_load_dwordx4 v5, s[24:27], s74 offen lds\n"
"s_nop 10\n"
"buffer_load_dwordx4 v6, s[24:27], s74 offen offset:1024 lds\n"
"ds_read_b64_tr_b16 v[176:177], v7 offset:2048\n"
"ds_read_b64_tr_b16 v[178:179], v7 offset:3072\n"
"ds_read_b64_tr_b16 v[180:181], v8 offset:2048\n"
"ds_read_b64_tr_b16 v[182:183], v8 offset:3072\n"
"s_nop 7\n"
"s_waitcnt lgkmcnt(4)\n"
"s_add_u32 s74, s68, 1048576\n"
"s_add_u32 m0, s59, 14336\n"
"s_nop 0\n"
"buffer_load_dwordx4 v5, s[24:27], s74 offen lds\n"
"s_nop 9\n"
"buffer_load_dwordx4 v6, s[24:27], s74 offen offset:1024 lds\n"
"LPAD9:\n"
"s_cbranch_execz LBKB\n"
"ds_read_b64_tr_b16 v[184:185], v7 offset:0\n"
"ds_read_b64_tr_b16 v[186:187], v7 offset:1024\n"
"ds_read_b64_tr_b16 v[188:189], v8 offset:0\n"
"ds_read_b64_tr_b16 v[190:191], v8 offset:1024\n"
"s_waitcnt vmcnt(22)\n"
"v_mfma_f32_32x32x16_bf16 v[32:47], v[108:111], v[208:211], v[32:47]\n"
"s_waitcnt vmcnt(21)\n"
"v_mfma_f32_32x32x16_bf16 v[48:63], v[104:107], v[212:215], v[48:63]\n"
"s_waitcnt lgkmcnt(4)\n"
"s_add_u32 s74, s68, 1114112\n"
"s_add_u32 m0, s59, 2048\n"
"s_nop 0\n"
"buffer_load_dwordx4 v5, s[24:27], s74 offen lds\n"
"v_mfma_f32_32x32x16_bf16 v[64:79], v[108:111], v[212:215], v[64:79]\n"
"v_mfma_f32_32x32x16_bf16 v[16:31], v[104:107], v[208:211], v[16:31]\n"
"buffer_load_dwordx4 v6, s[24:27], s74 offen offset:1024 lds\n"
"s_waitcnt vmcnt(16)\n"
"s_barrier\n"
"ds_read_b64_tr_b16 v[168:169], v7 offset:16384\n"
"ds_read_b64_tr_b16 v[170:171], v7 offset:17408\n"
"ds_read_b64_tr_b16 v[172:173], v8 offset:16384\n"
"ds_read_b64_tr_b16 v[174:175], v8 offset:17408\n"
"v_mfma_f32_32x32x16_bf16 v[32:47], v[116:119], v[216:219], v[32:47]\n"
"v_mfma_f32_32x32x16_bf16 v[48:63], v[112:115], v[220:223], v[48:63]\n"
"s_waitcnt lgkmcnt(4)\n"
"s_add_u32 s74, s69, 1048576\n"
"s_add_u32 m0, s59, 0\n"
"s_nop 0\n"
"buffer_load_dwordx4 v5, s[24:27], s74 offen lds\n"
"v_mfma_f32_32x32x16_bf16 v[64:79], v[116:119], v[220:223], v[64:79]\n"
"v_mfma_f32_32x32x16_bf16 v[16:31], v[112:115], v[216:219], v[16:31]\n"
"buffer_load_dwordx4 v6, s[24:27], s74 offen offset:1024 lds\n"
"ds_read_b64_tr_b16 v[80:81], v7 offset:6144\n"
"ds_read_b64_tr_b16 v[82:83], v7 offset:7168\n"
"ds_read_b64_tr_b16 v[84:85], v8 offset:6144\n"
"ds_read_b64_tr_b16 v[86:87], v8 offset:7168\n"
"s_nop 2\n"
"s_waitcnt lgkmcnt(4)\n"
"s_add_u32 s74, s69, 1114112\n"
"s_add_u32 m0, s59, 16384\n"
"s_nop 0\n"
"buffer_load_dwordx4 v5, s[24:27], s74 offen lds\n"
"s_nop 9\n"
"buffer_load_dwordx4 v6, s[24:27], s74 offen offset:1024 lds\n"
"ds_read_b64_tr_b16 v[192:193], v7 offset:4096\n"
"ds_read_b64_tr_b16 v[194:195], v7 offset:5120\n"
"ds_read_b64_tr_b16 v[196:197], v8 offset:4096\n"
"ds_read_b64_tr_b16 v[198:199], v8 offset:5120\n"
"s_nop 7\n"
"s_waitcnt lgkmcnt(4)\n"
"s_lshl_b32 s74, s42, 16\n"
"s_sub_u32 s74, s66, s74\n"
"s_add_u32 s74, s74, 1572864\n"
"s_add_u32 m0, s59, 6144\n"
"s_nop 0\n"
"buffer_load_dwordx4 v5, s[24:27], s74 offen lds\n"
"s_nop 7\n"
"buffer_load_dwordx4 v6, s[24:27], s74 offen offset:1024 lds\n"
"s_waitcnt vmcnt(14)\n"
"ds_read_b64_tr_b16 v[88:89], v7 offset:12288\n"
"ds_read_b64_tr_b16 v[90:91], v7 offset:13312\n"
"ds_read_b64_tr_b16 v[92:93], v8 offset:12288\n"
"ds_read_b64_tr_b16 v[94:95], v8 offset:13312\n"
"s_nop 6\n"
"s_waitcnt lgkmcnt(4)\n"
"s_lshl_b32 s74, s42, 16\n"
"s_sub_u32 s74, s67, s74\n"
"s_add_u32 s74, s74, 1572864\n"
"s_add_u32 m0, s59, 4096\n"
"s_nop 0\n"
"buffer_load_dwordx4 v5, s[24:27], s74 offen lds\n"
"v_mfma_f32_32x32x16_bf16 v[32:47], v[124:127], v[224:227], v[32:47]\n"
"v_mfma_f32_32x32x16_bf16 v[48:63], v[120:123], v[228:231], v[48:63]\n"
"buffer_load_dwordx4 v6, s[24:27], s74 offen offset:1024 lds\n"
"s_waitcnt vmcnt(14)\n"
"ds_read_b64_tr_b16 v[200:201], v7 offset:8192\n"
"ds_read_b64_tr_b16 v[202:203], v7 offset:9216\n"
"ds_read_b64_tr_b16 v[204:205], v8 offset:8192\n"
"ds_read_b64_tr_b16 v[206:207], v8 offset:9216\n"
"v_mfma_f32_32x32x16_bf16 v[64:79], v[124:127], v[228:231], v[64:79]\n"
"v_mfma_f32_32x32x16_bf16 v[16:31], v[120:123], v[224:227], v[16:31]\n"
"s_waitcnt lgkmcnt(4)\n"
"s_lshl_b32 s74, s42, 16\n"
"s_sub_u32 s74, s68, s74\n"
"s_add_u32 s74, s74, 1572864\n"
"s_add_u32 m0, s59, 12288\n"
"s_nop 0\n"
"buffer_load_dwordx4 v5, s[24:27], s74 offen lds\n"
"v_mfma_f32_32x32x16_bf16 v[32:47], v[132:135], v[232:235], v[32:47]\n"
"v_mfma_f32_32x32x16_bf16 v[48:63], v[128:131], v[236:239], v[48:63]\n"
"buffer_load_dwordx4 v6, s[24:27], s74 offen offset:1024 lds\n"
"v_mfma_f32_32x32x16_bf16 v[64:79], v[132:135], v[236:239], v[64:79]\n"
"v_mfma_f32_32x32x16_bf16 v[16:31], v[128:131], v[232:235], v[16:31]\n"
"s_waitcnt lgkmcnt(0)\n"
"s_lshl_b32 s74, s42, 16\n"
"s_sub_u32 s74, s69, s74\n"
"s_add_u32 s74, s74, 1572864\n"
"s_add_u32 m0, s59, 8192\n"
"s_nop 0\n"
"buffer_load_dwordx4 v5, s[24:27], s74 offen lds\n"
"v_mfma_f32_32x32x16_bf16 v[32:47], v[140:143], v[240:243], v[32:47]\n"
"buffer_load_dwordx4 v6, s[24:27], s74 offen offset:1024 lds\n"
"v_mfma_f32_32x32x16_bf16 v[48:63], v[136:139], v[244:247], v[48:63]\n"
"v_mfma_f32_32x32x16_bf16 v[64:79], v[140:143], v[244:247], v[64:79]\n"
"buffer_load_dwordx2 v[12:13], v11, s[32:35], 0 offen sc1\n"
"v_mfma_f32_32x32x16_bf16 v[16:31], v[136:139], v[240:243], v[16:31]\n"
"s_waitcnt vmcnt(0)\n"
"v_cmp_eq_u64 vcc, s[40:41], v[12:13]\n"
"s_nop 0\n"
"s_and_b32 s74, vcc_lo, 0xf0\n"
"s_cmp_eq_u32 s74, 0xf0\n"
"s_cbranch_scc0 LHB_slow_5\n"
"LHB_back_6:\n"
"v_cmp_eq_u64 vcc, s[40:41], v[12:13]\n"
"s_nop 0\n"
"s_and_b32 s74, vcc_lo, 0xf0000\n"
"s_cmp_eq_u32 s74, 0xf0000\n"
"s_cbranch_scc0 LHB_slow_7\n"
"LHB_back_8:\n"
"s_lshr_b32 s74, s46, 1\n"
"s_lshl_b32 s74, s74, 2\n"
"s_add_u32 s74, s74, s42\n"
"s_lshl_b32 s74, s74, 10\n"
"s_and_b32 s75, s46, 1\n"
"s_lshl_b32 s75, s75, 8\n"
"s_add_u32 s74, s74, s75\n"
"s_add_u32 s74, s74, 393216\n"
"v_lshlrev_b32 v3, 2, v0\n"
"buffer_load_dword v4, v3, s[32:35], s74 offen offset:512 sc1\n"
"buffer_load_dword v3, v3, s[32:35], s74 offen sc1\n"
"buffer_load_dwordx2 v[12:13], v11, s[32:35], 0 offen sc1\n"
"s_nop 1\n"
"s_add_u32 s74, s62, 32768\n"
"buffer_load_dwordx4 v[96:99], v9, s[32:35], s74 offen sc1\n"
"s_nop 12\n"
"buffer_load_dwordx4 v[100:103], v10, s[32:35], s74 offen sc1\n"
"s_sleep 1\n"
"s_add_u32 s74, s62, 36864\n"
"buffer_load_dwordx4 v[208:211], v9, s[32:35], s74 offen sc1\n"
"s_nop 9\n"
"buffer_load_dwordx4 v[212:215], v10, s[32:35], s74 offen sc1\n"
"s_sleep 1\n"
"s_add_u32 s74, s63, 32768\n"
"buffer_load_dwordx4 v[104:107], v9, s[32:35], s74 offen sc1\n"
"s_nop 9\n"
"buffer_load_dwordx4 v[108:111], v10, s[32:35], s74 offen sc1\n"
"s_sleep 1\n"
"s_add_u32 s74, s63, 36864\n"
"buffer_load_dwordx4 v[216:219], v9, s[32:35], s74 offen sc1\n"
"s_nop 9\n"
"buffer_load_dwordx4 v[220:223], v10, s[32:35], s74 offen sc1\n"
"s_sleep 1\n"
"s_add_u32 s74, s64, 32768\n"
"buffer_load_dwordx4 v[112:115], v9, s[32:35], s74 offen sc1\n"
"s_nop 9\n"
"buffer_load_dwordx4 v[116:119], v10, s[32:35], s74 offen sc1\n"
"s_sleep 1\n"
"s_add_u32 s74, s64, 36864\n"
"buffer_load_dwordx4 v[224:227], v9, s[32:35], s74 offen sc1\n"
"s_nop 9\n"
"buffer_load_dwordx4 v[228:231], v10, s[32:35], s74 offen sc1\n"
"s_sleep 1\n"
"s_add_u32 s74, s65, 32768\n"
"buffer_load_dwordx4 v[120:123], v9, s[32:35], s74 offen sc1\n"
"s_nop 9\n"
"buffer_load_dwordx4 v[124:127], v10, s[32:35], s74 offen sc1\n"
"s_sleep 1\n"
"s_add_u32 s74, s65, 36864\n"
"buffer_load_dwordx4 v[232:235], v9, s[32:35], s74 offen sc1\n"
"s_nop 9\n"
"buffer_load_dwordx4 v[236:239], v10, s[32:35], s74 offen sc1\n"
"s_sleep 1\n"
"s_add_u32 s74, s62, 65536\n"
"buffer_load_dwordx4 v[128:131], v9, s[32:35], s74 offen sc1\n"
"s_nop 9\n"
"buffer_load_dwordx4 v[132:135], v10, s[32:35], s74 offen sc1\n"
"s_sleep 1\n"
"s_add_u32 s74, s62, 69632\n"
"buffer_load_dwordx4 v[240:243], v9, s[32:35], s74 offen sc1\n"
"s_nop 9\n"
"buffer_load_dwordx4 v[244:247], v10, s[32:35], s74 offen sc1\n"
"s_sleep 1\n"
"s_add_u32 s74, s63, 65536\n"
"buffer_load_dwordx4 v[136:139], v9, s[32:35], s74 offen sc1\n"
"s_nop 9\n"
"buffer_load_dwordx4 v[140:143], v10, s[32:35], s74 offen sc1\n"
"s_waitcnt vmcnt(21)\n"
"v_mfma_f32_32x32x16_bf16 v[32:47], v[148:151], v[96:99], v[32:47]\n"
"s_waitcnt vmcnt(20)\n"
"v_mfma_f32_32x32x16_bf16 v[48:63], v[144:147], v[100:103], v[48:63]\n"
"v_mfma_f32_32x32x16_bf16 v[64:79], v[148:151], v[100:103], v[64:79]\n"
"v_mfma_f32_32x32x16_bf16 v[16:31], v[144:147], v[96:99], v[16:31]\n"
"s_add_u32 s74, s63, 69632\n"
"buffer_load_dwordx4 v[96:99], v9, s[32:35], s74 offen sc1\n"
"buffer_load_dwordx4 v[100:103], v10, s[32:35], s74 offen sc1\n"
"s_add_u32 s74, s64, 65536\n"
"buffer_load_dwordx4 v[144:147], v9, s[32:35], s74 offen sc1\n"
"s_waitcnt vmcnt(22)\n"
"v_mfma_f32_32x32x16_bf16 v[32:47], v[156:159], v[208:211], v[32:47]\n"
"s_waitcnt vmcnt(21)\n"
"v_mfma_f32_32x32x16_bf16 v[48:63], v[152:155], v[212:215], v[48:63]\n"
"buffer_load_dwordx4 v[148:151], v10, s[32:35], s74 offen sc1\n"
"v_mfma_f32_32x32x16_bf16 v[64:79], v[156:159], v[212:215], v[64:79]\n"
"v_mfma_f32_32x32x16_bf16 v[16:31], v[152:155], v[208:211], v[16:31]\n"
"s_add_u32 s74, s64, 69632\n"
"buffer_load_dwordx4 v[208:211], v9, s[32:35], s74 offen sc1\n"
"s_waitcnt vmcnt(22)\n"
"v_mfma_f32_32x32x16_bf16 v[32:47], v[164:167], v[104:107], v[32:47]\n"
"s_waitcnt vmcnt(21)\n"
"v_mfma_f32_32x32x16_bf16 v[48:63], v[160:163], v[108:111], v[48:63]\n"
"buffer_load_dwordx4 v[212:215], v10, s[32:35], s74 offen sc1\n"
"v_mfma_f32_32x32x16_bf16 v[64:79], v[164:167], v[108:111], v[64:79]\n"
"v_mfma_f32_32x32x16_bf16 v[16:31], v[160:163], v[104:107], v[16:31]\n"
"s_add_u32 s74, s65, 65536\n"
"buffer_load_dwordx4 v[152:155], v9, s[32:35], s74 offen sc1\n"
"s_waitcnt vmcnt(22)\n"
"v_mfma_f32_32x32x16_bf16 v[32:47], v[252:255], v[216:219], v[32:47]\n"
"s_waitcnt vmcnt(21)\n"
"v_mfma_f32_32x32x16_bf16 v[48:63], v[248:251], v[220:223], v[48:63]\n"
"buffer_load_dwordx4 v[156:159], v10, s[32:35], s74 offen sc1\n"
"v_mfma_f32_32x32x16_bf16 v[64:79], v[252:255], v[220:223], v[64:79]\n"
"v_mfma_f32_32x32x16_bf16 v[16:31], v[248:251], v[216:219], v[16:31]\n"
"s_add_u32 s74, s65, 69632\n"
"buffer_load_dwordx4 v[104:107], v9, s[32:35], s74 offen sc1\n"
"s_waitcnt vmcnt(22)\n"
"v_mfma_f32_32x32x16_bf16 v[32:47], v[180:183], v[112:115], v[32:47]\n"
"s_waitcnt vmcnt(21)\n"
"v_mfma_f32_32x32x16_bf16 v[48:63], v[176:179], v[116:119], v[48:63]\n"
"buffer_load_dwordx4 v[108:111], v10, s[32:35], s74 offen sc1\n"
"v_cmp_eq_u64 vcc, s[40:41], v[12:13]\n"
"s_nop 0\n"
"s_and_b32 s74, vcc_lo, 0xf00\n"
"s_cmp_eq_u32 s74, 0xf00\n"
"s_cbranch_scc0 LHB_slow_9\n"
"LHB_back_10:\n"
"v_mfma_f32_32x32x16_bf16 v[64:79], v[180:183], v[116:119], v[64:79]\n"
"v_mfma_f32_32x32x16_bf16 v[16:31], v[176:179], v[112:115], v[16:31]\n"
"s_lshl_b32 s74, s42, 12\n"
"s_sub_u32 s74, s62, s74\n"
"s_add_u32 s74, s74, 98304\n"
"buffer_load_dwordx4 v[160:163], v9, s[32:35], s74 offen sc1\n"
"s_waitcnt vmcnt(22)\n"
"v_mfma_f32_32x32x16_bf16 v[32:47], v[188:191], v[224:227], v[32:47]\n"
"s_waitcnt vmcnt(21)\n"
"v_mfma_f32_32x32x16_bf16 v[48:63], v[184:187], v[228:231], v[48:63]\n"
"buffer_load_dwordx4 v[164:167], v10, s[32:35], s74 offen sc1\n"
"v_mfma_f32_32x32x16_bf16 v[64:79], v[188:191], v[228:231], v[64:79]\n"
"v_mfma_f32_32x32x16_bf16 v[16:31], v[184:187], v[224:227], v[16:31]\n"
"s_lshl_b32 s74, s42, 12\n"
"s_sub_u32 s74, s63, s74\n"
"s_add_u32 s74, s74, 98304\n"
"buffer_load_dwordx4 v[216:219], v9, s[32:35], s74 offen sc1\n"
"s_waitcnt vmcnt(22)\n"
"v_mfma_f32_32x32x16_bf16 v[32:47], v[172:175], v[120:123], v[32:47]\n"
"s_waitcnt vmcnt(21)\n"
"v_mfma_f32_32x32x16_bf16 v[48:63], v[168:171], v[124:127], v[48:63]\n"
"buffer_load_dwordx4 v[220:223], v10, s[32:35], s74 offen sc1\n"
"v_mfma_f32_32x32x16_bf16 v[64:79], v[172:175], v[124:127], v[64:79]\n"
"v_mfma_f32_32x32x16_bf16 v[16:31], v[168:171], v[120:123], v[16:31]\n"
"s_lshl_b32 s74, s42, 12\n"
"s_sub_u32 s74, s64, s74\n"
"s_add_u32 s74, s74, 98304\n"
"buffer_load_dwordx4 v[248:251], v9, s[32:35], s74 offen sc1\n"
"s_waitcnt vmcnt(22)\n"
"v_mfma_f32_32x32x16_bf16 v[32:47], v[84:87], v[232:235], v[32:47]\n"
"s_waitcnt vmcnt(21)\n"
"v_mfma_f32_32x32x16_bf16 v[48:63], v[80:83], v[236:239], v[48:63]\n"
"buffer_load_dwordx4 v[252:255], v10, s[32:35], s74 offen sc1\n"
"v_mfma_f32_32x32x16_bf16 v[64:79], v[84:87], v[236:239], v[64:79]\n"
"v_mfma_f32_32x32x16_bf16 v[16:31], v[80:83], v[232:235], v[16:31]\n"
"s_lshl_b32 s74, s42, 12\n"
"s_sub_u32 s74, s65, s74\n"
"s_add_u32 s74, s74, 98304\n"
"buffer_load_dwordx4 v[112:115], v9, s[32:35], s74 offen sc1\n"
"ds_read_b64_tr_b16 v[176:177], v7 offset:10240\n"
"ds_read_b64_tr_b16 v[178:179], v7 offset:11264\n"
"ds_read_b64_tr_b16 v[180:181], v8 offset:10240\n"
"ds_read_b64_tr_b16 v[182:183], v8 offset:11264\n"
"s_waitcnt vmcnt(22)\n"
"v_mfma_f32_32x32x16_bf16 v[32:47], v[196:199], v[128:131], v[32:47]\n"
"buffer_load_dwordx4 v[116:119], v10, s[32:35], s74 offen sc1\n"
"v_lshlrev_b32 v5, 4, v0\n"
"v_and_b32 v2, 31, v0\n"
"v_lshlrev_b32 v2, 10, v2\n"
"v_lshrrev_b32 v10, 5, v0\n"
"s_waitcnt vmcnt(22)\n"
"v_mfma_f32_32x32x16_bf16 v[48:63], v[192:195], v[132:135], v[48:63]\n"
"v_lshl_add_u32 v2, v10, 4, v2\n"
"s_lshl_b32 s72, s46, 16\n"
"s_lshr_b32 s50, s42, 1\n"
"s_lshl_b32 s50, s50, 15\n"
"v_mfma_f32_32x32x16_bf16 v[64:79], v[196:199], v[132:135], v[64:79]\n"
"s_add_u32 s72, s72, s50\n"
"s_lshl_b32 s50, s47, 7\n"
"s_add_u32 s72, s72, s50\n"
"s_and_b32 s50, s42, 1\n"
"v_mfma_f32_32x32x16_bf16 v[16:31], v[192:195], v[128:131], v[16:31]\n"
"s_lshl_b32 s50, s50, 6\n"
"s_add_u32 s72, s72, s50\n"
"s_xor_b32 s49, s42, 1\n"
"s_mul_i32 s49, s49, 18432\n"
"s_add_u32 s49, s49, 81920\n"
"v_add_u32 v11, s49, v5\n"
"s_xor_b32 s49, s42, 2\n"
"s_mul_i32 s49, s49, 18432\n"
"s_add_u32 s49, s49, 86016\n"
"v_add_u32 v12, s49, v5\n"
"s_xor_b32 s49, s42, 3\n"
"s_mul_i32 s49, s49, 18432\n"
"s_add_u32 s49, s49, 90112\n"
"v_add_u32 v13, s49, v5\n"
"s_xor_b32 s49, s42, 1\n"
"s_lshl_b32 s49, s49, 12\n"
"s_add_u32 s49, s49, 65536\n"
"v_add_u32 v14, s49, v5\n"
"s_lshl_b32 s49, s42, 11\n"
"s_add_u32 s49, s49, 155648\n"
"v_add_u32 v15, s49, v5\n"
"s_lshr_b32 s49, s42, 1\n"
"s_lshl_b32 s49, s49, 7\n"
"s_add_u32 s49, s49, 96256\n"
"v_and_b32 v10, 31, v0\n"
"v_lshl_add_u32 v10, v10, 2, s49\n"
"v_add_u32 v1, s59, v5\n"
"ds_read_b64_tr_b16 v[224:225], v7 offset:14336\n"
"ds_read_b64_tr_b16 v[226:227], v7 offset:15360\n"
"ds_read_b64_tr_b16 v[228:229], v8 offset:14336\n"
"ds_read_b64_tr_b16 v[230:231], v8 offset:15360\n"
"s_waitcnt vmcnt(21)\n"
"v_mfma_f32_32x32x16_bf16 v[32:47], v[92:95], v[240:243], v[32:47]\n"
"s_waitcnt vmcnt(20)\n"
"v_mfma_f32_32x32x16_bf16 v[48:63], v[88:91], v[244:247], v[48:63]\n"
"v_mfma_f32_32x32x16_bf16 v[64:79], v[92:95], v[244:247], v[64:79]\n"
"v_mfma_f32_32x32x16_bf16 v[16:31], v[88:91], v[240:243], v[16:31]\n"
"ds_read_b64_tr_b16 v[184:185], v7 offset:2048\n"
"ds_read_b64_tr_b16 v[186:187], v7 offset:3072\n"
"ds_read_b64_tr_b16 v[188:189], v8 offset:2048\n"
"ds_read_b64_tr_b16 v[190:191], v8 offset:3072\n"
"s_waitcnt vmcnt(19)\n"
"v_mfma_f32_32x32x16_bf16 v[32:47], v[204:207], v[136:139], v[32:47]\n"
"s_waitcnt vmcnt(18)\n"
"v_mfma_f32_32x32x16_bf16 v[48:63], v[200:203], v[140:143], v[48:63]\n"
"v_mfma_f32_32x32x16_bf16 v[64:79], v[204:207], v[140:143], v[64:79]\n"
"v_mfma_f32_32x32x16_bf16 v[16:31], v[200:203], v[136:139], v[16:31]\n"
"ds_read_b64_tr_b16 v[120:121], v7 offset:0\n"
"ds_read_b64_tr_b16 v[122:123], v7 offset:1024\n"
"ds_read_b64_tr_b16 v[124:125], v8 offset:0\n"
"s_waitcnt lgkmcnt(14)\n"
"ds_read_b64_tr_b16 v[126:127], v8 offset:1024\n"
"s_waitcnt vmcnt(17) lgkmcnt(12)\n"
"v_mfma_f32_32x32x16_bf16 v[32:47], v[180:183], v[96:99], v[32:47]\n"
"s_waitcnt vmcnt(16)\n"
"v_mfma_f32_32x32x16_bf16 v[48:63], v[176:179], v[100:103], v[48:63]\n"
"v_mfma_f32_32x32x16_bf16 v[64:79], v[180:183], v[100:103], v[64:79]\n"
"v_mfma_f32_32x32x16_bf16 v[16:31], v[176:179], v[96:99], v[16:31]\n"
"ds_read_b64_tr_b16 v[168:169], v7 offset:16384\n"
"ds_read_b64_tr_b16 v[170:171], v7 offset:17408\n"
"ds_read_b64_tr_b16 v[172:173], v8 offset:16384\n"
"s_waitcnt lgkmcnt(14)\n"
"ds_read_b64_tr_b16 v[174:175], v8 offset:17408\n"
"s_waitcnt vmcnt(15) lgkmcnt(12)\n"
"v_mfma_f32_32x32x16_bf16 v[32:47], v[228:231], v[144:147], v[32:47]\n"
"s_waitcnt vmcnt(14)\n"
"v_mfma_f32_32x32x16_bf16 v[48:63], v[224:227], v[148:151], v[48:63]\n"
"v_mfma_f32_32x32x16_bf16 v[64:79], v[228:231], v[148:151], v[64:79]\n"
"v_mfma_f32_32x32x16_bf16 v[16:31], v[224:227], v[144:147], v[16:31]\n"
"ds_read_b64_tr_b16 v[232:233], v7 offset:6144\n"
"ds_read_b64_tr_b16 v[234:235], v7 offset:7168\n"
"ds_read_b64_tr_b16 v[236:237], v8 offset:6144\n"
"s_waitcnt lgkmcnt(14)\n"
"ds_read_b64_tr_b16 v[238:239], v8 offset:7168\n"
"s_waitcnt vmcnt(13) lgkmcnt(12)\n"
"v_mfma_f32_32x32x16_bf16 v[32:47], v[188:191], v[208:211], v[32:47]\n"
"s_waitcnt vmcnt(12)\n"
"v_mfma_f32_32x32x16_bf16 v[48:63], v[184:187], v[212:215], v[48:63]\n"
"v_mfma_f32_32x32x16_bf16 v[64:79], v[188:191], v[212:215], v[64:79]\n"
"v_mfma_f32_32x32x16_bf16 v[16:31], v[184:187], v[208:211], v[16:31]\n"
"ds_read_b64_tr_b16 v[80:81], v7 offset:4096\n"
"ds_read_b64_tr_b16 v[82:83], v7 offset:5120\n"
"ds_read_b64_tr_b16 v[84:85], v8 offset:4096\n"
"s_waitcnt lgkmcnt(14)\n"
"ds_read_b64_tr_b16 v[86:87], v8 offset:5120\n"
"s_waitcnt vmcnt(11) lgkmcnt(12)\n"
"v_mfma_f32_32x32x16_bf16 v[32:47], v[124:127], v[152:155], v[32:47]\n"
"s_waitcnt vmcnt(10)\n"
"v_mfma_f32_32x32x16_bf16 v[48:63], v[120:123], v[156:159], v[48:63]\n"
"v_mfma_f32_32x32x16_bf16 v[64:79], v[124:127], v[156:159], v[64:79]\n"
"v_mfma_f32_32x32x16_bf16 v[16:31], v[120:123], v[152:155], v[16:31]\n"
"ds_read_b64_tr_b16 v[128:129], v7 offset:12288\n"
"ds_read_b64_tr_b16 v[130:131], v7 offset:13312\n"
"ds_read_b64_tr_b16 v[132:133], v8 offset:12288\n"
"s_waitcnt lgkmcnt(14)\n"
"ds_read_b64_tr_b16 v[134:135], v8 offset:13312\n"
"s_waitcnt vmcnt(9) lgkmcnt(12)\n"
"v_mfma_f32_32x32x16_bf16 v[32:47], v[172:175], v[104:107], v[32:47]\n"
"s_waitcnt vmcnt(8)\n"
"v_mfma_f32_32x32x16_bf16 v[48:63], v[168:171], v[108:111], v[48:63]\n"
"v_mfma_f32_32x32x16_bf16 v[64:79], v[172:175], v[108:111], v[64:79]\n"
"v_mfma_f32_32x32x16_bf16 v[16:31], v[168:171], v[104:107], v[16:31]\n"
"ds_read_b64_tr_b16 v[192:193], v7 offset:8192\n"
"ds_read_b64_tr_b16 v[194:195], v7 offset:9216\n"
"ds_read_b64_tr_b16 v[196:197], v8 offset:8192\n"
"s_waitcnt lgkmcnt(14)\n"
"ds_read_b64_tr_b16 v[198:199], v8 offset:9216\n"
"s_waitcnt vmcnt(7) lgkmcnt(12)\n"
"v_mfma_f32_32x32x16_bf16 v[32:47], v[236:239], v[160:163], v[32:47]\n"
"s_waitcnt vmcnt(6)\n"
"v_mfma_f32_32x32x16_bf16 v[48:63], v[232:235], v[164:167], v[48:63]\n"
"v_mfma_f32_32x32x16_bf16 v[64:79], v[236:239], v[164:167], v[64:79]\n"
"v_mfma_f32_32x32x16_bf16 v[16:31], v[232:235], v[160:163], v[16:31]\n"
"s_waitcnt vmcnt(5) lgkmcnt(8)\n"
"v_mfma_f32_32x32x16_bf16 v[32:47], v[84:87], v[216:219], v[32:47]\n"
"s_waitcnt vmcnt(4)\n"
"v_mfma_f32_32x32x16_bf16 v[48:63], v[80:83], v[220:223], v[48:63]\n"
"v_mfma_f32_32x32x16_bf16 v[64:79], v[84:87], v[220:223], v[64:79]\n"
"v_mfma_f32_32x32x16_bf16 v[16:31], v[80:83], v[216:219], v[16:31]\n"
"s_waitcnt vmcnt(3) lgkmcnt(4)\n"
"v_mfma_f32_32x32x16_bf16 v[32:47], v[132:135], v[248:251], v[32:47]\n"
"s_waitcnt vmcnt(2)\n"
"v_mfma_f32_32x32x16_bf16 v[48:63], v[128:131], v[252:255], v[48:63]\n"
"v_mfma_f32_32x32x16_bf16 v[64:79], v[132:135], v[252:255], v[64:79]\n"
"v_mfma_f32_32x32x16_bf16 v[16:31], v[128:131], v[248:251], v[16:31]\n"
"s_waitcnt vmcnt(1) lgkmcnt(0)\n"
"v_mfma_f32_32x32x16_bf16 v[32:47], v[196:199], v[112:115], v[32:47]\n"
"s_waitcnt vmcnt(0)\n"
"v_mfma_f32_32x32x16_bf16 v[48:63], v[192:195], v[116:119], v[48:63]\n"
"v_mfma_f32_32x32x16_bf16 v[64:79], v[196:199], v[116:119], v[64:79]\n"
"v_mfma_f32_32x32x16_bf16 v[16:31], v[192:195], v[112:115], v[16:31]\n"
"v_add_f32 v3, v3, v4\n"
"v_lshlrev_b32 v4, 2, v0\n"
"v_add_u32 v4, s59, v4\n"
"ds_write_b32 v4, v3 offset:14336\n"
"s_nop 3\n"
"ds_write_b128 v1, v[32:35] offset:0\n"
"ds_write_b128 v1, v[36:39] offset:1024\n"
"ds_write_b128 v1, v[40:43] offset:2048\n"
"ds_write_b128 v1, v[44:47] offset:3072\n"
"ds_write_b128 v1, v[48:51] offset:4096\n"
"ds_write_b128 v1, v[52:55] offset:5120\n"
"ds_write_b128 v1, v[56:59] offset:6144\n"
"ds_write_b128 v1, v[60:63] offset:7168\n"
"ds_write_b128 v1, v[64:67] offset:8192\n"
"ds_write_b128 v1, v[68:71] offset:9216\n"
"ds_write_b128 v1, v[72:75] offset:10240\n"
"ds_write_b128 v1, v[76:79] offset:11264\n"
"ds_write_b128 v1, v[24:27] offset:12288\n"
"ds_write_b128 v1, v[28:31] offset:13312\n"
"s_waitcnt lgkmcnt(0)\n"
"s_barrier\n"
"ds_read_b32 v120, v10 offset:0\n"
"ds_read_b32 v121, v10 offset:18432\n"
"ds_read_b32 v122, v10 offset:36864\n"
"ds_read_b32 v123, v10 offset:55296\n"
"ds_read_b128 v[104:107], v14 offset:0\n"
"ds_read_b128 v[108:111], v14 offset:1024\n"
"ds_read_b128 v[112:115], v15 offset:0\n"
"ds_read_b128 v[116:119], v15 offset:1024\n"
"ds_read_b128 v[80:83], v11 offset:0\n"
"ds_read_b128 v[84:87], v11 offset:1024\n"
"ds_read_b128 v[88:91], v12 offset:0\n"
"ds_read_b128 v[92:95], v12 offset:1024\n"
"ds_read_b128 v[96:99], v13 offset:0\n"
"ds_read_b128 v[100:103], v13 offset:1024\n"
"s_waitcnt lgkmcnt(12)\n"
"v_add_f32 v120, v120, v121\n"
"s_waitcnt lgkmcnt(10)\n"
"v_add_f32 v122, v122, v123\n"
"v_add_f32 v120, v120, v122\n"
"v_mul_f32 v120, 0xbfb8aa3b, v120\n"
"v_exp_f32 v120, v120\n"
"s_nop 0\n"
"v_add_f32 v120, 1.0, v120\n"
"v_rcp_f32 v120, v120\n"
"s_waitcnt lgkmcnt(7)\n"
"v_add_f32 v104, v104, v112\n"
"v_add_f32 v105, v105, v113\n"
"v_add_f32 v106, v106, v114\n"
"v_add_f32 v107, v107, v115\n"
"s_waitcnt lgkmcnt(6)\n"
"v_add_f32 v108, v108, v116\n"
"v_add_f32 v109, v109, v117\n"
"v_add_f32 v110, v110, v118\n"
"v_add_f32 v111, v111, v119\n"
"s_waitcnt lgkmcnt(5)\n"
"v_add_f32 v16, v16, v80\n"
"v_add_f32 v17, v17, v81\n"
"v_add_f32 v18, v18, v82\n"
"v_add_f32 v19, v19, v83\n"
"s_waitcnt lgkmcnt(4)\n"
"v_add_f32 v20, v20, v84\n"
"v_add_f32 v21, v21, v85\n"
"v_add_f32 v22, v22, v86\n"
"v_add_f32 v23, v23, v87\n"
"s_waitcnt lgkmcnt(3)\n"
"v_add_f32 v16, v16, v88\n"
"v_add_f32 v17, v17, v89\n"
"v_add_f32 v18, v18, v90\n"
"v_add_f32 v19, v19, v91\n"
"s_waitcnt lgkmcnt(2)\n"
"v_add_f32 v20, v20, v92\n"
"v_add_f32 v21, v21, v93\n"
"v_add_f32 v22, v22, v94\n"
"v_add_f32 v23, v23, v95\n"
"s_waitcnt lgkmcnt(1)\n"
"v_add_f32 v16, v16, v96\n"
"v_add_f32 v17, v17, v97\n"
"v_add_f32 v18, v18, v98\n"
"v_add_f32 v19, v19, v99\n"
"s_waitcnt lgkmcnt(0)\n"
"v_add_f32 v20, v20, v100\n"
"v_add_f32 v21, v21, v101\n"
"v_add_f32 v22, v22, v102\n"
"v_add_f32 v23, v23, v103\n"
"v_sub_f32 v104, v104, v16\n"
"v_sub_f32 v105, v105, v17\n"
"v_sub_f32 v106, v106, v18\n"
"v_sub_f32 v107, v107, v19\n"
"v_sub_f32 v108, v108, v20\n"
"v_sub_f32 v109, v109, v21\n"
"v_sub_f32 v110, v110, v22\n"
"v_sub_f32 v111, v111, v23\n"
"v_fma_f32 v16, v120, v104, v16\n"
"v_fma_f32 v17, v120, v105, v17\n"
"v_fma_f32 v18, v120, v106, v18\n"
"v_fma_f32 v19, v120, v107, v19\n"
"v_fma_f32 v20, v120, v108, v20\n"
"v_fma_f32 v21, v120, v109, v21\n"
"v_fma_f32 v22, v120, v110, v22\n"
"v_fma_f32 v23, v120, v111, v23\n"
"v_cvt_pk_bf16_f32 v16, v16, v17\n"
"v_cvt_pk_bf16_f32 v17, v18, v19\n"
"v_cvt_pk_bf16_f32 v18, v20, v21\n"
"v_cvt_pk_bf16_f32 v19, v22, v23\n"
"s_nop 0\n"
"v_permlane32_swap_b32 v16, v18\n"
"v_permlane32_swap_b32 v17, v19\n"
"buffer_store_dwordx4 v[16:19], v2, s[36:39], s72 offen\n"
"s_endpgm\n"
"LPAD10:\n"
"s_cbranch_execz LPAD13\n"
"LTHB:\n"
"LHB_eb_1:\n"
"s_mov_b32 s50, 131072\n"
"LHB_eb_1l:\n"
"s_mov_b64 s[52:53], -1\n"
"v_cmp_eq_u64 vcc, s[40:41], v[32:33]\n"
"s_nop 1\n"
"s_and_b64 s[52:53], s[52:53], vcc\n"
"v_cmp_eq_u64 vcc, s[40:41], v[34:35]\n"
"s_nop 1\n"
"s_and_b64 s[52:53], s[52:53], vcc\n"
"v_cmp_eq_u64 vcc, s[40:41], v[36:37]\n"
"s_nop 1\n"
"s_and_b64 s[52:53], s[52:53], vcc\n"
"v_cmp_eq_u64 vcc, s[40:41], v[38:39]\n"
"s_nop 1\n"
"s_and_b64 s[52:53], s[52:53], vcc\n"
"s_cmp_eq_u64 s[52:53], -1\n"
"s_cbranch_scc1 LHB_eb_1s\n"
"s_sleep 2\n"
"buffer_load_dwordx4 v[32:35], v40, s[32:35], 0 offen sc1\n"
"buffer_load_dwordx4 v[36:39], v40, s[32:35], 0 offen offset:16 sc1\n"
"s_waitcnt vmcnt(0)\n"
"s_sub_u32 s50, s50, 1\n"
"s_cmp_lg_u32 s50, 0\n"
"s_cbranch_scc1 LHB_eb_1l\n"
"LHB_eb_1s:\n"
"s_mov_b64 exec, 1\n"
"buffer_store_dword v14, off, s[32:35], 0 sc1\n"
"s_mov_b64 exec, -1\n"
"s_nop 1\n"
"s_branch LHB_ebk_2\n"
"LHB_slow_3:\n"
"s_mov_b32 s75, 131072\n"
"LHB_slow_3l:\n"
"buffer_load_dwordx2 v[12:13], v11, s[32:35], 0 offen sc1\n"
"s_waitcnt vmcnt(0)\n"
"v_cmp_eq_u64 vcc, s[40:41], v[12:13]\n"
"s_nop 1\n"
"s_and_b32 s74, vcc_lo, 0xf\n"
"s_cmp_eq_u32 s74, 0xf\n"
"s_cbranch_scc1 LHB_back_4\n"
"s_sleep 1\n"
"s_sub_u32 s75, s75, 1\n"
"s_cmp_lg_u32 s75, 0\n"
"s_cbranch_scc1 LHB_slow_3l\n"
"s_branch LHB_back_4\n"
"LHB_slow_5:\n"
"s_mov_b32 s75, 131072\n"
"LHB_slow_5l:\n"
"buffer_load_dwordx2 v[12:13], v11, s[32:35], 0 offen sc1\n"
"s_waitcnt vmcnt(0)\n"
"v_cmp_eq_u64 vcc, s[40:41], v[12:13]\n"
"s_nop 1\n"
"s_and_b32 s74, vcc_lo, 0xf0\n"
"s_cmp_eq_u32 s74, 0xf0\n"
"s_cbranch_scc1 LHB_back_6\n"
"s_sleep 1\n"
"s_sub_u32 s75, s75, 1\n"
"s_cmp_lg_u32 s75, 0\n"
"s_cbranch_scc1 LHB_slow_5l\n"
"s_branch LHB_back_6\n"
"LHB_slow_7:\n"
"s_mov_b32 s75, 131072\n"
"LHB_slow_7l:\n"
"buffer_load_dwordx2 v[12:13], v11, s[32:35], 0 offen sc1\n"
"s_waitcnt vmcnt(0)\n"
"v_cmp_eq_u64 vcc, s[40:41], v[12:13]\n"
"s_nop 1\n"
"s_and_b32 s74, vcc_lo, 0xf0000\n"
"s_cmp_eq_u32 s74, 0xf0000\n"
"s_cbranch_scc1 LHB_back_8\n"
"s_sleep 1\n"
"s_sub_u32 s75, s75, 1\n"
"s_cmp_lg_u32 s75, 0\n"
"s_cbranch_scc1 LHB_slow_7l\n"
"s_branch LHB_back_8\n"
"LHB_slow_9:\n"
"s_mov_b32 s75, 131072\n"
"LHB_slow_9l:\n"
"buffer_load_dwordx2 v[12:13], v11, s[32:35], 0 offen sc1\n"
"s_waitcnt vmcnt(0)\n"
"v_cmp_eq_u64 vcc, s[40:41], v[12:13]\n"
"s_nop 1\n"
"s_and_b32 s74, vcc_lo, 0xf00\n"
"s_cmp_eq_u32 s74, 0xf00\n"
"s_cbranch_scc1 LHB_back_10\n"
"s_sleep 1\n"
"s_sub_u32 s75, s75, 1\n"
"s_cmp_lg_u32 s75, 0\n"
"s_cbranch_scc1 LHB_slow_9l\n"
"s_branch LHB_back_10\n"
"LBCB:\n"
"s_barrier\n"
"ds_read_b128 v[128:131], v96 offset:0\n"
"ds_read_b64_tr_b16 v[16:17], v1 offset:0\n"
"ds_read_b64_tr_b16 v[18:19], v1 offset:512\n"
"ds_read_b64_tr_b16 v[20:21], v2 offset:0\n"
"ds_read_b64_tr_b16 v[22:23], v2 offset:512\n"
"ds_read_b64_tr_b16 v[24:25], v1 offset:8192\n"
"ds_read_b64_tr_b16 v[26:27], v1 offset:8704\n"
"ds_read_b64_tr_b16 v[28:29], v2 offset:8192\n"
"ds_read_b64_tr_b16 v[30:31], v2 offset:8704\n"
"s_waitcnt lgkmcnt(6)\n"
"v_mfma_f32_32x32x16_bf16 v[32:47], v[16:19], v[128:131], 0\n"
"ds_read_b128 v[132:135], v97 offset:0\n"
"ds_read_b64_tr_b16 v[16:17], v1 offset:2048\n"
"ds_read_b64_tr_b16 v[18:19], v1 offset:2560\n"
"s_waitcnt lgkmcnt(7)\n"
"v_mfma_f32_32x32x16_bf16 v[64:79], v[20:23], v[128:131], 0\n"
"ds_read_b64_tr_b16 v[20:21], v2 offset:2048\n"
"ds_read_b64_tr_b16 v[22:23], v2 offset:2560\n"
"s_waitcnt lgkmcnt(7)\n"
"v_mfma_f32_32x32x16_bf16 v[48:63], v[24:27], v[128:131], 0\n"
"ds_read_b64_tr_b16 v[24:25], v1 offset:10240\n"
"ds_read_b64_tr_b16 v[26:27], v1 offset:10752\n"
"s_waitcnt lgkmcnt(7)\n"
"v_mfma_f32_32x32x16_bf16 v[80:95], v[28:31], v[128:131], 0\n"
"ds_read_b64_tr_b16 v[28:29], v2 offset:10240\n"
"ds_read_b64_tr_b16 v[30:31], v2 offset:10752\n"
"s_waitcnt lgkmcnt(6)\n"
"v_mfma_f32_32x32x16_bf16 v[32:47], v[16:19], v[132:135], v[32:47]\n"
"ds_read_b128 v[136:139], v98 offset:0\n"
"ds_read_b64_tr_b16 v[16:17], v1 offset:4096\n"
"ds_read_b64_tr_b16 v[18:19], v1 offset:4608\n"
"s_waitcnt lgkmcnt(7)\n"
"v_mfma_f32_32x32x16_bf16 v[64:79], v[20:23], v[132:135], v[64:79]\n"
"ds_read_b64_tr_b16 v[20:21], v2 offset:4096\n"
"ds_read_b64_tr_b16 v[22:23], v2 offset:4608\n"
"s_waitcnt lgkmcnt(7)\n"
"v_mfma_f32_32x32x16_bf16 v[48:63], v[24:27], v[132:135], v[48:63]\n"
"ds_read_b64_tr_b16 v[24:25], v1 offset:12288\n"
"ds_read_b64_tr_b16 v[26:27], v1 offset:12800\n"
"s_waitcnt lgkmcnt(7)\n"
"v_mfma_f32_32x32x16_bf16 v[80:95], v[28:31], v[132:135], v[80:95]\n"
"ds_read_b64_tr_b16 v[28:29], v2 offset:12288\n"
"LPAD11:\n"
"s_cbranch_execz LPAD14\n"
"ds_read_b64_tr_b16 v[30:31], v2 offset:12800\n"
"s_waitcnt lgkmcnt(6)\n"
"v_mfma_f32_32x32x16_bf16 v[32:47], v[16:19], v[136:139], v[32:47]\n"
"ds_read_b128 v[140:143], v99 offset:0\n"
"ds_read_b64_tr_b16 v[16:17], v1 offset:6144\n"
"ds_read_b64_tr_b16 v[18:19], v1 offset:6656\n"
"s_waitcnt lgkmcnt(7)\n"
"v_mfma_f32_32x32x16_bf16 v[64:79], v[20:23], v[136:139], v[64:79]\n"
"ds_read_b64_tr_b16 v[20:21], v2 offset:6144\n"
"ds_read_b64_tr_b16 v[22:23], v2 offset:6656\n"
"s_waitcnt lgkmcnt(7)\n"
"v_mfma_f32_32x32x16_bf16 v[48:63], v[24:27], v[136:139], v[48:63]\n"
"ds_read_b64_tr_b16 v[24:25], v1 offset:14336\n"
"ds_read_b64_tr_b16 v[26:27], v1 offset:14848\n"
"s_waitcnt lgkmcnt(7)\n"
"v_mfma_f32_32x32x16_bf16 v[80:95], v[28:31], v[136:139], v[80:95]\n"
"ds_read_b64_tr_b16 v[28:29], v2 offset:14336\n"
"ds_read_b64_tr_b16 v[30:31], v2 offset:14848\n"
"s_waitcnt lgkmcnt(6)\n"
"v_mfma_f32_32x32x16_bf16 v[32:47], v[16:19], v[140:143], v[32:47]\n"
"s_barrier\n"
"ds_read_b128 v[144:147], v96 offset:4096\n"
"ds_read_b64_tr_b16 v[16:17], v1 offset:16384\n"
"ds_read_b64_tr_b16 v[18:19], v1 offset:16896\n"
"s_waitcnt lgkmcnt(7)\n"
"v_mfma_f32_32x32x16_bf16 v[64:79], v[20:23], v[140:143], v[64:79]\n"
"ds_read_b64_tr_b16 v[20:21], v2 offset:16384\n"
"ds_read_b64_tr_b16 v[22:23], v2 offset:16896\n"
"s_waitcnt lgkmcnt(7)\n"
"v_mfma_f32_32x32x16_bf16 v[48:63], v[24:27], v[140:143], v[48:63]\n"
"ds_read_b64_tr_b16 v[24:25], v1 offset:24576\n"
"ds_read_b64_tr_b16 v[26:27], v1 offset:25088\n"
"s_waitcnt lgkmcnt(7)\n"
"v_mfma_f32_32x32x16_bf16 v[80:95], v[28:31], v[140:143], v[80:95]\n"
"ds_read_b64_tr_b16 v[28:29], v2 offset:24576\n"
"ds_read_b64_tr_b16 v[30:31], v2 offset:25088\n"
"s_waitcnt lgkmcnt(6)\n"
"v_mfma_f32_32x32x16_bf16 v[32:47], v[16:19], v[144:147], v[32:47]\n"
"ds_read_b128 v[148:151], v97 offset:4096\n"
"ds_read_b64_tr_b16 v[16:17], v1 offset:18432\n"
"ds_read_b64_tr_b16 v[18:19], v1 offset:18944\n"
"s_waitcnt lgkmcnt(7)\n"
"v_mfma_f32_32x32x16_bf16 v[64:79], v[20:23], v[144:147], v[64:79]\n"
"ds_read_b64_tr_b16 v[20:21], v2 offset:18432\n"
"ds_read_b64_tr_b16 v[22:23], v2 offset:18944\n"
"s_waitcnt lgkmcnt(7)\n"
"v_mfma_f32_32x32x16_bf16 v[48:63], v[24:27], v[144:147], v[48:63]\n"
"ds_read_b64_tr_b16 v[24:25], v1 offset:26624\n"
"ds_read_b64_tr_b16 v[26:27], v1 offset:27136\n"
"s_waitcnt lgkmcnt(7)\n"
"v_mfma_f32_32x32x16_bf16 v[80:95], v[28:31], v[144:147], v[80:95]\n"
"ds_read_b64_tr_b16 v[28:29], v2 offset:26624\n"
"ds_read_b64_tr_b16 v[30:31], v2 offset:27136\n"
"s_waitcnt lgkmcnt(6)\n"
"v_mfma_f32_32x32x16_bf16 v[32:47], v[16:19], v[148:151], v[32:47]\n"
"ds_read_b128 v[152:155], v98 offset:4096\n"
"ds_read_b64_tr_b16 v[16:17], v1 offset:20480\n"
"ds_read_b64_tr_b16 v[18:19], v1 offset:20992\n"
"s_waitcnt lgkmcnt(7)\n"
"v_mfma_f32_32x32x16_bf16 v[64:79], v[20:23], v[148:151], v[64:79]\n"
"ds_read_b64_tr_b16 v[20:21], v2 offset:20480\n"
"ds_read_b64_tr_b16 v[22:23], v2 offset:20992\n"
"s_waitcnt lgkmcnt(7)\n"
"v_mfma_f32_32x32x16_bf16 v[48:63], v[24:27], v[148:151], v[48:63]\n"
"ds_read_b64_tr_b16 v[24:25], v1 offset:28672\n"
"ds_read_b64_tr_b16 v[26:27], v1 offset:29184\n"
"s_waitcnt lgkmcnt(7)\n"
"v_mfma_f32_32x32x16_bf16 v[80:95], v[28:31], v[148:151], v[80:95]\n"
"ds_read_b64_tr_b16 v[28:29], v2 offset:28672\n"
"ds_read_b64_tr_b16 v[30:31], v2 offset:29184\n"
"s_waitcnt lgkmcnt(6)\n"
"v_mfma_f32_32x32x16_bf16 v[32:47], v[16:19], v[152:155], v[32:47]\n"
"ds_read_b128 v[156:159], v99 offset:4096\n"
"ds_read_b64_tr_b16 v[16:17], v1 offset:22528\n"
"ds_read_b64_tr_b16 v[18:19], v1 offset:23040\n"
"s_waitcnt lgkmcnt(7)\n"
"v_mfma_f32_32x32x16_bf16 v[64:79], v[20:23], v[152:155], v[64:79]\n"
"ds_read_b64_tr_b16 v[20:21], v2 offset:22528\n"
"ds_read_b64_tr_b16 v[22:23], v2 offset:23040\n"
"s_waitcnt lgkmcnt(7)\n"
"v_mfma_f32_32x32x16_bf16 v[48:63], v[24:27], v[152:155], v[48:63]\n"
"ds_read_b64_tr_b16 v[24:25], v1 offset:30720\n"
"ds_read_b64_tr_b16 v[26:27], v1 offset:31232\n"
"s_waitcnt lgkmcnt(7)\n"
"v_mfma_f32_32x32x16_bf16 v[80:95], v[28:31], v[152:155], v[80:95]\n"
"ds_read_b64_tr_b16 v[28:29], v2 offset:30720\n"
"ds_read_b64_tr_b16 v[30:31], v2 offset:31232\n"
"s_waitcnt lgkmcnt(6)\n"
"v_mfma_f32_32x32x16_bf16 v[32:47], v[16:19], v[156:159], v[32:47]\n"
"s_barrier\n"
"ds_read_b128 v[160:163], v96 offset:8192\n"
"ds_read_b64_tr_b16 v[16:17], v1 offset:32768\n"
"ds_read_b64_tr_b16 v[18:19], v1 offset:33280\n"
"s_waitcnt lgkmcnt(7)\n"
"v_mfma_f32_32x32x16_bf16 v[64:79], v[20:23], v[156:159], v[64:79]\n"
"ds_read_b64_tr_b16 v[20:21], v2 offset:32768\n"
"ds_read_b64_tr_b16 v[22:23], v2 offset:33280\n"
"s_waitcnt lgkmcnt(7)\n"
"v_mfma_f32_32x32x16_bf16 v[48:63], v[24:27], v[156:159], v[48:63]\n"
"ds_read_b64_tr_b16 v[24:25], v1 offset:40960\n"
"ds_read_b64_tr_b16 v[26:27], v1 offset:41472\n"
"s_waitcnt lgkmcnt(7)\n"
"v_mfma_f32_32x32x16_bf16 v[80:95], v[28:31], v[156:159], v[80:95]\n"
"ds_read_b64_tr_b16 v[28:29], v2 offset:40960\n"
"ds_read_b64_tr_b16 v[30:31], v2 offset:41472\n"
"s_waitcnt lgkmcnt(6)\n"
"v_mfma_f32_32x32x16_bf16 v[32:47], v[16:19], v[160:163], v[32:47]\n"
"ds_read_b128 v[164:167], v97 offset:8192\n"
"ds_read_b64_tr_b16 v[16:17], v1 offset:34816\n"
"ds_read_b64_tr_b16 v[18:19], v1 offset:35328\n"
"s_waitcnt lgkmcnt(7)\n"
"v_mfma_f32_32x32x16_bf16 v[64:79], v[20:23], v[160:163], v[64:79]\n"
"ds_read_b64_tr_b16 v[20:21], v2 offset:34816\n"
"ds_read_b64_tr_b16 v[22:23], v2 offset:35328\n"
"s_waitcnt lgkmcnt(7)\n"
"v_mfma_f32_32x32x16_bf16 v[48:63], v[24:27], v[160:163], v[48:63]\n"
"ds_read_b64_tr_b16 v[24:25], v1 offset:43008\n"
"ds_read_b64_tr_b16 v[26:27], v1 offset:43520\n"
"s_waitcnt lgkmcnt(7)\n"
"v_mfma_f32_32x32x16_bf16 v[80:95], v[28:31], v[160:163], v[80:95]\n"
"ds_read_b64_tr_b16 v[28:29], v2 offset:43008\n"
"ds_read_b64_tr_b16 v[30:31], v2 offset:43520\n"
"s_waitcnt lgkmcnt(6)\n"
"v_mfma_f32_32x32x16_bf16 v[32:47], v[16:19], v[164:167], v[32:47]\n"
"ds_read_b128 v[168:171], v98 offset:8192\n"
"ds_read_b64_tr_b16 v[16:17], v1 offset:36864\n"
"ds_read_b64_tr_b16 v[18:19], v1 offset:37376\n"
"s_waitcnt lgkmcnt(7)\n"
"v_mfma_f32_32x32x16_bf16 v[64:79], v[20:23], v[164:167], v[64:79]\n"
"ds_read_b64_tr_b16 v[20:21], v2 offset:36864\n"
"ds_read_b64_tr_b16 v[22:23], v2 offset:37376\n"
"s_waitcnt lgkmcnt(7)\n"
"v_mfma_f32_32x32x16_bf16 v[48:63], v[24:27], v[164:167], v[48:63]\n"
"ds_read_b64_tr_b16 v[24:25], v1 offset:45056\n"
"ds_read_b64_tr_b16 v[26:27], v1 offset:45568\n"
"s_waitcnt lgkmcnt(7)\n"
"v_mfma_f32_32x32x16_bf16 v[80:95], v[28:31], v[164:167], v[80:95]\n"
"ds_read_b64_tr_b16 v[28:29], v2 offset:45056\n"
"ds_read_b64_tr_b16 v[30:31], v2 offset:45568\n"
"s_waitcnt lgkmcnt(6)\n"
"v_mfma_f32_32x32x16_bf16 v[32:47], v[16:19], v[168:171], v[32:47]\n"
"ds_read_b128 v[172:175], v99 offset:8192\n"
"ds_read_b64_tr_b16 v[16:17], v1 offset:38912\n"
"ds_read_b64_tr_b16 v[18:19], v1 offset:39424\n"
"s_waitcnt lgkmcnt(7)\n"
"v_mfma_f32_32x32x16_bf16 v[64:79], v[20:23], v[168:171], v[64:79]\n"
"ds_read_b64_tr_b16 v[20:21], v2 offset:38912\n"
"ds_read_b64_tr_b16 v[22:23], v2 offset:39424\n"
"s_waitcnt lgkmcnt(7)\n"
"v_mfma_f32_32x32x16_bf16 v[48:63], v[24:27], v[168:171], v[48:63]\n"
"ds_read_b64_tr_b16 v[24:25], v1 offset:47104\n"
"ds_read_b64_tr_b16 v[26:27], v1 offset:47616\n"
"s_waitcnt lgkmcnt(7)\n"
"v_mfma_f32_32x32x16_bf16 v[80:95], v[28:31], v[168:171], v[80:95]\n"
"ds_read_b64_tr_b16 v[28:29], v2 offset:47104\n"
"ds_read_b64_tr_b16 v[30:31], v2 offset:47616\n"
"s_waitcnt lgkmcnt(6)\n"
"v_mfma_f32_32x32x16_bf16 v[32:47], v[16:19], v[172:175], v[32:47]\n"
"s_barrier\n"
"ds_read_b128 v[176:179], v96 offset:12288\n"
"ds_read_b64_tr_b16 v[16:17], v1 offset:49152\n"
"ds_read_b64_tr_b16 v[18:19], v1 offset:49664\n"
"s_waitcnt lgkmcnt(7)\n"
"v_mfma_f32_32x32x16_bf16 v[64:79], v[20:23], v[172:175], v[64:79]\n"
"ds_read_b64_tr_b16 v[20:21], v2 offset:49152\n"
"ds_read_b64_tr_b16 v[22:23], v2 offset:49664\n"
"s_waitcnt lgkmcnt(7)\n"
"v_mfma_f32_32x32x16_bf16 v[48:63], v[24:27], v[172:175], v[48:63]\n"
"ds_read_b64_tr_b16 v[24:25], v1 offset:57344\n"
"ds_read_b64_tr_b16 v[26:27], v1 offset:57856\n"
"s_waitcnt lgkmcnt(7)\n"
"v_mfma_f32_32x32x16_bf16 v[80:95], v[28:31], v[172:175], v[80:95]\n"
"ds_read_b64_tr_b16 v[28:29], v2 offset:57344\n"
"ds_read_b64_tr_b16 v[30:31], v2 offset:57856\n"
"s_waitcnt lgkmcnt(6)\n"
"v_mfma_f32_32x32x16_bf16 v[32:47], v[16:19], v[176:179], v[32:47]\n"
"ds_read_b128 v[180:183], v97 offset:12288\n"
"ds_read_b64_tr_b16 v[16:17], v1 offset:51200\n"
"ds_read_b64_tr_b16 v[18:19], v1 offset:51712\n"
"s_waitcnt lgkmcnt(7)\n"
"v_mfma_f32_32x32x16_bf16 v[64:79], v[20:23], v[176:179], v[64:79]\n"
"ds_read_b64_tr_b16 v[20:21], v2 offset:51200\n"
"ds_read_b64_tr_b16 v[22:23], v2 offset:51712\n"
"s_waitcnt lgkmcnt(7)\n"
"v_mfma_f32_32x32x16_bf16 v[48:63], v[24:27], v[176:179], v[48:63]\n"
"ds_read_b64_tr_b16 v[24:25], v1 offset:59392\n"
"ds_read_b64_tr_b16 v[26:27], v1 offset:59904\n"
"s_waitcnt lgkmcnt(7)\n"
"v_mfma_f32_32x32x16_bf16 v[80:95], v[28:31], v[176:179], v[80:95]\n"
"ds_read_b64_tr_b16 v[28:29], v2 offset:59392\n"
"ds_read_b64_tr_b16 v[30:31], v2 offset:59904\n"
"s_waitcnt lgkmcnt(6)\n"
"v_mfma_f32_32x32x16_bf16 v[32:47], v[16:19], v[180:183], v[32:47]\n"
"ds_read_b128 v[184:187], v98 offset:12288\n"
"ds_read_b64_tr_b16 v[16:17], v1 offset:53248\n"
"ds_read_b64_tr_b16 v[18:19], v1 offset:53760\n"
"s_waitcnt lgkmcnt(7)\n"
"v_mfma_f32_32x32x16_bf16 v[64:79], v[20:23], v[180:183], v[64:79]\n"
"ds_read_b64_tr_b16 v[20:21], v2 offset:53248\n"
"ds_read_b64_tr_b16 v[22:23], v2 offset:53760\n"
"s_waitcnt lgkmcnt(7)\n"
"v_mfma_f32_32x32x16_bf16 v[48:63], v[24:27], v[180:183], v[48:63]\n"
"ds_read_b64_tr_b16 v[24:25], v1 offset:61440\n"
"ds_read_b64_tr_b16 v[26:27], v1 offset:61952\n"
"s_waitcnt lgkmcnt(7)\n"
"v_mfma_f32_32x32x16_bf16 v[80:95], v[28:31], v[180:183], v[80:95]\n"
"ds_read_b64_tr_b16 v[28:29], v2 offset:61440\n"
"ds_read_b64_tr_b16 v[30:31], v2 offset:61952\n"
"s_waitcnt lgkmcnt(6)\n"
"v_mfma_f32_32x32x16_bf16 v[32:47], v[16:19], v[184:187], v[32:47]\n"
"ds_read_b128 v[188:191], v99 offset:12288\n"
"ds_read_b64_tr_b16 v[16:17], v1 offset:55296\n"
"ds_read_b64_tr_b16 v[18:19], v1 offset:55808\n"
"s_waitcnt lgkmcnt(7)\n"
"v_mfma_f32_32x32x16_bf16 v[64:79], v[20:23], v[184:187], v[64:79]\n"
"ds_read_b64_tr_b16 v[20:21], v2 offset:55296\n"
"ds_read_b64_tr_b16 v[22:23], v2 offset:55808\n"
"s_waitcnt lgkmcnt(7)\n"
"v_mfma_f32_32x32x16_bf16 v[48:63], v[24:27], v[184:187], v[48:63]\n"
"ds_read_b64_tr_b16 v[24:25], v1 offset:63488\n"
"ds_read_b64_tr_b16 v[26:27], v1 offset:64000\n"
"s_waitcnt lgkmcnt(7)\n"
"v_mfma_f32_32x32x16_bf16 v[80:95], v[28:31], v[184:187], v[80:95]\n"
"ds_read_b64_tr_b16 v[28:29], v2 offset:63488\n"
"ds_read_b64_tr_b16 v[30:31], v2 offset:64000\n"
"s_waitcnt lgkmcnt(6)\n"
"v_mfma_f32_32x32x16_bf16 v[32:47], v[16:19], v[188:191], v[32:47]\n"
"s_barrier\n"
"ds_read_b128 v[192:195], v96 offset:0\n"
"ds_read_b64_tr_b16 v[16:17], v3 offset:0\n"
"ds_read_b64_tr_b16 v[18:19], v3 offset:512\n"
"s_waitcnt lgkmcnt(7)\n"
"v_mfma_f32_32x32x16_bf16 v[64:79], v[20:23], v[188:191], v[64:79]\n"
"ds_read_b64_tr_b16 v[20:21], v4 offset:0\n"
"ds_read_b64_tr_b16 v[22:23], v4 offset:512\n"
"s_waitcnt lgkmcnt(7)\n"
"v_mfma_f32_32x32x16_bf16 v[48:63], v[24:27], v[188:191], v[48:63]\n"
"ds_read_b64_tr_b16 v[24:25], v3 offset:8192\n"
"ds_read_b64_tr_b16 v[26:27], v3 offset:8704\n"
"s_waitcnt lgkmcnt(7)\n"
"v_mfma_f32_32x32x16_bf16 v[80:95], v[28:31], v[188:191], v[80:95]\n"
"ds_read_b64_tr_b16 v[28:29], v4 offset:8192\n"
"ds_read_b64_tr_b16 v[30:31], v4 offset:8704\n"
"s_waitcnt lgkmcnt(6)\n"
"v_mfma_f32_32x32x16_bf16 v[32:47], v[16:19], v[192:195], v[32:47]\n"
"ds_read_b128 v[196:199], v97 offset:0\n"
"ds_read_b64_tr_b16 v[16:17], v3 offset:2048\n"
"ds_read_b64_tr_b16 v[18:19], v3 offset:2560\n"
"s_waitcnt lgkmcnt(7)\n"
"v_mfma_f32_32x32x16_bf16 v[64:79], v[20:23], v[192:195], v[64:79]\n"
"ds_read_b64_tr_b16 v[20:21], v4 offset:2048\n"
"ds_read_b64_tr_b16 v[22:23], v4 offset:2560\n"
"s_waitcnt lgkmcnt(7)\n"
"v_mfma_f32_32x32x16_bf16 v[48:63], v[24:27], v[192:195], v[48:63]\n"
"ds_read_b64_tr_b16 v[24:25], v3 offset:10240\n"
"ds_read_b64_tr_b16 v[26:27], v3 offset:10752\n"
"s_waitcnt lgkmcnt(7)\n"
"v_mfma_f32_32x32x16_bf16 v[80:95], v[28:31], v[192:195], v[80:95]\n"
"ds_read_b64_tr_b16 v[28:29], v4 offset:10240\n"
"ds_read_b64_tr_b16 v[30:31], v4 offset:10752\n"
"s_waitcnt lgkmcnt(6)\n"
"v_mfma_f32_32x32x16_bf16 v[32:47], v[16:19], v[196:199], v[32:47]\n"
"ds_read_b128 v[200:203], v98 offset:0\n"
"ds_read_b64_tr_b16 v[16:17], v3 offset:4096\n"
"ds_read_b64_tr_b16 v[18:19], v3 offset:4608\n"
"s_waitcnt lgkmcnt(7)\n"
"v_mfma_f32_32x32x16_bf16 v[64:79], v[20:23], v[196:199], v[64:79]\n"
"ds_read_b64_tr_b16 v[20:21], v4 offset:4096\n"
"ds_read_b64_tr_b16 v[22:23], v4 offset:4608\n"
"s_waitcnt lgkmcnt(7)\n"
"v_mfma_f32_32x32x16_bf16 v[48:63], v[24:27], v[196:199], v[48:63]\n"
"ds_read_b64_tr_b16 v[24:25], v3 offset:12288\n"
"ds_read_b64_tr_b16 v[26:27], v3 offset:12800\n"
"s_waitcnt lgkmcnt(7)\n"
"v_mfma_f32_32x32x16_bf16 v[80:95], v[28:31], v[196:199], v[80:95]\n"
"ds_read_b64_tr_b16 v[28:29], v4 offset:12288\n"
"ds_read_b64_tr_b16 v[30:31], v4 offset:12800\n"
"s_waitcnt lgkmcnt(6)\n"
"v_mfma_f32_32x32x16_bf16 v[32:47], v[16:19], v[200:203], v[32:47]\n"
"ds_read_b128 v[204:207], v99 offset:0\n"
"ds_read_b64_tr_b16 v[16:17], v3 offset:6144\n"
"ds_read_b64_tr_b16 v[18:19], v3 offset:6656\n"
"s_waitcnt lgkmcnt(7)\n"
"v_mfma_f32_32x32x16_bf16 v[64:79], v[20:23], v[200:203], v[64:79]\n"
"ds_read_b64_tr_b16 v[20:21], v4 offset:6144\n"
"ds_read_b64_tr_b16 v[22:23], v4 offset:6656\n"
"s_waitcnt lgkmcnt(7)\n"
"v_mfma_f32_32x32x16_bf16 v[48:63], v[24:27], v[200:203], v[48:63]\n"
"ds_read_b64_tr_b16 v[24:25], v3 offset:14336\n"
"ds_read_b64_tr_b16 v[26:27], v3 offset:14848\n"
"s_waitcnt lgkmcnt(7)\n"
"v_mfma_f32_32x32x16_bf16 v[80:95], v[28:31], v[200:203], v[80:95]\n"
"ds_read_b64_tr_b16 v[28:29], v4 offset:14336\n"
"ds_read_b64_tr_b16 v[30:31], v4 offset:14848\n"
"s_waitcnt lgkmcnt(6)\n"
"v_mfma_f32_32x32x16_bf16 v[32:47], v[16:19], v[204:207], v[32:47]\n"
"s_barrier\n"
"ds_read_b128 v[208:211], v96 offset:4096\n"
"ds_read_b64_tr_b16 v[16:17], v1 offset:0\n"
"ds_read_b64_tr_b16 v[18:19], v1 offset:512\n"
"s_waitcnt lgkmcnt(7)\n"
"v_mfma_f32_32x32x16_bf16 v[64:79], v[20:23], v[204:207], v[64:79]\n"
"ds_read_b64_tr_b16 v[20:21], v2 offset:0\n"
"ds_read_b64_tr_b16 v[22:23], v2 offset:512\n"
"s_waitcnt lgkmcnt(7)\n"
"v_mfma_f32_32x32x16_bf16 v[48:63], v[24:27], v[204:207], v[48:63]\n"
"ds_read_b64_tr_b16 v[24:25], v1 offset:8192\n"
"ds_read_b64_tr_b16 v[26:27], v1 offset:8704\n"
"s_waitcnt lgkmcnt(7)\n"
"v_mfma_f32_32x32x16_bf16 v[80:95], v[28:31], v[204:207], v[80:95]\n"
"ds_read_b64_tr_b16 v[28:29], v2 offset:8192\n"
"ds_read_b64_tr_b16 v[30:31], v2 offset:8704\n"
"s_waitcnt lgkmcnt(6)\n"
"v_mfma_f32_32x32x16_bf16 v[32:47], v[16:19], v[208:211], v[32:47]\n"
"ds_read_b128 v[212:215], v97 offset:4096\n"
"ds_read_b64_tr_b16 v[16:17], v1 offset:2048\n"
"ds_read_b64_tr_b16 v[18:19], v1 offset:2560\n"
"s_waitcnt lgkmcnt(7)\n"
"v_mfma_f32_32x32x16_bf16 v[64:79], v[20:23], v[208:211], v[64:79]\n"
"ds_read_b64_tr_b16 v[20:21], v2 offset:2048\n"
"ds_read_b64_tr_b16 v[22:23], v2 offset:2560\n"
"s_waitcnt lgkmcnt(7)\n"
"v_mfma_f32_32x32x16_bf16 v[48:63], v[24:27], v[208:211], v[48:63]\n"
"ds_read_b64_tr_b16 v[24:25], v1 offset:10240\n"
"ds_read_b64_tr_b16 v[26:27], v1 offset:10752\n"
"s_waitcnt lgkmcnt(7)\n"
"v_mfma_f32_32x32x16_bf16 v[80:95], v[28:31], v[208:211], v[80:95]\n"
"ds_read_b64_tr_b16 v[28:29], v2 offset:10240\n"
"ds_read_b64_tr_b16 v[30:31], v2 offset:10752\n"
"s_waitcnt lgkmcnt(6)\n"
"v_mfma_f32_32x32x16_bf16 v[32:47], v[16:19], v[212:215], v[32:47]\n"
"ds_read_b128 v[216:219], v98 offset:4096\n"
"ds_read_b64_tr_b16 v[16:17], v1 offset:4096\n"
"ds_read_b64_tr_b16 v[18:19], v1 offset:4608\n"
"s_waitcnt lgkmcnt(7)\n"
"v_mfma_f32_32x32x16_bf16 v[64:79], v[20:23], v[212:215], v[64:79]\n"
"ds_read_b64_tr_b16 v[20:21], v2 offset:4096\n"
"ds_read_b64_tr_b16 v[22:23], v2 offset:4608\n"
"s_waitcnt lgkmcnt(7)\n"
"v_mfma_f32_32x32x16_bf16 v[48:63], v[24:27], v[212:215], v[48:63]\n"
"ds_read_b64_tr_b16 v[24:25], v1 offset:12288\n"
"ds_read_b64_tr_b16 v[26:27], v1 offset:12800\n"
"s_waitcnt lgkmcnt(7)\n"
"v_mfma_f32_32x32x16_bf16 v[80:95], v[28:31], v[212:215], v[80:95]\n"
"ds_read_b64_tr_b16 v[28:29], v2 offset:12288\n"
"ds_read_b64_tr_b16 v[30:31], v2 offset:12800\n"
"s_waitcnt lgkmcnt(6)\n"
"v_mfma_f32_32x32x16_bf16 v[32:47], v[16:19], v[216:219], v[32:47]\n"
"ds_read_b128 v[220:223], v99 offset:4096\n"
"ds_read_b64_tr_b16 v[16:17], v1 offset:6144\n"
"ds_read_b64_tr_b16 v[18:19], v1 offset:6656\n"
"s_waitcnt lgkmcnt(7)\n"
"v_mfma_f32_32x32x16_bf16 v[64:79], v[20:23], v[216:219], v[64:79]\n"
"ds_read_b64_tr_b16 v[20:21], v2 offset:6144\n"
"ds_read_b64_tr_b16 v[22:23], v2 offset:6656\n"
"s_waitcnt lgkmcnt(7)\n"
"v_mfma_f32_32x32x16_bf16 v[48:63], v[24:27], v[216:219], v[48:63]\n"
"ds_read_b64_tr_b16 v[24:25], v1 offset:14336\n"
"ds_read_b64_tr_b16 v[26:27], v1 offset:14848\n"
"s_waitcnt lgkmcnt(7)\n"
"v_mfma_f32_32x32x16_bf16 v[80:95], v[28:31], v[216:219], v[80:95]\n"
"ds_read_b64_tr_b16 v[28:29], v2 offset:14336\n"
"ds_read_b64_tr_b16 v[30:31], v2 offset:14848\n"
"s_waitcnt lgkmcnt(6)\n"
"v_mfma_f32_32x32x16_bf16 v[32:47], v[16:19], v[220:223], v[32:47]\n"
"s_barrier\n"
"ds_read_b128 v[224:227], v96 offset:8192\n"
"ds_read_b64_tr_b16 v[16:17], v1 offset:16384\n"
"ds_read_b64_tr_b16 v[18:19], v1 offset:16896\n"
"s_waitcnt lgkmcnt(7)\n"
"v_mfma_f32_32x32x16_bf16 v[64:79], v[20:23], v[220:223], v[64:79]\n"
"ds_read_b64_tr_b16 v[20:21], v2 offset:16384\n"
"ds_read_b64_tr_b16 v[22:23], v2 offset:16896\n"
"s_waitcnt lgkmcnt(7)\n"
"v_mfma_f32_32x32x16_bf16 v[48:63], v[24:27], v[220:223], v[48:63]\n"
"ds_read_b64_tr_b16 v[24:25], v1 offset:24576\n"
"ds_read_b64_tr_b16 v[26:27], v1 offset:25088\n"
"s_waitcnt lgkmcnt(7)\n"
"v_mfma_f32_32x32x16_bf16 v[80:95], v[28:31], v[220:223], v[80:95]\n"
"ds_read_b64_tr_b16 v[28:29], v2 offset:24576\n"
"ds_read_b64_tr_b16 v[30:31], v2 offset:25088\n"
"s_waitcnt lgkmcnt(6)\n"
"v_mfma_f32_32x32x16_bf16 v[32:47], v[16:19], v[224:227], v[32:47]\n"
"ds_read_b128 v[228:231], v97 offset:8192\n"
"ds_read_b64_tr_b16 v[16:17], v1 offset:18432\n"
"ds_read_b64_tr_b16 v[18:19], v1 offset:18944\n"
"s_waitcnt lgkmcnt(7)\n"
"v_mfma_f32_32x32x16_bf16 v[64:79], v[20:23], v[224:227], v[64:79]\n"
"ds_read_b64_tr_b16 v[20:21], v2 offset:18432\n"
"ds_read_b64_tr_b16 v[22:23], v2 offset:18944\n"
"s_waitcnt lgkmcnt(7)\n"
"v_mfma_f32_32x32x16_bf16 v[48:63], v[24:27], v[224:227], v[48:63]\n"
"ds_read_b64_tr_b16 v[24:25], v1 offset:26624\n"
"ds_read_b64_tr_b16 v[26:27], v1 offset:27136\n"
"s_waitcnt lgkmcnt(7)\n"
"v_mfma_f32_32x32x16_bf16 v[80:95], v[28:31], v[224:227], v[80:95]\n"
"ds_read_b64_tr_b16 v[28:29], v2 offset:26624\n"
"ds_read_b64_tr_b16 v[30:31], v2 offset:27136\n"
"s_waitcnt lgkmcnt(6)\n"
"v_mfma_f32_32x32x16_bf16 v[32:47], v[16:19], v[228:231], v[32:47]\n"
"ds_read_b128 v[232:235], v98 offset:8192\n"
"ds_read_b64_tr_b16 v[16:17], v1 offset:20480\n"
"ds_read_b64_tr_b16 v[18:19], v1 offset:20992\n"
"s_waitcnt lgkmcnt(7)\n"
"v_mfma_f32_32x32x16_bf16 v[64:79], v[20:23], v[228:231], v[64:79]\n"
"ds_read_b64_tr_b16 v[20:21], v2 offset:20480\n"
"ds_read_b64_tr_b16 v[22:23], v2 offset:20992\n"
"s_waitcnt lgkmcnt(7)\n"
"v_mfma_f32_32x32x16_bf16 v[48:63], v[24:27], v[228:231], v[48:63]\n"
"ds_read_b64_tr_b16 v[24:25], v1 offset:28672\n"
"ds_read_b64_tr_b16 v[26:27], v1 offset:29184\n"
"s_waitcnt lgkmcnt(7)\n"
"v_mfma_f32_32x32x16_bf16 v[80:95], v[28:31], v[228:231], v[80:95]\n"
"ds_read_b64_tr_b16 v[28:29], v2 offset:28672\n"
"ds_read_b64_tr_b16 v[30:31], v2 offset:29184\n"
"s_waitcnt lgkmcnt(6)\n"
"v_mfma_f32_32x32x16_bf16 v[32:47], v[16:19], v[232:235], v[32:47]\n"
"ds_read_b128 v[236:239], v99 offset:8192\n"
"ds_read_b64_tr_b16 v[16:17], v1 offset:22528\n"
"ds_read_b64_tr_b16 v[18:19], v1 offset:23040\n"
"s_waitcnt lgkmcnt(7)\n"
"v_mfma_f32_32x32x16_bf16 v[64:79], v[20:23], v[232:235], v[64:79]\n"
"ds_read_b64_tr_b16 v[20:21], v2 offset:22528\n"
"ds_read_b64_tr_b16 v[22:23], v2 offset:23040\n"
"s_waitcnt lgkmcnt(7)\n"
"v_mfma_f32_32x32x16_bf16 v[48:63], v[24:27], v[232:235], v[48:63]\n"
"ds_read_b64_tr_b16 v[24:25], v1 offset:30720\n"
"ds_read_b64_tr_b16 v[26:27], v1 offset:31232\n"
"s_waitcnt lgkmcnt(7)\n"
"v_mfma_f32_32x32x16_bf16 v[80:95], v[28:31], v[232:235], v[80:95]\n"
"ds_read_b64_tr_b16 v[28:29], v2 offset:30720\n"
"ds_read_b64_tr_b16 v[30:31], v2 offset:31232\n"
"s_waitcnt lgkmcnt(6)\n"
"v_mfma_f32_32x32x16_bf16 v[32:47], v[16:19], v[236:239], v[32:47]\n"
"s_barrier\n"
"ds_read_b128 v[240:243], v96 offset:12288\n"
"ds_read_b64_tr_b16 v[16:17], v1 offset:32768\n"
"ds_read_b64_tr_b16 v[18:19], v1 offset:33280\n"
"s_waitcnt lgkmcnt(7)\n"
"v_mfma_f32_32x32x16_bf16 v[64:79], v[20:23], v[236:239], v[64:79]\n"
"ds_read_b64_tr_b16 v[20:21], v1 offset:40960\n"
"ds_read_b64_tr_b16 v[22:23], v1 offset:41472\n"
"s_waitcnt lgkmcnt(7)\n"
"v_mfma_f32_32x32x16_bf16 v[48:63], v[24:27], v[236:239], v[48:63]\n"
"ds_read_b128 v[244:247], v97 offset:12288\n"
"ds_read_b64_tr_b16 v[24:25], v1 offset:34816\n"
"ds_read_b64_tr_b16 v[26:27], v1 offset:35328\n"
"s_waitcnt lgkmcnt(8)\n"
"v_mfma_f32_32x32x16_bf16 v[80:95], v[28:31], v[236:239], v[80:95]\n"
"ds_read_b64_tr_b16 v[28:29], v1 offset:43008\n"
"ds_read_b64_tr_b16 v[30:31], v1 offset:43520\n"
"s_waitcnt lgkmcnt(7)\n"
"v_mfma_f32_32x32x16_bf16 v[32:47], v[16:19], v[240:243], v[32:47]\n"
"ds_read_b128 v[248:251], v98 offset:12288\n"
"ds_read_b64_tr_b16 v[16:17], v1 offset:36864\n"
"ds_read_b64_tr_b16 v[18:19], v1 offset:37376\n"
"s_waitcnt lgkmcnt(8)\n"
"v_mfma_f32_32x32x16_bf16 v[48:63], v[20:23], v[240:243], v[48:63]\n"
"ds_read_b64_tr_b16 v[20:21], v1 offset:45056\n"
"ds_read_b64_tr_b16 v[22:23], v1 offset:45568\n"
"s_waitcnt lgkmcnt(7)\n"
"v_mfma_f32_32x32x16_bf16 v[32:47], v[24:27], v[244:247], v[32:47]\n"
"ds_read_b128 v[252:255], v99 offset:12288\n"
"ds_read_b64_tr_b16 v[24:25], v1 offset:38912\n"
"ds_read_b64_tr_b16 v[26:27], v1 offset:39424\n"
"s_waitcnt lgkmcnt(8)\n"
"v_mfma_f32_32x32x16_bf16 v[48:63], v[28:31], v[244:247], v[48:63]\n"
"ds_read_b64_tr_b16 v[28:29], v1 offset:47104\n"
"ds_read_b64_tr_b16 v[30:31], v1 offset:47616\n"
"s_waitcnt lgkmcnt(7)\n"
"v_mfma_f32_32x32x16_bf16 v[32:47], v[16:19], v[248:251], v[32:47]\n"
"ds_read_b64_tr_b16 v[16:17], v2 offset:32768\n"
"ds_read_b64_tr_b16 v[18:19], v2 offset:33280\n"
"s_waitcnt lgkmcnt(7)\n"
"v_mfma_f32_32x32x16_bf16 v[48:63], v[20:23], v[248:251], v[48:63]\n"
"ds_read_b64_tr_b16 v[20:21], v2 offset:40960\n"
"ds_read_b64_tr_b16 v[22:23], v2 offset:41472\n"
"s_waitcnt lgkmcnt(6)\n"
"v_mfma_f32_32x32x16_bf16 v[32:47], v[24:27], v[252:255], v[32:47]\n"
"ds_read_b64_tr_b16 v[24:25], v2 offset:34816\n"
"ds_read_b64_tr_b16 v[26:27], v2 offset:35328\n"
"s_waitcnt lgkmcnt(6)\n"
"v_mfma_f32_32x32x16_bf16 v[48:63], v[28:31], v[252:255], v[48:63]\n"
"ds_read_b64_tr_b16 v[28:29], v2 offset:43008\n"
"ds_read_b64_tr_b16 v[30:31], v2 offset:43520\n"
"s_waitcnt lgkmcnt(6)\n"
"v_mfma_f32_32x32x16_bf16 v[64:79], v[16:19], v[240:243], v[64:79]\n"
"ds_read_b64_tr_b16 v[16:17], v2 offset:36864\n"
"ds_read_b64_tr_b16 v[18:19], v2 offset:37376\n"
"s_waitcnt lgkmcnt(6)\n"
"v_mfma_f32_32x32x16_bf16 v[80:95], v[20:23], v[240:243], v[80:95]\n"
"ds_read_b64_tr_b16 v[20:21], v2 offset:45056\n"
"ds_read_b64_tr_b16 v[22:23], v2 offset:45568\n"
"s_waitcnt lgkmcnt(6)\n"
"v_mfma_f32_32x32x16_bf16 v[64:79], v[24:27], v[244:247], v[64:79]\n"
"ds_read_b64_tr_b16 v[24:25], v2 offset:38912\n"
"ds_read_b64_tr_b16 v[26:27], v2 offset:39424\n"
"v_mul_f32 v32, v32, v48\n"
"v_mul_f32 v33, v33, v49\n"
"v_mul_f32 v34, v34, v50\n"
"v_mul_f32 v35, v35, v51\n"
"s_waitcnt lgkmcnt(6)\n"
"v_mfma_f32_32x32x16_bf16 v[80:95], v[28:31], v[244:247], v[80:95]\n"
"ds_read_b64_tr_b16 v[28:29], v2 offset:47104\n"
"ds_read_b64_tr_b16 v[30:31], v2 offset:47616\n"
"v_mul_f32 v36, v36, v52\n"
"v_mul_f32 v37, v37, v53\n"
"v_mul_f32 v38, v38, v54\n"
"v_mul_f32 v39, v39, v55\n"
"s_waitcnt lgkmcnt(6)\n"
"v_mfma_f32_32x32x16_bf16 v[64:79], v[16:19], v[248:251], v[64:79]\n"
"s_barrier\n"
"ds_read_b64_tr_b16 v[16:17], v1 offset:49152\n"
"ds_read_b64_tr_b16 v[18:19], v1 offset:49664\n"
"v_cvt_pk_bf16_f32 v32, v32, v33\n"
"v_cvt_pk_bf16_f32 v33, v34, v35\n"
"v_cvt_pk_bf16_f32 v34, v36, v37\n"
"v_cvt_pk_bf16_f32 v35, v38, v39\n"
"s_waitcnt lgkmcnt(6)\n"
"v_mfma_f32_32x32x16_bf16 v[80:95], v[20:23], v[248:251], v[80:95]\n"
"ds_read_b64_tr_b16 v[20:21], v1 offset:57344\n"
"ds_read_b64_tr_b16 v[22:23], v1 offset:57856\n"
"ds_write_b128 v6, v[32:35] offset:8192\n"
"s_waitcnt lgkmcnt(7)\n"
"v_mfma_f32_32x32x16_bf16 v[64:79], v[24:27], v[252:255], v[64:79]\n"
"ds_read_b64_tr_b16 v[24:25], v1 offset:51200\n"
"ds_read_b64_tr_b16 v[26:27], v1 offset:51712\n"
"v_mul_f32 v40, v40, v56\n"
"v_mul_f32 v41, v41, v57\n"
"v_mul_f32 v42, v42, v58\n"
"v_mul_f32 v43, v43, v59\n"
"s_waitcnt lgkmcnt(7)\n"
"v_mfma_f32_32x32x16_bf16 v[80:95], v[28:31], v[252:255], v[80:95]\n"
"ds_read_b64_tr_b16 v[28:29], v1 offset:59392\n"
"ds_read_b64_tr_b16 v[30:31], v1 offset:59904\n"
"v_mul_f32 v44, v44, v60\n"
"v_mul_f32 v45, v45, v61\n"
"v_mul_f32 v46, v46, v62\n"
"v_mul_f32 v47, v47, v63\n"
"s_waitcnt lgkmcnt(7)\n"
"v_mfma_f32_32x32x16_bf16 v[96:111], v[16:19], v[128:131], 0\n"
"ds_read_b64_tr_b16 v[16:17], v1 offset:53248\n"
"ds_read_b64_tr_b16 v[18:19], v1 offset:53760\n"
"v_cvt_pk_bf16_f32 v40, v40, v41\n"
"v_cvt_pk_bf16_f32 v41, v42, v43\n"
"v_cvt_pk_bf16_f32 v42, v44, v45\n"
"v_cvt_pk_bf16_f32 v43, v46, v47\n"
"s_waitcnt lgkmcnt(7)\n"
"v_mfma_f32_32x32x16_bf16 v[112:127], v[20:23], v[128:131], 0\n"
"ds_read_b64_tr_b16 v[20:21], v1 offset:61440\n"
"ds_read_b64_tr_b16 v[22:23], v1 offset:61952\n"
"ds_write_b128 v6, v[40:43] offset:9216\n"
"s_waitcnt lgkmcnt(7)\n"
"v_mfma_f32_32x32x16_bf16 v[96:111], v[24:27], v[132:135], v[96:111]\n"
"ds_read_b64_tr_b16 v[24:25], v1 offset:55296\n"
"ds_read_b64_tr_b16 v[26:27], v1 offset:55808\n"
"v_mul_f32 v64, v64, v80\n"
"v_mul_f32 v65, v65, v81\n"
"v_mul_f32 v66, v66, v82\n"
"v_mul_f32 v67, v67, v83\n"
"s_waitcnt lgkmcnt(7)\n"
"v_mfma_f32_32x32x16_bf16 v[112:127], v[28:31], v[132:135], v[112:127]\n"
"ds_read_b64_tr_b16 v[28:29], v1 offset:63488\n"
"ds_read_b64_tr_b16 v[30:31], v1 offset:64000\n"
"v_mul_f32 v68, v68, v84\n"
"v_mul_f32 v69, v69, v85\n"
"v_mul_f32 v70, v70, v86\n"
"v_mul_f32 v71, v71, v87\n"
"s_waitcnt lgkmcnt(7)\n"
"v_mfma_f32_32x32x16_bf16 v[96:111], v[16:19], v[136:139], v[96:111]\n"
"ds_read_b64_tr_b16 v[16:17], v2 offset:49152\n"
"ds_read_b64_tr_b16 v[18:19], v2 offset:49664\n"
"v_cvt_pk_bf16_f32 v64, v64, v65\n"
"v_cvt_pk_bf16_f32 v65, v66, v67\n"
"v_cvt_pk_bf16_f32 v66, v68, v69\n"
"v_cvt_pk_bf16_f32 v67, v70, v71\n"
"s_waitcnt lgkmcnt(7)\n"
"v_mfma_f32_32x32x16_bf16 v[112:127], v[20:23], v[136:139], v[112:127]\n"
"ds_read_b64_tr_b16 v[20:21], v2 offset:57344\n"
"ds_read_b64_tr_b16 v[22:23], v2 offset:57856\n"
"ds_write_b128 v6, v[64:67] offset:10240\n"
"s_waitcnt lgkmcnt(7)\n"
"v_mfma_f32_32x32x16_bf16 v[96:111], v[24:27], v[140:143], v[96:111]\n"
"ds_read_b64_tr_b16 v[24:25], v2 offset:51200\n"
"ds_read_b64_tr_b16 v[26:27], v2 offset:51712\n"
"v_mul_f32 v72, v72, v88\n"
"v_mul_f32 v73, v73, v89\n"
"v_mul_f32 v74, v74, v90\n"
"v_mul_f32 v75, v75, v91\n"
"s_waitcnt lgkmcnt(7)\n"
"v_mfma_f32_32x32x16_bf16 v[112:127], v[28:31], v[140:143], v[112:127]\n"
"ds_read_b64_tr_b16 v[28:29], v2 offset:59392\n"
"ds_read_b64_tr_b16 v[30:31], v2 offset:59904\n"
"LPAD12:\n"
"s_cbranch_execz LBKB\n"
"v_mul_f32 v76, v76, v92\n"
"v_mul_f32 v77, v77, v93\n"
"v_mul_f32 v78, v78, v94\n"
"v_mul_f32 v79, v79, v95\n"
"s_waitcnt lgkmcnt(7)\n"
"v_mfma_f32_32x32x16_bf16 v[32:47], v[16:19], v[128:131], 0\n"
"ds_read_b64_tr_b16 v[16:17], v2 offset:53248\n"
"ds_read_b64_tr_b16 v[18:19], v2 offset:53760\n"
"v_cvt_pk_bf16_f32 v72, v72, v73\n"
"v_cvt_pk_bf16_f32 v73, v74, v75\n"
"v_cvt_pk_bf16_f32 v74, v76, v77\n"
"v_cvt_pk_bf16_f32 v75, v78, v79\n"
"s_waitcnt lgkmcnt(7)\n"
"v_mfma_f32_32x32x16_bf16 v[48:63], v[20:23], v[128:131], 0\n"
"ds_read_b64_tr_b16 v[20:21], v2 offset:61440\n"
"ds_read_b64_tr_b16 v[22:23], v2 offset:61952\n"
"ds_write_b128 v6, v[72:75] offset:11264\n"
"s_waitcnt lgkmcnt(7)\n"
"v_mfma_f32_32x32x16_bf16 v[32:47], v[24:27], v[132:135], v[32:47]\n"
"ds_read_b64_tr_b16 v[24:25], v2 offset:55296\n"
"ds_read_b64_tr_b16 v[26:27], v2 offset:55808\n"
"s_waitcnt lgkmcnt(7)\n"
"v_mfma_f32_32x32x16_bf16 v[48:63], v[28:31], v[132:135], v[48:63]\n"
"ds_read_b64_tr_b16 v[28:29], v2 offset:63488\n"
"ds_read_b64_tr_b16 v[30:31], v2 offset:64000\n"
"s_waitcnt lgkmcnt(7)\n"
"v_mfma_f32_32x32x16_bf16 v[32:47], v[16:19], v[136:139], v[32:47]\n"
"s_waitcnt lgkmcnt(4)\n"
"s_barrier\n"
"ds_read_b64_tr_b16 v[16:17], v3 offset:0\n"
"ds_read_b64_tr_b16 v[18:19], v3 offset:512\n"
"v_mfma_f32_32x32x16_bf16 v[48:63], v[20:23], v[136:139], v[48:63]\n"
"ds_read_b64_tr_b16 v[20:21], v4 offset:0\n"
"ds_read_b64_tr_b16 v[22:23], v4 offset:512\n"
"s_waitcnt lgkmcnt(6)\n"
"v_mfma_f32_32x32x16_bf16 v[32:47], v[24:27], v[140:143], v[32:47]\n"
"ds_read_b64_tr_b16 v[24:25], v3 offset:8192\n"
"ds_read_b64_tr_b16 v[26:27], v3 offset:8704\n"
"s_waitcnt lgkmcnt(6)\n"
"v_mfma_f32_32x32x16_bf16 v[48:63], v[28:31], v[140:143], v[48:63]\n"
"ds_read_b64_tr_b16 v[28:29], v4 offset:8192\n"
"ds_read_b64_tr_b16 v[30:31], v4 offset:8704\n"
"s_waitcnt lgkmcnt(6)\n"
"v_mfma_f32_32x32x16_bf16 v[96:111], v[16:19], v[144:147], v[96:111]\n"
"ds_read_b64_tr_b16 v[16:17], v3 offset:2048\n"
"ds_read_b64_tr_b16 v[18:19], v3 offset:2560\n"
"s_waitcnt lgkmcnt(6)\n"
"v_mfma_f32_32x32x16_bf16 v[32:47], v[20:23], v[144:147], v[32:47]\n"
"ds_read_b64_tr_b16 v[20:21], v4 offset:2048\n"
"ds_read_b64_tr_b16 v[22:23], v4 offset:2560\n"
"s_waitcnt lgkmcnt(6)\n"
"v_mfma_f32_32x32x16_bf16 v[112:127], v[24:27], v[144:147], v[112:127]\n"
"ds_read_b64_tr_b16 v[24:25], v3 offset:10240\n"
"ds_read_b64_tr_b16 v[26:27], v3 offset:10752\n"
"s_waitcnt lgkmcnt(6)\n"
"v_mfma_f32_32x32x16_bf16 v[48:63], v[28:31], v[144:147], v[48:63]\n"
"ds_read_b64_tr_b16 v[28:29], v4 offset:10240\n"
"ds_read_b64_tr_b16 v[30:31], v4 offset:10752\n"
"s_waitcnt lgkmcnt(6)\n"
"v_mfma_f32_32x32x16_bf16 v[96:111], v[16:19], v[148:151], v[96:111]\n"
"ds_read_b64_tr_b16 v[16:17], v3 offset:4096\n"
"ds_read_b64_tr_b16 v[18:19], v3 offset:4608\n"
"s_waitcnt lgkmcnt(6)\n"
"v_mfma_f32_32x32x16_bf16 v[32:47], v[20:23], v[148:151], v[32:47]\n"
"ds_read_b64_tr_b16 v[20:21], v4 offset:4096\n"
"ds_read_b64_tr_b16 v[22:23], v4 offset:4608\n"
"s_waitcnt lgkmcnt(6)\n"
"v_mfma_f32_32x32x16_bf16 v[112:127], v[24:27], v[148:151], v[112:127]\n"
"ds_read_b64_tr_b16 v[24:25], v3 offset:12288\n"
"ds_read_b64_tr_b16 v[26:27], v3 offset:12800\n"
"s_waitcnt lgkmcnt(6)\n"
"v_mfma_f32_32x32x16_bf16 v[48:63], v[28:31], v[148:151], v[48:63]\n"
"ds_read_b64_tr_b16 v[28:29], v4 offset:12288\n"
"ds_read_b64_tr_b16 v[30:31], v4 offset:12800\n"
"s_waitcnt lgkmcnt(6)\n"
"v_mfma_f32_32x32x16_bf16 v[96:111], v[16:19], v[152:155], v[96:111]\n"
"ds_read_b64_tr_b16 v[16:17], v3 offset:6144\n"
"ds_read_b64_tr_b16 v[18:19], v3 offset:6656\n"
"s_waitcnt lgkmcnt(6)\n"
"v_mfma_f32_32x32x16_bf16 v[32:47], v[20:23], v[152:155], v[32:47]\n"
"ds_read_b64_tr_b16 v[20:21], v4 offset:6144\n"
"ds_read_b64_tr_b16 v[22:23], v4 offset:6656\n"
"s_waitcnt lgkmcnt(6)\n"
"v_mfma_f32_32x32x16_bf16 v[112:127], v[24:27], v[152:155], v[112:127]\n"
"ds_read_b64_tr_b16 v[24:25], v3 offset:14336\n"
"ds_read_b64_tr_b16 v[26:27], v3 offset:14848\n"
"s_waitcnt lgkmcnt(6)\n"
"v_mfma_f32_32x32x16_bf16 v[48:63], v[28:31], v[152:155], v[48:63]\n"
"ds_read_b64_tr_b16 v[28:29], v4 offset:14336\n"
"ds_read_b64_tr_b16 v[30:31], v4 offset:14848\n"
"s_waitcnt lgkmcnt(6)\n"
"v_mfma_f32_32x32x16_bf16 v[96:111], v[16:19], v[156:159], v[96:111]\n"
"s_barrier\n"
"ds_read_b64_tr_b16 v[16:17], v1 offset:0\n"
"ds_read_b64_tr_b16 v[18:19], v1 offset:512\n"
"s_waitcnt lgkmcnt(6)\n"
"v_mfma_f32_32x32x16_bf16 v[32:47], v[20:23], v[156:159], v[32:47]\n"
"ds_read_b64_tr_b16 v[20:21], v2 offset:0\n"
"ds_read_b64_tr_b16 v[22:23], v2 offset:512\n"
"s_waitcnt lgkmcnt(6)\n"
"v_mfma_f32_32x32x16_bf16 v[112:127], v[24:27], v[156:159], v[112:127]\n"
"ds_read_b64_tr_b16 v[24:25], v1 offset:8192\n"
"ds_read_b64_tr_b16 v[26:27], v1 offset:8704\n"
"s_waitcnt lgkmcnt(6)\n"
"v_mfma_f32_32x32x16_bf16 v[48:63], v[28:31], v[156:159], v[48:63]\n"
"ds_read_b64_tr_b16 v[28:29], v2 offset:8192\n"
"ds_read_b64_tr_b16 v[30:31], v2 offset:8704\n"
"s_waitcnt lgkmcnt(6)\n"
"v_mfma_f32_32x32x16_bf16 v[96:111], v[16:19], v[160:163], v[96:111]\n"
"ds_read_b64_tr_b16 v[16:17], v1 offset:2048\n"
"ds_read_b64_tr_b16 v[18:19], v1 offset:2560\n"
"s_waitcnt lgkmcnt(6)\n"
"v_mfma_f32_32x32x16_bf16 v[32:47], v[20:23], v[160:163], v[32:47]\n"
"ds_read_b64_tr_b16 v[20:21], v2 offset:2048\n"
"ds_read_b64_tr_b16 v[22:23], v2 offset:2560\n"
"s_waitcnt lgkmcnt(6)\n"
"v_mfma_f32_32x32x16_bf16 v[112:127], v[24:27], v[160:163], v[112:127]\n"
"ds_read_b64_tr_b16 v[24:25], v1 offset:10240\n"
"ds_read_b64_tr_b16 v[26:27], v1 offset:10752\n"
"s_waitcnt lgkmcnt(6)\n"
"v_mfma_f32_32x32x16_bf16 v[48:63], v[28:31], v[160:163], v[48:63]\n"
"ds_read_b64_tr_b16 v[28:29], v2 offset:10240\n"
"ds_read_b64_tr_b16 v[30:31], v2 offset:10752\n"
"s_waitcnt lgkmcnt(6)\n"
"v_mfma_f32_32x32x16_bf16 v[96:111], v[16:19], v[164:167], v[96:111]\n"
"ds_read_b64_tr_b16 v[16:17], v1 offset:4096\n"
"ds_read_b64_tr_b16 v[18:19], v1 offset:4608\n"
"s_waitcnt lgkmcnt(6)\n"
"v_mfma_f32_32x32x16_bf16 v[32:47], v[20:23], v[164:167], v[32:47]\n"
"ds_read_b64_tr_b16 v[20:21], v2 offset:4096\n"
"ds_read_b64_tr_b16 v[22:23], v2 offset:4608\n"
"s_waitcnt lgkmcnt(6)\n"
"v_mfma_f32_32x32x16_bf16 v[112:127], v[24:27], v[164:167], v[112:127]\n"
"ds_read_b64_tr_b16 v[24:25], v1 offset:12288\n"
"ds_read_b64_tr_b16 v[26:27], v1 offset:12800\n"
"s_waitcnt lgkmcnt(6)\n"
"v_mfma_f32_32x32x16_bf16 v[48:63], v[28:31], v[164:167], v[48:63]\n"
"ds_read_b64_tr_b16 v[28:29], v2 offset:12288\n"
"ds_read_b64_tr_b16 v[30:31], v2 offset:12800\n"
"s_waitcnt lgkmcnt(6)\n"
"v_mfma_f32_32x32x16_bf16 v[96:111], v[16:19], v[168:171], v[96:111]\n"
"ds_read_b64_tr_b16 v[16:17], v1 offset:6144\n"
"ds_read_b64_tr_b16 v[18:19], v1 offset:6656\n"
"s_waitcnt lgkmcnt(6)\n"
"v_mfma_f32_32x32x16_bf16 v[32:47], v[20:23], v[168:171], v[32:47]\n"
"ds_read_b64_tr_b16 v[20:21], v2 offset:6144\n"
"ds_read_b64_tr_b16 v[22:23], v2 offset:6656\n"
"s_waitcnt lgkmcnt(6)\n"
"v_mfma_f32_32x32x16_bf16 v[112:127], v[24:27], v[168:171], v[112:127]\n"
"ds_read_b64_tr_b16 v[24:25], v1 offset:14336\n"
"ds_read_b64_tr_b16 v[26:27], v1 offset:14848\n"
"s_waitcnt lgkmcnt(6)\n"
"v_mfma_f32_32x32x16_bf16 v[48:63], v[28:31], v[168:171], v[48:63]\n"
"ds_read_b64_tr_b16 v[28:29], v2 offset:14336\n"
"ds_read_b64_tr_b16 v[30:31], v2 offset:14848\n"
"s_waitcnt lgkmcnt(6)\n"
"v_mfma_f32_32x32x16_bf16 v[96:111], v[16:19], v[172:175], v[96:111]\n"
"s_barrier\n"
"ds_read_b64_tr_b16 v[16:17], v1 offset:16384\n"
"ds_read_b64_tr_b16 v[18:19], v1 offset:16896\n"
"s_waitcnt lgkmcnt(6)\n"
"v_mfma_f32_32x32x16_bf16 v[32:47], v[20:23], v[172:175], v[32:47]\n"
"ds_read_b64_tr_b16 v[20:21], v2 offset:16384\n"
"ds_read_b64_tr_b16 v[22:23], v2 offset:16896\n"
"s_waitcnt lgkmcnt(6)\n"
"v_mfma_f32_32x32x16_bf16 v[112:127], v[24:27], v[172:175], v[112:127]\n"
"ds_read_b64_tr_b16 v[24:25], v1 offset:24576\n"
"ds_read_b64_tr_b16 v[26:27], v1 offset:25088\n"
"s_waitcnt lgkmcnt(6)\n"
"v_mfma_f32_32x32x16_bf16 v[48:63], v[28:31], v[172:175], v[48:63]\n"
"ds_read_b64_tr_b16 v[28:29], v2 offset:24576\n"
"ds_read_b64_tr_b16 v[30:31], v2 offset:25088\n"
"s_waitcnt lgkmcnt(6)\n"
"v_mfma_f32_32x32x16_bf16 v[96:111], v[16:19], v[176:179], v[96:111]\n"
"ds_read_b64_tr_b16 v[16:17], v1 offset:18432\n"
"ds_read_b64_tr_b16 v[18:19], v1 offset:18944\n"
"s_waitcnt lgkmcnt(6)\n"
"v_mfma_f32_32x32x16_bf16 v[32:47], v[20:23], v[176:179], v[32:47]\n"
"ds_read_b64_tr_b16 v[20:21], v2 offset:18432\n"
"ds_read_b64_tr_b16 v[22:23], v2 offset:18944\n"
"s_waitcnt lgkmcnt(6)\n"
"v_mfma_f32_32x32x16_bf16 v[112:127], v[24:27], v[176:179], v[112:127]\n"
"ds_read_b64_tr_b16 v[24:25], v1 offset:26624\n"
"ds_read_b64_tr_b16 v[26:27], v1 offset:27136\n"
"s_waitcnt lgkmcnt(6)\n"
"v_mfma_f32_32x32x16_bf16 v[48:63], v[28:31], v[176:179], v[48:63]\n"
"ds_read_b64_tr_b16 v[28:29], v2 offset:26624\n"
"ds_read_b64_tr_b16 v[30:31], v2 offset:27136\n"
"s_waitcnt lgkmcnt(6)\n"
"v_mfma_f32_32x32x16_bf16 v[96:111], v[16:19], v[180:183], v[96:111]\n"
"ds_read_b64_tr_b16 v[16:17], v1 offset:20480\n"
"ds_read_b64_tr_b16 v[18:19], v1 offset:20992\n"
"s_waitcnt lgkmcnt(6)\n"
"v_mfma_f32_32x32x16_bf16 v[32:47], v[20:23], v[180:183], v[32:47]\n"
"ds_read_b64_tr_b16 v[20:21], v2 offset:20480\n"
"ds_read_b64_tr_b16 v[22:23], v2 offset:20992\n"
"s_waitcnt lgkmcnt(6)\n"
"v_mfma_f32_32x32x16_bf16 v[112:127], v[24:27], v[180:183], v[112:127]\n"
"ds_read_b64_tr_b16 v[24:25], v1 offset:28672\n"
"ds_read_b64_tr_b16 v[26:27], v1 offset:29184\n"
"s_waitcnt lgkmcnt(6)\n"
"v_mfma_f32_32x32x16_bf16 v[48:63], v[28:31], v[180:183], v[48:63]\n"
"ds_read_b64_tr_b16 v[28:29], v2 offset:28672\n"
"ds_read_b64_tr_b16 v[30:31], v2 offset:29184\n"
"s_waitcnt lgkmcnt(6)\n"
"v_mfma_f32_32x32x16_bf16 v[96:111], v[16:19], v[184:187], v[96:111]\n"
"ds_read_b64_tr_b16 v[16:17], v1 offset:22528\n"
"ds_read_b64_tr_b16 v[18:19], v1 offset:23040\n"
"s_waitcnt lgkmcnt(6)\n"
"v_mfma_f32_32x32x16_bf16 v[32:47], v[20:23], v[184:187], v[32:47]\n"
"ds_read_b64_tr_b16 v[20:21], v2 offset:22528\n"
"ds_read_b64_tr_b16 v[22:23], v2 offset:23040\n"
"s_waitcnt lgkmcnt(6)\n"
"v_mfma_f32_32x32x16_bf16 v[112:127], v[24:27], v[184:187], v[112:127]\n"
"ds_read_b64_tr_b16 v[24:25], v1 offset:30720\n"
"ds_read_b64_tr_b16 v[26:27], v1 offset:31232\n"
"s_waitcnt lgkmcnt(6)\n"
"v_mfma_f32_32x32x16_bf16 v[48:63], v[28:31], v[184:187], v[48:63]\n"
"ds_read_b64_tr_b16 v[28:29], v2 offset:30720\n"
"ds_read_b64_tr_b16 v[30:31], v2 offset:31232\n"
"s_waitcnt lgkmcnt(6)\n"
"v_mfma_f32_32x32x16_bf16 v[96:111], v[16:19], v[188:191], v[96:111]\n"
"s_barrier\n"
"ds_read_b64_tr_b16 v[16:17], v1 offset:32768\n"
"ds_read_b64_tr_b16 v[18:19], v1 offset:33280\n"
"s_waitcnt lgkmcnt(6)\n"
"v_mfma_f32_32x32x16_bf16 v[32:47], v[20:23], v[188:191], v[32:47]\n"
"ds_read_b64_tr_b16 v[20:21], v2 offset:32768\n"
"ds_read_b64_tr_b16 v[22:23], v2 offset:33280\n"
"s_waitcnt lgkmcnt(6)\n"
"v_mfma_f32_32x32x16_bf16 v[112:127], v[24:27], v[188:191], v[112:127]\n"
"ds_read_b64_tr_b16 v[24:25], v1 offset:40960\n"
"ds_read_b64_tr_b16 v[26:27], v1 offset:41472\n"
"s_waitcnt lgkmcnt(6)\n"
"v_mfma_f32_32x32x16_bf16 v[48:63], v[28:31], v[188:191], v[48:63]\n"
"ds_read_b64_tr_b16 v[28:29], v2 offset:40960\n"
"ds_read_b64_tr_b16 v[30:31], v2 offset:41472\n"
"s_waitcnt lgkmcnt(6)\n"
"v_mfma_f32_32x32x16_bf16 v[96:111], v[16:19], v[192:195], v[96:111]\n"
"ds_read_b64_tr_b16 v[16:17], v1 offset:34816\n"
"ds_read_b64_tr_b16 v[18:19], v1 offset:35328\n"
"s_waitcnt lgkmcnt(6)\n"
"v_mfma_f32_32x32x16_bf16 v[32:47], v[20:23], v[192:195], v[32:47]\n"
"ds_read_b64_tr_b16 v[20:21], v2 offset:34816\n"
"ds_read_b64_tr_b16 v[22:23], v2 offset:35328\n"
"s_waitcnt lgkmcnt(6)\n"
"v_mfma_f32_32x32x16_bf16 v[112:127], v[24:27], v[192:195], v[112:127]\n"
"ds_read_b64_tr_b16 v[24:25], v1 offset:43008\n"
"ds_read_b64_tr_b16 v[26:27], v1 offset:43520\n"
"s_waitcnt lgkmcnt(6)\n"
"v_mfma_f32_32x32x16_bf16 v[48:63], v[28:31], v[192:195], v[48:63]\n"
"ds_read_b64_tr_b16 v[28:29], v2 offset:43008\n"
"ds_read_b64_tr_b16 v[30:31], v2 offset:43520\n"
"s_waitcnt lgkmcnt(6)\n"
"v_mfma_f32_32x32x16_bf16 v[96:111], v[16:19], v[196:199], v[96:111]\n"
"ds_read_b64_tr_b16 v[16:17], v1 offset:36864\n"
"ds_read_b64_tr_b16 v[18:19], v1 offset:37376\n"
"s_waitcnt lgkmcnt(6)\n"
"v_mfma_f32_32x32x16_bf16 v[32:47], v[20:23], v[196:199], v[32:47]\n"
"ds_read_b64_tr_b16 v[20:21], v2 offset:36864\n"
"ds_read_b64_tr_b16 v[22:23], v2 offset:37376\n"
"s_waitcnt lgkmcnt(6)\n"
"v_mfma_f32_32x32x16_bf16 v[112:127], v[24:27], v[196:199], v[112:127]\n"
"ds_read_b64_tr_b16 v[24:25], v1 offset:45056\n"
"ds_read_b64_tr_b16 v[26:27], v1 offset:45568\n"
"s_waitcnt lgkmcnt(6)\n"
"v_mfma_f32_32x32x16_bf16 v[48:63], v[28:31], v[196:199], v[48:63]\n"
"ds_read_b64_tr_b16 v[28:29], v2 offset:45056\n"
"ds_read_b64_tr_b16 v[30:31], v2 offset:45568\n"
"s_waitcnt lgkmcnt(6)\n"
"v_mfma_f32_32x32x16_bf16 v[96:111], v[16:19], v[200:203], v[96:111]\n"
"ds_read_b64_tr_b16 v[16:17], v1 offset:38912\n"
"ds_read_b64_tr_b16 v[18:19], v1 offset:39424\n"
"s_waitcnt lgkmcnt(6)\n"
"v_mfma_f32_32x32x16_bf16 v[32:47], v[20:23], v[200:203], v[32:47]\n"
"ds_read_b64_tr_b16 v[20:21], v2 offset:38912\n"
"ds_read_b64_tr_b16 v[22:23], v2 offset:39424\n"
"s_waitcnt lgkmcnt(6)\n"
"v_mfma_f32_32x32x16_bf16 v[112:127], v[24:27], v[200:203], v[112:127]\n"
"ds_read_b64_tr_b16 v[24:25], v1 offset:47104\n"
"ds_read_b64_tr_b16 v[26:27], v1 offset:47616\n"
"s_waitcnt lgkmcnt(6)\n"
"v_mfma_f32_32x32x16_bf16 v[48:63], v[28:31], v[200:203], v[48:63]\n"
"ds_read_b64_tr_b16 v[28:29], v2 offset:47104\n"
"ds_read_b64_tr_b16 v[30:31], v2 offset:47616\n"
"s_waitcnt lgkmcnt(6)\n"
"v_mfma_f32_32x32x16_bf16 v[96:111], v[16:19], v[204:207], v[96:111]\n"
"s_barrier\n"
"ds_read_b64_tr_b16 v[16:17], v1 offset:49152\n"
"ds_read_b64_tr_b16 v[18:19], v1 offset:49664\n"
"s_waitcnt lgkmcnt(6)\n"
"v_mfma_f32_32x32x16_bf16 v[32:47], v[20:23], v[204:207], v[32:47]\n"
"ds_read_b64_tr_b16 v[20:21], v2 offset:49152\n"
"ds_read_b64_tr_b16 v[22:23], v2 offset:49664\n"
"s_waitcnt lgkmcnt(6)\n"
"v_mfma_f32_32x32x16_bf16 v[112:127], v[24:27], v[204:207], v[112:127]\n"
"ds_read_b64_tr_b16 v[24:25], v1 offset:57344\n"
"ds_read_b64_tr_b16 v[26:27], v1 offset:57856\n"
"s_waitcnt lgkmcnt(6)\n"
"v_mfma_f32_32x32x16_bf16 v[48:63], v[28:31], v[204:207], v[48:63]\n"
"ds_read_b64_tr_b16 v[28:29], v2 offset:57344\n"
"ds_read_b64_tr_b16 v[30:31], v2 offset:57856\n"
"s_waitcnt lgkmcnt(6)\n"
"v_mfma_f32_32x32x16_bf16 v[96:111], v[16:19], v[208:211], v[96:111]\n"
"ds_read_b64_tr_b16 v[16:17], v1 offset:51200\n"
"ds_read_b64_tr_b16 v[18:19], v1 offset:51712\n"
"s_waitcnt lgkmcnt(6)\n"
"v_mfma_f32_32x32x16_bf16 v[32:47], v[20:23], v[208:211], v[32:47]\n"
"ds_read_b64_tr_b16 v[20:21], v2 offset:51200\n"
"ds_read_b64_tr_b16 v[22:23], v2 offset:51712\n"
"s_waitcnt lgkmcnt(6)\n"
"v_mfma_f32_32x32x16_bf16 v[112:127], v[24:27], v[208:211], v[112:127]\n"
"ds_read_b64_tr_b16 v[24:25], v1 offset:59392\n"
"ds_read_b64_tr_b16 v[26:27], v1 offset:59904\n"
"s_waitcnt lgkmcnt(6)\n"
"v_mfma_f32_32x32x16_bf16 v[48:63], v[28:31], v[208:211], v[48:63]\n"
"ds_read_b64_tr_b16 v[28:29], v2 offset:59392\n"
"ds_read_b64_tr_b16 v[30:31], v2 offset:59904\n"
"s_waitcnt lgkmcnt(6)\n"
"v_mfma_f32_32x32x16_bf16 v[96:111], v[16:19], v[212:215], v[96:111]\n"
"ds_read_b64_tr_b16 v[16:17], v1 offset:53248\n"
"ds_read_b64_tr_b16 v[18:19], v1 offset:53760\n"
"s_waitcnt lgkmcnt(6)\n"
"v_mfma_f32_32x32x16_bf16 v[32:47], v[20:23], v[212:215], v[32:47]\n"
"ds_read_b64_tr_b16 v[20:21], v2 offset:53248\n"
"ds_read_b64_tr_b16 v[22:23], v2 offset:53760\n"
"s_waitcnt lgkmcnt(6)\n"
"v_mfma_f32_32x32x16_bf16 v[112:127], v[24:27], v[212:215], v[112:127]\n"
"ds_read_b64_tr_b16 v[24:25], v1 offset:61440\n"
"ds_read_b64_tr_b16 v[26:27], v1 offset:61952\n"
"s_waitcnt lgkmcnt(6)\n"
"v_mfma_f32_32x32x16_bf16 v[48:63], v[28:31], v[212:215], v[48:63]\n"
"ds_read_b64_tr_b16 v[28:29], v2 offset:61440\n"
"ds_read_b64_tr_b16 v[30:31], v2 offset:61952\n"
"s_waitcnt lgkmcnt(6)\n"
"v_mfma_f32_32x32x16_bf16 v[96:111], v[16:19], v[216:219], v[96:111]\n"
"ds_read_b64_tr_b16 v[16:17], v1 offset:55296\n"
"ds_read_b64_tr_b16 v[18:19], v1 offset:55808\n"
"s_waitcnt lgkmcnt(6)\n"
"v_mfma_f32_32x32x16_bf16 v[32:47], v[20:23], v[216:219], v[32:47]\n"
"ds_read_b64_tr_b16 v[20:21], v2 offset:55296\n"
"ds_read_b64_tr_b16 v[22:23], v2 offset:55808\n"
"s_waitcnt lgkmcnt(6)\n"
"v_mfma_f32_32x32x16_bf16 v[112:127], v[24:27], v[216:219], v[112:127]\n"
"ds_read_b64_tr_b16 v[24:25], v1 offset:63488\n"
"ds_read_b64_tr_b16 v[26:27], v1 offset:64000\n"
"s_waitcnt lgkmcnt(6)\n"
"v_mfma_f32_32x32x16_bf16 v[48:63], v[28:31], v[216:219], v[48:63]\n"
"ds_read_b64_tr_b16 v[28:29], v2 offset:63488\n"
"ds_read_b64_tr_b16 v[30:31], v2 offset:64000\n"
"s_waitcnt lgkmcnt(6)\n"
"v_mfma_f32_32x32x16_bf16 v[96:111], v[16:19], v[220:223], v[96:111]\n"
"s_barrier\n"
"ds_read_b64_tr_b16 v[16:17], v3 offset:0\n"
"ds_read_b64_tr_b16 v[18:19], v3 offset:512\n"
"s_waitcnt lgkmcnt(6)\n"
"v_mfma_f32_32x32x16_bf16 v[32:47], v[20:23], v[220:223], v[32:47]\n"
"ds_read_b64_tr_b16 v[20:21], v4 offset:0\n"
"ds_read_b64_tr_b16 v[22:23], v4 offset:512\n"
"s_waitcnt lgkmcnt(6)\n"
"v_mfma_f32_32x32x16_bf16 v[112:127], v[24:27], v[220:223], v[112:127]\n"
"ds_read_b64_tr_b16 v[24:25], v3 offset:8192\n"
"ds_read_b64_tr_b16 v[26:27], v3 offset:8704\n"
"s_waitcnt lgkmcnt(6)\n"
"v_mfma_f32_32x32x16_bf16 v[48:63], v[28:31], v[220:223], v[48:63]\n"
"ds_read_b64_tr_b16 v[28:29], v4 offset:8192\n"
"ds_read_b64_tr_b16 v[30:31], v4 offset:8704\n"
"s_waitcnt lgkmcnt(6)\n"
"v_mfma_f32_32x32x16_bf16 v[96:111], v[16:19], v[224:227], v[96:111]\n"
"ds_read_b64_tr_b16 v[16:17], v3 offset:2048\n"
"ds_read_b64_tr_b16 v[18:19], v3 offset:2560\n"
"s_waitcnt lgkmcnt(6)\n"
"v_mfma_f32_32x32x16_bf16 v[32:47], v[20:23], v[224:227], v[32:47]\n"
"ds_read_b64_tr_b16 v[20:21], v4 offset:2048\n"
"ds_read_b64_tr_b16 v[22:23], v4 offset:2560\n"
"s_waitcnt lgkmcnt(6)\n"
"v_mfma_f32_32x32x16_bf16 v[112:127], v[24:27], v[224:227], v[112:127]\n"
"ds_read_b64_tr_b16 v[24:25], v3 offset:10240\n"
"ds_read_b64_tr_b16 v[26:27], v3 offset:10752\n"
"s_waitcnt lgkmcnt(6)\n"
"v_mfma_f32_32x32x16_bf16 v[48:63], v[28:31], v[224:227], v[48:63]\n"
"ds_read_b64_tr_b16 v[28:29], v4 offset:10240\n"
"ds_read_b64_tr_b16 v[30:31], v4 offset:10752\n"
"s_waitcnt lgkmcnt(6)\n"
"v_mfma_f32_32x32x16_bf16 v[96:111], v[16:19], v[228:231], v[96:111]\n"
"ds_read_b64_tr_b16 v[16:17], v3 offset:4096\n"
"ds_read_b64_tr_b16 v[18:19], v3 offset:4608\n"
"s_waitcnt lgkmcnt(6)\n"
"v_mfma_f32_32x32x16_bf16 v[32:47], v[20:23], v[228:231], v[32:47]\n"
"ds_read_b64_tr_b16 v[20:21], v4 offset:4096\n"
"ds_read_b64_tr_b16 v[22:23], v4 offset:4608\n"
"s_waitcnt lgkmcnt(6)\n"
"v_mfma_f32_32x32x16_bf16 v[112:127], v[24:27], v[228:231], v[112:127]\n"
"ds_read_b64_tr_b16 v[24:25], v3 offset:12288\n"
"ds_read_b64_tr_b16 v[26:27], v3 offset:12800\n"
"s_waitcnt lgkmcnt(6)\n"
"v_mfma_f32_32x32x16_bf16 v[48:63], v[28:31], v[228:231], v[48:63]\n"
"ds_read_b64_tr_b16 v[28:29], v4 offset:12288\n"
"ds_read_b64_tr_b16 v[30:31], v4 offset:12800\n"
"s_waitcnt lgkmcnt(6)\n"
"v_mfma_f32_32x32x16_bf16 v[96:111], v[16:19], v[232:235], v[96:111]\n"
"ds_read_b64_tr_b16 v[16:17], v3 offset:6144\n"
"ds_read_b64_tr_b16 v[18:19], v3 offset:6656\n"
"s_waitcnt lgkmcnt(6)\n"
"v_mfma_f32_32x32x16_bf16 v[32:47], v[20:23], v[232:235], v[32:47]\n"
"ds_read_b64_tr_b16 v[20:21], v4 offset:6144\n"
"ds_read_b64_tr_b16 v[22:23], v4 offset:6656\n"
"s_waitcnt lgkmcnt(6)\n"
"v_mfma_f32_32x32x16_bf16 v[112:127], v[24:27], v[232:235], v[112:127]\n"
"ds_read_b64_tr_b16 v[24:25], v3 offset:14336\n"
"ds_read_b64_tr_b16 v[26:27], v3 offset:14848\n"
"s_waitcnt lgkmcnt(6)\n"
"v_mfma_f32_32x32x16_bf16 v[48:63], v[28:31], v[232:235], v[48:63]\n"
"ds_read_b64_tr_b16 v[28:29], v4 offset:14336\n"
"ds_read_b64_tr_b16 v[30:31], v4 offset:14848\n"
"s_waitcnt lgkmcnt(6)\n"
"v_mfma_f32_32x32x16_bf16 v[96:111], v[16:19], v[236:239], v[96:111]\n"
"s_barrier\n"
"ds_read_b64_tr_b16 v[16:17], v1 offset:0\n"
"ds_read_b64_tr_b16 v[18:19], v1 offset:512\n"
"s_waitcnt lgkmcnt(6)\n"
"v_mfma_f32_32x32x16_bf16 v[32:47], v[20:23], v[236:239], v[32:47]\n"
"ds_read_b64_tr_b16 v[20:21], v1 offset:8192\n"
"ds_read_b64_tr_b16 v[22:23], v1 offset:8704\n"
"s_waitcnt lgkmcnt(6)\n"
"v_mfma_f32_32x32x16_bf16 v[112:127], v[24:27], v[236:239], v[112:127]\n"
"ds_read_b64_tr_b16 v[24:25], v1 offset:2048\n"
"ds_read_b64_tr_b16 v[26:27], v1 offset:2560\n"
"s_waitcnt lgkmcnt(6)\n"
"v_mfma_f32_32x32x16_bf16 v[48:63], v[28:31], v[236:239], v[48:63]\n"
"ds_read_b64_tr_b16 v[28:29], v1 offset:10240\n"
"ds_read_b64_tr_b16 v[30:31], v1 offset:10752\n"
"s_waitcnt lgkmcnt(6)\n"
"v_mfma_f32_32x32x16_bf16 v[96:111], v[16:19], v[240:243], v[96:111]\n"
"ds_read_b64_tr_b16 v[16:17], v1 offset:4096\n"
"ds_read_b64_tr_b16 v[18:19], v1 offset:4608\n"
"s_waitcnt lgkmcnt(6)\n"
"v_mfma_f32_32x32x16_bf16 v[112:127], v[20:23], v[240:243], v[112:127]\n"
"ds_read_b64_tr_b16 v[20:21], v1 offset:12288\n"
"ds_read_b64_tr_b16 v[22:23], v1 offset:12800\n"
"s_waitcnt lgkmcnt(6)\n"
"v_mfma_f32_32x32x16_bf16 v[96:111], v[24:27], v[244:247], v[96:111]\n"
"ds_read_b64_tr_b16 v[24:25], v1 offset:6144\n"
"ds_read_b64_tr_b16 v[26:27], v1 offset:6656\n"
"s_waitcnt lgkmcnt(6)\n"
"v_mfma_f32_32x32x16_bf16 v[112:127], v[28:31], v[244:247], v[112:127]\n"
"ds_read_b64_tr_b16 v[28:29], v1 offset:14336\n"
"ds_read_b64_tr_b16 v[30:31], v1 offset:14848\n"
"s_waitcnt lgkmcnt(6)\n"
"v_mfma_f32_32x32x16_bf16 v[96:111], v[16:19], v[248:251], v[96:111]\n"
"ds_read_b64_tr_b16 v[16:17], v2 offset:0\n"
"ds_read_b64_tr_b16 v[18:19], v2 offset:512\n"
"s_waitcnt lgkmcnt(6)\n"
"v_mfma_f32_32x32x16_bf16 v[112:127], v[20:23], v[248:251], v[112:127]\n"
"ds_read_b64_tr_b16 v[20:21], v2 offset:8192\n"
"ds_read_b64_tr_b16 v[22:23], v2 offset:8704\n"
"s_waitcnt lgkmcnt(6)\n"
"v_mfma_f32_32x32x16_bf16 v[96:111], v[24:27], v[252:255], v[96:111]\n"
"ds_read_b64_tr_b16 v[24:25], v2 offset:2048\n"
"ds_read_b64_tr_b16 v[26:27], v2 offset:2560\n"
"s_waitcnt lgkmcnt(6)\n"
"v_mfma_f32_32x32x16_bf16 v[112:127], v[28:31], v[252:255], v[112:127]\n"
"ds_read_b64_tr_b16 v[28:29], v2 offset:10240\n"
"ds_read_b64_tr_b16 v[30:31], v2 offset:10752\n"
"s_waitcnt lgkmcnt(6)\n"
"v_mfma_f32_32x32x16_bf16 v[32:47], v[16:19], v[240:243], v[32:47]\n"
"ds_read_b64_tr_b16 v[16:17], v2 offset:4096\n"
"ds_read_b64_tr_b16 v[18:19], v2 offset:4608\n"
"s_waitcnt lgkmcnt(6)\n"
"v_mfma_f32_32x32x16_bf16 v[48:63], v[20:23], v[240:243], v[48:63]\n"
"ds_read_b64_tr_b16 v[20:21], v2 offset:12288\n"
"ds_read_b64_tr_b16 v[22:23], v2 offset:12800\n"
"s_waitcnt lgkmcnt(6)\n"
"v_mfma_f32_32x32x16_bf16 v[32:47], v[24:27], v[244:247], v[32:47]\n"
"ds_read_b64_tr_b16 v[24:25], v2 offset:6144\n"
"ds_read_b64_tr_b16 v[26:27], v2 offset:6656\n"
"v_mul_f32 v96, v96, v112\n"
"v_mul_f32 v97, v97, v113\n"
"v_mul_f32 v98, v98, v114\n"
"v_mul_f32 v99, v99, v115\n"
"s_waitcnt lgkmcnt(6)\n"
"v_mfma_f32_32x32x16_bf16 v[48:63], v[28:31], v[244:247], v[48:63]\n"
"ds_read_b64_tr_b16 v[28:29], v2 offset:14336\n"
"ds_read_b64_tr_b16 v[30:31], v2 offset:14848\n"
"v_mul_f32 v100, v100, v116\n"
"v_mul_f32 v101, v101, v117\n"
"v_mul_f32 v102, v102, v118\n"
"v_mul_f32 v103, v103, v119\n"
"s_waitcnt lgkmcnt(6)\n"
"v_mfma_f32_32x32x16_bf16 v[32:47], v[16:19], v[248:251], v[32:47]\n"
"s_barrier\n"
"ds_read_b64_tr_b16 v[16:17], v5 offset:0\n"
"ds_read_b64_tr_b16 v[18:19], v5 offset:256\n"
"v_cvt_pk_bf16_f32 v96, v96, v97\n"
"v_cvt_pk_bf16_f32 v97, v98, v99\n"
"v_cvt_pk_bf16_f32 v98, v100, v101\n"
"v_cvt_pk_bf16_f32 v99, v102, v103\n"
"s_waitcnt lgkmcnt(6)\n"
"v_mfma_f32_32x32x16_bf16 v[48:63], v[20:23], v[248:251], v[48:63]\n"
"ds_read_b64_tr_b16 v[20:21], v5 offset:8192\n"
"ds_read_b64_tr_b16 v[22:23], v5 offset:8448\n"
"ds_write_b128 v6, v[96:99] offset:8192\n"
"s_waitcnt lgkmcnt(7)\n"
"v_mfma_f32_32x32x16_bf16 v[32:47], v[24:27], v[252:255], v[32:47]\n"
"ds_read_b64_tr_b16 v[24:25], v5 offset:1024\n"
"ds_read_b64_tr_b16 v[26:27], v5 offset:1280\n"
"v_mul_f32 v104, v104, v120\n"
"v_mul_f32 v105, v105, v121\n"
"v_mul_f32 v106, v106, v122\n"
"v_mul_f32 v107, v107, v123\n"
"s_waitcnt lgkmcnt(7)\n"
"v_mfma_f32_32x32x16_bf16 v[48:63], v[28:31], v[252:255], v[48:63]\n"
"ds_read_b64_tr_b16 v[28:29], v5 offset:9216\n"
"ds_read_b64_tr_b16 v[30:31], v5 offset:9472\n"
"v_mul_f32 v108, v108, v124\n"
"v_mul_f32 v109, v109, v125\n"
"v_mul_f32 v110, v110, v126\n"
"v_mul_f32 v111, v111, v127\n"
"s_waitcnt lgkmcnt(7)\n"
"v_mfma_f32_32x32x16_bf16 v[64:79], v[16:19], v[128:131], 0\n"
"ds_read_b64_tr_b16 v[16:17], v5 offset:2048\n"
"ds_read_b64_tr_b16 v[18:19], v5 offset:2304\n"
"v_cvt_pk_bf16_f32 v104, v104, v105\n"
"v_cvt_pk_bf16_f32 v105, v106, v107\n"
"v_cvt_pk_bf16_f32 v106, v108, v109\n"
"v_cvt_pk_bf16_f32 v107, v110, v111\n"
"s_waitcnt lgkmcnt(7)\n"
"v_mfma_f32_32x32x16_bf16 v[80:95], v[20:23], v[128:131], 0\n"
"ds_read_b64_tr_b16 v[20:21], v5 offset:10240\n"
"ds_read_b64_tr_b16 v[22:23], v5 offset:10496\n"
"ds_write_b128 v6, v[104:107] offset:9216\n"
"s_waitcnt lgkmcnt(7)\n"
"v_mfma_f32_32x32x16_bf16 v[64:79], v[24:27], v[132:135], v[64:79]\n"
"ds_read_b64_tr_b16 v[24:25], v5 offset:3072\n"
"ds_read_b64_tr_b16 v[26:27], v5 offset:3328\n"
"v_mul_f32 v32, v32, v48\n"
"v_mul_f32 v33, v33, v49\n"
"v_mul_f32 v34, v34, v50\n"
"v_mul_f32 v35, v35, v51\n"
"s_waitcnt lgkmcnt(7)\n"
"v_mfma_f32_32x32x16_bf16 v[80:95], v[28:31], v[132:135], v[80:95]\n"
"ds_read_b64_tr_b16 v[28:29], v5 offset:11264\n"
"ds_read_b64_tr_b16 v[30:31], v5 offset:11520\n"
"v_mul_f32 v36, v36, v52\n"
"v_mul_f32 v37, v37, v53\n"
"v_mul_f32 v38, v38, v54\n"
"v_mul_f32 v39, v39, v55\n"
"s_waitcnt lgkmcnt(7)\n"
"v_mfma_f32_32x32x16_bf16 v[64:79], v[16:19], v[136:139], v[64:79]\n"
"ds_read_b64_tr_b16 v[16:17], v5 offset:4096\n"
"ds_read_b64_tr_b16 v[18:19], v5 offset:4352\n"
"v_cvt_pk_bf16_f32 v32, v32, v33\n"
"v_cvt_pk_bf16_f32 v33, v34, v35\n"
"v_cvt_pk_bf16_f32 v34, v36, v37\n"
"v_cvt_pk_bf16_f32 v35, v38, v39\n"
"s_waitcnt lgkmcnt(7)\n"
"v_mfma_f32_32x32x16_bf16 v[80:95], v[20:23], v[136:139], v[80:95]\n"
"ds_read_b64_tr_b16 v[20:21], v5 offset:12288\n"
"ds_read_b64_tr_b16 v[22:23], v5 offset:12544\n"
"ds_write_b128 v6, v[32:35] offset:10240\n"
"s_waitcnt lgkmcnt(7)\n"
"v_mfma_f32_32x32x16_bf16 v[64:79], v[24:27], v[140:143], v[64:79]\n"
"ds_read_b64_tr_b16 v[24:25], v5 offset:5120\n"
"ds_read_b64_tr_b16 v[26:27], v5 offset:5376\n"
"v_mul_f32 v40, v40, v56\n"
"v_mul_f32 v41, v41, v57\n"
"v_mul_f32 v42, v42, v58\n"
"v_mul_f32 v43, v43, v59\n"
"s_waitcnt lgkmcnt(7)\n"
"v_mfma_f32_32x32x16_bf16 v[80:95], v[28:31], v[140:143], v[80:95]\n"
"ds_read_b64_tr_b16 v[28:29], v5 offset:13312\n"
"ds_read_b64_tr_b16 v[30:31], v5 offset:13568\n"
"v_mul_f32 v44, v44, v60\n"
"v_mul_f32 v45, v45, v61\n"
"v_mul_f32 v46, v46, v62\n"
"v_mul_f32 v47, v47, v63\n"
"s_waitcnt lgkmcnt(7)\n"
"v_mfma_f32_32x32x16_bf16 v[64:79], v[16:19], v[144:147], v[64:79]\n"
"ds_read_b64_tr_b16 v[16:17], v5 offset:6144\n"
"ds_read_b64_tr_b16 v[18:19], v5 offset:6400\n"
"v_cvt_pk_bf16_f32 v40, v40, v41\n"
"v_cvt_pk_bf16_f32 v41, v42, v43\n"
"v_cvt_pk_bf16_f32 v42, v44, v45\n"
"v_cvt_pk_bf16_f32 v43, v46, v47\n"
"s_waitcnt lgkmcnt(7)\n"
"v_mfma_f32_32x32x16_bf16 v[80:95], v[20:23], v[144:147], v[80:95]\n"
"ds_read_b64_tr_b16 v[20:21], v5 offset:14336\n"
"ds_read_b64_tr_b16 v[22:23], v5 offset:14592\n"
"ds_write_b128 v6, v[40:43] offset:11264\n"
"s_waitcnt lgkmcnt(7)\n"
"v_mfma_f32_32x32x16_bf16 v[64:79], v[24:27], v[148:151], v[64:79]\n"
"ds_read_b64_tr_b16 v[24:25], v5 offset:7168\n"
"ds_read_b64_tr_b16 v[26:27], v5 offset:7424\n"
"s_waitcnt lgkmcnt(7)\n"
"v_mfma_f32_32x32x16_bf16 v[80:95], v[28:31], v[148:151], v[80:95]\n"
"ds_read_b64_tr_b16 v[28:29], v5 offset:15360\n"
"ds_read_b64_tr_b16 v[30:31], v5 offset:15616\n"
"s_waitcnt lgkmcnt(7)\n"
"v_mfma_f32_32x32x16_bf16 v[64:79], v[16:19], v[152:155], v[64:79]\n"
"s_waitcnt lgkmcnt(4)\n"
"s_barrier\n"
"LPAD13:\n"
"s_cbranch_execz LBKB\n"
"ds_read_b64_tr_b16 v[16:17], v5 offset:16384\n"
"ds_read_b64_tr_b16 v[18:19], v5 offset:16640\n"
"v_mfma_f32_32x32x16_bf16 v[80:95], v[20:23], v[152:155], v[80:95]\n"
"ds_read_b64_tr_b16 v[20:21], v5 offset:24576\n"
"ds_read_b64_tr_b16 v[22:23], v5 offset:24832\n"
"s_waitcnt lgkmcnt(6)\n"
"v_mfma_f32_32x32x16_bf16 v[64:79], v[24:27], v[156:159], v[64:79]\n"
"ds_read_b64_tr_b16 v[24:25], v5 offset:17408\n"
"ds_read_b64_tr_b16 v[26:27], v5 offset:17664\n"
"s_waitcnt lgkmcnt(6)\n"
"v_mfma_f32_32x32x16_bf16 v[80:95], v[28:31], v[156:159], v[80:95]\n"
"ds_read_b64_tr_b16 v[28:29], v5 offset:25600\n"
"ds_read_b64_tr_b16 v[30:31], v5 offset:25856\n"
"s_waitcnt lgkmcnt(6)\n"
"v_mfma_f32_32x32x16_bf16 v[64:79], v[16:19], v[160:163], v[64:79]\n"
"ds_read_b64_tr_b16 v[16:17], v5 offset:18432\n"
"ds_read_b64_tr_b16 v[18:19], v5 offset:18688\n"
"s_waitcnt lgkmcnt(6)\n"
"v_mfma_f32_32x32x16_bf16 v[80:95], v[20:23], v[160:163], v[80:95]\n"
"ds_read_b64_tr_b16 v[20:21], v5 offset:26624\n"
"ds_read_b64_tr_b16 v[22:23], v5 offset:26880\n"
"s_waitcnt lgkmcnt(6)\n"
"v_mfma_f32_32x32x16_bf16 v[64:79], v[24:27], v[164:167], v[64:79]\n"
"ds_read_b64_tr_b16 v[24:25], v5 offset:19456\n"
"ds_read_b64_tr_b16 v[26:27], v5 offset:19712\n"
"s_waitcnt lgkmcnt(6)\n"
"v_mfma_f32_32x32x16_bf16 v[80:95], v[28:31], v[164:167], v[80:95]\n"
"ds_read_b64_tr_b16 v[28:29], v5 offset:27648\n"
"ds_read_b64_tr_b16 v[30:31], v5 offset:27904\n"
"s_waitcnt lgkmcnt(6)\n"
"v_mfma_f32_32x32x16_bf16 v[64:79], v[16:19], v[168:171], v[64:79]\n"
"ds_read_b64_tr_b16 v[16:17], v5 offset:20480\n"
"ds_read_b64_tr_b16 v[18:19], v5 offset:20736\n"
"s_waitcnt lgkmcnt(6)\n"
"v_mfma_f32_32x32x16_bf16 v[80:95], v[20:23], v[168:171], v[80:95]\n"
"ds_read_b64_tr_b16 v[20:21], v5 offset:28672\n"
"ds_read_b64_tr_b16 v[22:23], v5 offset:28928\n"
"s_waitcnt lgkmcnt(6)\n"
"v_mfma_f32_32x32x16_bf16 v[64:79], v[24:27], v[172:175], v[64:79]\n"
"ds_read_b64_tr_b16 v[24:25], v5 offset:21504\n"
"ds_read_b64_tr_b16 v[26:27], v5 offset:21760\n"
"s_waitcnt lgkmcnt(6)\n"
"v_mfma_f32_32x32x16_bf16 v[80:95], v[28:31], v[172:175], v[80:95]\n"
"ds_read_b64_tr_b16 v[28:29], v5 offset:29696\n"
"ds_read_b64_tr_b16 v[30:31], v5 offset:29952\n"
"s_waitcnt lgkmcnt(6)\n"
"v_mfma_f32_32x32x16_bf16 v[64:79], v[16:19], v[176:179], v[64:79]\n"
"ds_read_b64_tr_b16 v[16:17], v5 offset:22528\n"
"ds_read_b64_tr_b16 v[18:19], v5 offset:22784\n"
"s_waitcnt lgkmcnt(6)\n"
"v_mfma_f32_32x32x16_bf16 v[80:95], v[20:23], v[176:179], v[80:95]\n"
"ds_read_b64_tr_b16 v[20:21], v5 offset:30720\n"
"ds_read_b64_tr_b16 v[22:23], v5 offset:30976\n"
"s_waitcnt lgkmcnt(6)\n"
"v_mfma_f32_32x32x16_bf16 v[64:79], v[24:27], v[180:183], v[64:79]\n"
"ds_read_b64_tr_b16 v[24:25], v5 offset:23552\n"
"ds_read_b64_tr_b16 v[26:27], v5 offset:23808\n"
"s_waitcnt lgkmcnt(6)\n"
"v_mfma_f32_32x32x16_bf16 v[80:95], v[28:31], v[180:183], v[80:95]\n"
"ds_read_b64_tr_b16 v[28:29], v5 offset:31744\n"
"ds_read_b64_tr_b16 v[30:31], v5 offset:32000\n"
"s_waitcnt lgkmcnt(6)\n"
"v_mfma_f32_32x32x16_bf16 v[64:79], v[16:19], v[184:187], v[64:79]\n"
"s_barrier\n"
"ds_read_b64_tr_b16 v[16:17], v5 offset:32768\n"
"ds_read_b64_tr_b16 v[18:19], v5 offset:33024\n"
"s_waitcnt lgkmcnt(6)\n"
"v_mfma_f32_32x32x16_bf16 v[80:95], v[20:23], v[184:187], v[80:95]\n"
"ds_read_b64_tr_b16 v[20:21], v5 offset:40960\n"
"ds_read_b64_tr_b16 v[22:23], v5 offset:41216\n"
"s_waitcnt lgkmcnt(6)\n"
"v_mfma_f32_32x32x16_bf16 v[64:79], v[24:27], v[188:191], v[64:79]\n"
"ds_read_b64_tr_b16 v[24:25], v5 offset:33792\n"
"ds_read_b64_tr_b16 v[26:27], v5 offset:34048\n"
"s_waitcnt lgkmcnt(6)\n"
"v_mfma_f32_32x32x16_bf16 v[80:95], v[28:31], v[188:191], v[80:95]\n"
"ds_read_b64_tr_b16 v[28:29], v5 offset:41984\n"
"ds_read_b64_tr_b16 v[30:31], v5 offset:42240\n"
"s_waitcnt lgkmcnt(6)\n"
"v_mfma_f32_32x32x16_bf16 v[64:79], v[16:19], v[192:195], v[64:79]\n"
"ds_read_b64_tr_b16 v[16:17], v5 offset:34816\n"
"ds_read_b64_tr_b16 v[18:19], v5 offset:35072\n"
"s_waitcnt lgkmcnt(6)\n"
"v_mfma_f32_32x32x16_bf16 v[80:95], v[20:23], v[192:195], v[80:95]\n"
"ds_read_b64_tr_b16 v[20:21], v5 offset:43008\n"
"ds_read_b64_tr_b16 v[22:23], v5 offset:43264\n"
"s_waitcnt lgkmcnt(6)\n"
"v_mfma_f32_32x32x16_bf16 v[64:79], v[24:27], v[196:199], v[64:79]\n"
"ds_read_b64_tr_b16 v[24:25], v5 offset:35840\n"
"ds_read_b64_tr_b16 v[26:27], v5 offset:36096\n"
"s_waitcnt lgkmcnt(6)\n"
"v_mfma_f32_32x32x16_bf16 v[80:95], v[28:31], v[196:199], v[80:95]\n"
"ds_read_b64_tr_b16 v[28:29], v5 offset:44032\n"
"ds_read_b64_tr_b16 v[30:31], v5 offset:44288\n"
"s_waitcnt lgkmcnt(6)\n"
"v_mfma_f32_32x32x16_bf16 v[64:79], v[16:19], v[200:203], v[64:79]\n"
"ds_read_b64_tr_b16 v[16:17], v5 offset:36864\n"
"ds_read_b64_tr_b16 v[18:19], v5 offset:37120\n"
"s_waitcnt lgkmcnt(6)\n"
"v_mfma_f32_32x32x16_bf16 v[80:95], v[20:23], v[200:203], v[80:95]\n"
"ds_read_b64_tr_b16 v[20:21], v5 offset:45056\n"
"ds_read_b64_tr_b16 v[22:23], v5 offset:45312\n"
"s_waitcnt lgkmcnt(6)\n"
"v_mfma_f32_32x32x16_bf16 v[64:79], v[24:27], v[204:207], v[64:79]\n"
"ds_read_b64_tr_b16 v[24:25], v5 offset:37888\n"
"ds_read_b64_tr_b16 v[26:27], v5 offset:38144\n"
"s_waitcnt lgkmcnt(6)\n"
"v_mfma_f32_32x32x16_bf16 v[80:95], v[28:31], v[204:207], v[80:95]\n"
"ds_read_b64_tr_b16 v[28:29], v5 offset:46080\n"
"ds_read_b64_tr_b16 v[30:31], v5 offset:46336\n"
"s_waitcnt lgkmcnt(6)\n"
"v_mfma_f32_32x32x16_bf16 v[64:79], v[16:19], v[208:211], v[64:79]\n"
"ds_read_b64_tr_b16 v[16:17], v5 offset:38912\n"
"ds_read_b64_tr_b16 v[18:19], v5 offset:39168\n"
"s_waitcnt lgkmcnt(6)\n"
"v_mfma_f32_32x32x16_bf16 v[80:95], v[20:23], v[208:211], v[80:95]\n"
"ds_read_b64_tr_b16 v[20:21], v5 offset:47104\n"
"ds_read_b64_tr_b16 v[22:23], v5 offset:47360\n"
"s_waitcnt lgkmcnt(6)\n"
"v_mfma_f32_32x32x16_bf16 v[64:79], v[24:27], v[212:215], v[64:79]\n"
"ds_read_b64_tr_b16 v[24:25], v5 offset:39936\n"
"ds_read_b64_tr_b16 v[26:27], v5 offset:40192\n"
"s_waitcnt lgkmcnt(6)\n"
"v_mfma_f32_32x32x16_bf16 v[80:95], v[28:31], v[212:215], v[80:95]\n"
"ds_read_b64_tr_b16 v[28:29], v5 offset:48128\n"
"ds_read_b64_tr_b16 v[30:31], v5 offset:48384\n"
"s_waitcnt lgkmcnt(6)\n"
"v_mfma_f32_32x32x16_bf16 v[64:79], v[16:19], v[216:219], v[64:79]\n"
"s_barrier\n"
"ds_read_b64_tr_b16 v[16:17], v5 offset:49152\n"
"ds_read_b64_tr_b16 v[18:19], v5 offset:49408\n"
"s_waitcnt lgkmcnt(6)\n"
"v_mfma_f32_32x32x16_bf16 v[80:95], v[20:23], v[216:219], v[80:95]\n"
"ds_read_b64_tr_b16 v[20:21], v5 offset:57344\n"
"ds_read_b64_tr_b16 v[22:23], v5 offset:57600\n"
"s_waitcnt lgkmcnt(6)\n"
"v_mfma_f32_32x32x16_bf16 v[64:79], v[24:27], v[220:223], v[64:79]\n"
"ds_read_b64_tr_b16 v[24:25], v5 offset:50176\n"
"ds_read_b64_tr_b16 v[26:27], v5 offset:50432\n"
"s_waitcnt lgkmcnt(6)\n"
"v_mfma_f32_32x32x16_bf16 v[80:95], v[28:31], v[220:223], v[80:95]\n"
"ds_read_b64_tr_b16 v[28:29], v5 offset:58368\n"
"ds_read_b64_tr_b16 v[30:31], v5 offset:58624\n"
"s_waitcnt lgkmcnt(6)\n"
"v_mfma_f32_32x32x16_bf16 v[64:79], v[16:19], v[224:227], v[64:79]\n"
"ds_read_b64_tr_b16 v[16:17], v5 offset:51200\n"
"ds_read_b64_tr_b16 v[18:19], v5 offset:51456\n"
"s_waitcnt lgkmcnt(6)\n"
"v_mfma_f32_32x32x16_bf16 v[80:95], v[20:23], v[224:227], v[80:95]\n"
"ds_read_b64_tr_b16 v[20:21], v5 offset:59392\n"
"ds_read_b64_tr_b16 v[22:23], v5 offset:59648\n"
"s_waitcnt lgkmcnt(6)\n"
"v_mfma_f32_32x32x16_bf16 v[64:79], v[24:27], v[228:231], v[64:79]\n"
"ds_read_b64_tr_b16 v[24:25], v5 offset:52224\n"
"ds_read_b64_tr_b16 v[26:27], v5 offset:52480\n"
"s_waitcnt lgkmcnt(6)\n"
"v_mfma_f32_32x32x16_bf16 v[80:95], v[28:31], v[228:231], v[80:95]\n"
"ds_read_b64_tr_b16 v[28:29], v5 offset:60416\n"
"ds_read_b64_tr_b16 v[30:31], v5 offset:60672\n"
"s_waitcnt lgkmcnt(6)\n"
"v_mfma_f32_32x32x16_bf16 v[64:79], v[16:19], v[232:235], v[64:79]\n"
"ds_read_b64_tr_b16 v[16:17], v5 offset:53248\n"
"ds_read_b64_tr_b16 v[18:19], v5 offset:53504\n"
"s_waitcnt lgkmcnt(6)\n"
"v_mfma_f32_32x32x16_bf16 v[80:95], v[20:23], v[232:235], v[80:95]\n"
"ds_read_b64_tr_b16 v[20:21], v5 offset:61440\n"
"ds_read_b64_tr_b16 v[22:23], v5 offset:61696\n"
"s_waitcnt lgkmcnt(6)\n"
"v_mfma_f32_32x32x16_bf16 v[64:79], v[24:27], v[236:239], v[64:79]\n"
"ds_read_b64_tr_b16 v[24:25], v5 offset:54272\n"
"ds_read_b64_tr_b16 v[26:27], v5 offset:54528\n"
"s_waitcnt lgkmcnt(6)\n"
"v_mfma_f32_32x32x16_bf16 v[80:95], v[28:31], v[236:239], v[80:95]\n"
"ds_read_b64_tr_b16 v[28:29], v5 offset:62464\n"
"ds_read_b64_tr_b16 v[30:31], v5 offset:62720\n"
"s_waitcnt lgkmcnt(6)\n"
"v_mfma_f32_32x32x16_bf16 v[64:79], v[16:19], v[240:243], v[64:79]\n"
"ds_read_b64_tr_b16 v[16:17], v5 offset:55296\n"
"ds_read_b64_tr_b16 v[18:19], v5 offset:55552\n"
"s_waitcnt lgkmcnt(6)\n"
"v_mfma_f32_32x32x16_bf16 v[80:95], v[20:23], v[240:243], v[80:95]\n"
"ds_read_b64_tr_b16 v[20:21], v5 offset:63488\n"
"ds_read_b64_tr_b16 v[22:23], v5 offset:63744\n"
"s_waitcnt lgkmcnt(6)\n"
"v_mfma_f32_32x32x16_bf16 v[64:79], v[24:27], v[244:247], v[64:79]\n"
"ds_read_b64_tr_b16 v[24:25], v5 offset:56320\n"
"ds_read_b64_tr_b16 v[26:27], v5 offset:56576\n"
"s_waitcnt lgkmcnt(6)\n"
"v_mfma_f32_32x32x16_bf16 v[80:95], v[28:31], v[244:247], v[80:95]\n"
"ds_read_b64_tr_b16 v[28:29], v5 offset:64512\n"
"ds_read_b64_tr_b16 v[30:31], v5 offset:64768\n"
"s_waitcnt lgkmcnt(6)\n"
"v_mfma_f32_32x32x16_bf16 v[64:79], v[16:19], v[248:251], v[64:79]\n"
"s_barrier\n"
"s_waitcnt lgkmcnt(4)\n"
"v_mfma_f32_32x32x16_bf16 v[80:95], v[20:23], v[248:251], v[80:95]\n"
"s_waitcnt lgkmcnt(2)\n"
"v_mfma_f32_32x32x16_bf16 v[64:79], v[24:27], v[252:255], v[64:79]\n"
"s_waitcnt lgkmcnt(0)\n"
"v_mfma_f32_32x32x16_bf16 v[80:95], v[28:31], v[252:255], v[80:95]\n"
"s_nop 7\n"
"s_nop 3\n"
"v_mul_f32 v64, v64, v80\n"
"v_mul_f32 v65, v65, v81\n"
"v_mul_f32 v66, v66, v82\n"
"v_mul_f32 v67, v67, v83\n"
"v_mul_f32 v68, v68, v84\n"
"v_mul_f32 v69, v69, v85\n"
"v_mul_f32 v70, v70, v86\n"
"v_mul_f32 v71, v71, v87\n"
"v_cvt_pk_bf16_f32 v64, v64, v65\n"
"v_cvt_pk_bf16_f32 v65, v66, v67\n"
"v_cvt_pk_bf16_f32 v66, v68, v69\n"
"v_cvt_pk_bf16_f32 v67, v70, v71\n"
"buffer_store_dwordx4 v[64:67], v6, s[32:35], s57 offen sc1\n"
"v_mul_f32 v72, v72, v88\n"
"v_mul_f32 v73, v73, v89\n"
"v_mul_f32 v74, v74, v90\n"
"v_mul_f32 v75, v75, v91\n"
"v_mul_f32 v76, v76, v92\n"
"v_mul_f32 v77, v77, v93\n"
"v_mul_f32 v78, v78, v94\n"
"v_mul_f32 v79, v79, v95\n"
"v_cvt_pk_bf16_f32 v72, v72, v73\n"
"v_cvt_pk_bf16_f32 v73, v74, v75\n"
"v_cvt_pk_bf16_f32 v74, v76, v77\n"
"v_cvt_pk_bf16_f32 v75, v78, v79\n"
"buffer_store_dwordx4 v[72:75], v6, s[32:35], s57 offen offset:1024 sc1\n"
"v_mov_b32 v14, s40\n"
"v_mov_b32 v15, s41\n"
"v_lshrrev_b32 v1, 5, v0\n"
"v_lshlrev_b32 v1, 9, v1\n"
"v_bfe_u32 v16, v0, 2, 2\n"
"v_lshl_add_u32 v1, v16, 7, v1\n"
"v_bfe_u32 v16, v0, 3, 1\n"
"v_lshl_add_u32 v1, v16, 6, v1\n"
"v_bfe_u32 v16, v0, 4, 1\n"
"v_lshl_add_u32 v1, v16, 5, v1\n"
"v_and_b32 v16, 3, v0\n"
"v_lshl_add_u32 v1, v16, 3, v1\n"
"s_and_b32 s51, s42, 1\n"
"s_mul_i32 s49, s51, 8256\n"
"v_xor_b32 v1, s49, v1\n"
"v_xor_b32 v2, 64, v1\n"
"s_lshr_b32 s50, s42, 1\n"
"s_mul_i32 s49, s50, 147456\n"
"v_lshlrev_b32 v16, 4, v0\n"
"v_add_u32 v3, s49, v16\n"
"s_lshl_b32 s49, s46, 9\n"
"s_lshl_b32 s52, s42, 5\n"
"s_add_u32 s49, s49, s52\n"
"s_add_u32 s49, s49, 655744\n"
"v_and_b32 v4, 3, v0\n"
"v_lshlrev_b32 v4, 3, v4\n"
"v_add_u32 v4, s49, v4\n"
"s_mul_i32 s49, s46, 294912\n"
"s_lshl_b32 s52, s51, 14\n"
"s_add_u32 s49, s49, s52\n"
"s_add_u32 s49, s49, 1163264\n"
"s_add_u32 s52, s3, 0\n"
"s_and_b32 s52, s52, 3\n"
"s_lshl_b32 s52, s52, 10\n"
"s_add_u32 s68, s52, s49\n"
"s_add_u32 s52, s3, 1\n"
"s_and_b32 s52, s52, 3\n"
"s_lshl_b32 s52, s52, 10\n"
"s_add_u32 s69, s52, s49\n"
"s_add_u32 s52, s3, 2\n"
"s_and_b32 s52, s52, 3\n"
"s_lshl_b32 s52, s52, 10\n"
"s_add_u32 s70, s52, s49\n"
"s_add_u32 s52, s3, 3\n"
"s_and_b32 s52, s52, 3\n"
"s_lshl_b32 s52, s52, 10\n"
"s_add_u32 s71, s52, s49\n"
"s_lshl_b32 s49, s42, 12\n"
"s_add_u32 s49, s49, 65536\n"
"v_add_u32 v10, s49, v16\n"
"s_lshl_b32 s49, s42, 11\n"
"s_add_u32 s49, s49, 155648\n"
"v_add_u32 v11, s49, v16\n"
"v_and_b32 v12, 31, v0\n"
"v_lshlrev_b32 v12, 10, v12\n"
"v_lshrrev_b32 v17, 5, v0\n"
"v_lshl_add_u32 v12, v17, 4, v12\n"
"s_lshl_b32 s72, s46, 16\n"
"s_lshr_b32 s50, s42, 1\n"
"s_lshl_b32 s50, s50, 15\n"
"s_add_u32 s72, s72, s50\n"
"s_lshl_b32 s50, s47, 7\n"
"s_add_u32 s72, s72, s50\n"
"s_and_b32 s50, s42, 1\n"
"s_lshl_b32 s50, s50, 6\n"
"s_add_u32 s72, s72, s50\n"
"s_xor_b32 s49, s42, 1\n"
"s_mul_i32 s49, s49, 18432\n"
"s_add_u32 s49, s49, 83968\n"
"v_add_u32 v18, s49, v16\n"
"s_xor_b32 s49, s42, 2\n"
"s_mul_i32 s49, s49, 18432\n"
"s_add_u32 s49, s49, 88064\n"
"v_add_u32 v19, s49, v16\n"
"s_xor_b32 s49, s42, 3\n"
"s_mul_i32 s49, s49, 18432\n"
"s_add_u32 s49, s49, 92160\n"
"v_add_u32 v20, s49, v16\n"
"s_xor_b32 s49, s42, 1\n"
"s_lshl_b32 s49, s49, 12\n"
"s_add_u32 s49, s49, 67584\n"
"v_add_u32 v21, s49, v16\n"
"s_mul_i32 s49, s42, 18432\n"
"s_add_u32 s49, s49, 94208\n"
"v_add_u32 v22, s49, v16\n"
"s_lshr_b32 s49, s42, 1\n"
"s_lshl_b32 s49, s49, 7\n"
"s_add_u32 s49, s49, 96256\n"
"v_and_b32 v17, 31, v0\n"
"v_lshl_add_u32 v17, v17, 2, s49\n"
"s_barrier\n"
"ds_read_b64_tr_b16 v[192:193], v1 offset:0\n"
"ds_read_b64_tr_b16 v[194:195], v1 offset:1024\n"
"ds_read_b64_tr_b16 v[196:197], v2 offset:0\n"
"ds_read_b64_tr_b16 v[198:199], v2 offset:1024\n"
"ds_read_b64_tr_b16 v[200:201], v1 offset:2048\n"
"ds_read_b64_tr_b16 v[202:203], v1 offset:3072\n"
"ds_read_b64_tr_b16 v[204:205], v2 offset:2048\n"
"ds_read_b64_tr_b16 v[206:207], v2 offset:3072\n"
"ds_read_b64_tr_b16 v[208:209], v1 offset:4096\n"
"ds_read_b64_tr_b16 v[210:211], v1 offset:5120\n"
"ds_read_b64_tr_b16 v[212:213], v2 offset:4096\n"
"ds_read_b64_tr_b16 v[214:215], v2 offset:5120\n"
"s_waitcnt vmcnt(0)\n"
"s_mov_b64 exec, 1\n"
"buffer_store_dwordx2 v[14:15], off, s[32:35], s58 sc1\n"
"s_mov_b64 exec, -1\n"
"buffer_load_dwordx2 v[8:9], v4, s[32:35], 0 offen sc1\n"
"s_waitcnt vmcnt(0)\n"
"v_cmp_eq_u64 vcc, s[40:41], v[8:9]\n"
"s_nop 0\n"
"s_and_b32 s49, vcc_lo, 0xf\n"
"s_cmp_eq_u32 s49, 0xf\n"
"s_cbranch_scc0 LCB_slow_1\n"
"LCB_back_2:\n"
"buffer_load_dwordx4 v[128:131], v3, s[32:35], s68 offen sc1\n"
"s_add_u32 s68, s68, 4096\n"
"buffer_load_dwordx4 v[132:135], v3, s[32:35], s68 offen sc1\n"
"s_add_u32 s68, s68, 4096\n"
"buffer_load_dwordx4 v[136:139], v3, s[32:35], s68 offen sc1\n"
"s_add_u32 s68, s68, 4096\n"
"buffer_load_dwordx4 v[140:143], v3, s[32:35], s68 offen sc1\n"
"buffer_load_dwordx4 v[144:147], v3, s[32:35], s69 offen sc1\n"
"s_add_u32 s69, s69, 4096\n"
"buffer_load_dwordx4 v[148:151], v3, s[32:35], s69 offen sc1\n"
"s_add_u32 s69, s69, 4096\n"
"buffer_load_dwordx4 v[152:155], v3, s[32:35], s69 offen sc1\n"
"s_add_u32 s69, s69, 4096\n"
"buffer_load_dwordx4 v[156:159], v3, s[32:35], s69 offen sc1\n"
"buffer_load_dwordx4 v[160:163], v3, s[32:35], s70 offen sc1\n"
"s_add_u32 s70, s70, 4096\n"
"buffer_load_dwordx4 v[164:167], v3, s[32:35], s70 offen sc1\n"
"s_add_u32 s70, s70, 4096\n"
"buffer_load_dwordx4 v[168:171], v3, s[32:35], s70 offen sc1\n"
"s_add_u32 s70, s70, 4096\n"
"buffer_load_dwordx4 v[172:175], v3, s[32:35], s70 offen sc1\n"
"buffer_load_dwordx4 v[176:179], v3, s[32:35], s71 offen sc1\n"
"s_add_u32 s71, s71, 4096\n"
"buffer_load_dwordx4 v[180:183], v3, s[32:35], s71 offen sc1\n"
"s_add_u32 s71, s71, 4096\n"
"buffer_load_dwordx4 v[184:187], v3, s[32:35], s71 offen sc1\n"
"s_add_u32 s71, s71, 4096\n"
"buffer_load_dwordx4 v[188:191], v3, s[32:35], s71 offen sc1\n"
"s_waitcnt vmcnt(15) lgkmcnt(8)\n"
"v_mfma_f32_32x32x16_bf16 v[112:127], v[196:199], v[128:131], 0\n"
"v_mfma_f32_32x32x16_bf16 v[96:111], v[192:195], v[128:131], 0\n"
"ds_read_b64_tr_b16 v[216:217], v1 offset:6144\n"
"ds_read_b64_tr_b16 v[218:219], v1 offset:7168\n"
"ds_read_b64_tr_b16 v[220:221], v2 offset:6144\n"
"ds_read_b64_tr_b16 v[222:223], v2 offset:7168\n"
"s_waitcnt vmcnt(14) lgkmcnt(8)\n"
"v_mfma_f32_32x32x16_bf16 v[112:127], v[204:207], v[132:135], v[112:127]\n"
"v_mfma_f32_32x32x16_bf16 v[96:111], v[200:203], v[132:135], v[96:111]\n"
"ds_read_b64_tr_b16 v[192:193], v1 offset:16384\n"
"ds_read_b64_tr_b16 v[194:195], v1 offset:17408\n"
"ds_read_b64_tr_b16 v[196:197], v2 offset:16384\n"
"ds_read_b64_tr_b16 v[198:199], v2 offset:17408\n"
"s_waitcnt vmcnt(13) lgkmcnt(8)\n"
"v_mfma_f32_32x32x16_bf16 v[112:127], v[212:215], v[136:139], v[112:127]\n"
"v_mfma_f32_32x32x16_bf16 v[96:111], v[208:211], v[136:139], v[96:111]\n"
"ds_read_b64_tr_b16 v[200:201], v1 offset:18432\n"
"ds_read_b64_tr_b16 v[202:203], v1 offset:19456\n"
"ds_read_b64_tr_b16 v[204:205], v2 offset:18432\n"
"ds_read_b64_tr_b16 v[206:207], v2 offset:19456\n"
"s_waitcnt vmcnt(12) lgkmcnt(8)\n"
"v_mfma_f32_32x32x16_bf16 v[112:127], v[220:223], v[140:143], v[112:127]\n"
"v_mfma_f32_32x32x16_bf16 v[96:111], v[216:219], v[140:143], v[96:111]\n"
"ds_read_b64_tr_b16 v[208:209], v1 offset:20480\n"
"ds_read_b64_tr_b16 v[210:211], v1 offset:21504\n"
"ds_read_b64_tr_b16 v[212:213], v2 offset:20480\n"
"ds_read_b64_tr_b16 v[214:215], v2 offset:21504\n"
"s_waitcnt vmcnt(11) lgkmcnt(8)\n"
"v_mfma_f32_32x32x16_bf16 v[112:127], v[196:199], v[144:147], v[112:127]\n"
"v_mfma_f32_32x32x16_bf16 v[96:111], v[192:195], v[144:147], v[96:111]\n"
"ds_read_b64_tr_b16 v[216:217], v1 offset:22528\n"
"ds_read_b64_tr_b16 v[218:219], v1 offset:23552\n"
"ds_read_b64_tr_b16 v[220:221], v2 offset:22528\n"
"ds_read_b64_tr_b16 v[222:223], v2 offset:23552\n"
"s_waitcnt vmcnt(10) lgkmcnt(8)\n"
"v_mfma_f32_32x32x16_bf16 v[112:127], v[204:207], v[148:151], v[112:127]\n"
"v_mfma_f32_32x32x16_bf16 v[96:111], v[200:203], v[148:151], v[96:111]\n"
"ds_read_b64_tr_b16 v[192:193], v1 offset:32768\n"
"ds_read_b64_tr_b16 v[194:195], v1 offset:33792\n"
"ds_read_b64_tr_b16 v[196:197], v2 offset:32768\n"
"ds_read_b64_tr_b16 v[198:199], v2 offset:33792\n"
"s_waitcnt vmcnt(9) lgkmcnt(8)\n"
"v_mfma_f32_32x32x16_bf16 v[112:127], v[212:215], v[152:155], v[112:127]\n"
"v_mfma_f32_32x32x16_bf16 v[96:111], v[208:211], v[152:155], v[96:111]\n"
"ds_read_b64_tr_b16 v[200:201], v1 offset:34816\n"
"ds_read_b64_tr_b16 v[202:203], v1 offset:35840\n"
"ds_read_b64_tr_b16 v[204:205], v2 offset:34816\n"
"ds_read_b64_tr_b16 v[206:207], v2 offset:35840\n"
"s_waitcnt vmcnt(8) lgkmcnt(8)\n"
"v_mfma_f32_32x32x16_bf16 v[112:127], v[220:223], v[156:159], v[112:127]\n"
"v_mfma_f32_32x32x16_bf16 v[96:111], v[216:219], v[156:159], v[96:111]\n"
"ds_read_b64_tr_b16 v[208:209], v1 offset:36864\n"
"ds_read_b64_tr_b16 v[210:211], v1 offset:37888\n"
"ds_read_b64_tr_b16 v[212:213], v2 offset:36864\n"
"ds_read_b64_tr_b16 v[214:215], v2 offset:37888\n"
"s_waitcnt vmcnt(7) lgkmcnt(8)\n"
"v_mfma_f32_32x32x16_bf16 v[112:127], v[196:199], v[160:163], v[112:127]\n"
"v_mfma_f32_32x32x16_bf16 v[96:111], v[192:195], v[160:163], v[96:111]\n"
"ds_read_b64_tr_b16 v[216:217], v1 offset:38912\n"
"ds_read_b64_tr_b16 v[218:219], v1 offset:39936\n"
"ds_read_b64_tr_b16 v[220:221], v2 offset:38912\n"
"ds_read_b64_tr_b16 v[222:223], v2 offset:39936\n"
"s_waitcnt vmcnt(6) lgkmcnt(8)\n"
"v_mfma_f32_32x32x16_bf16 v[112:127], v[204:207], v[164:167], v[112:127]\n"
"v_mfma_f32_32x32x16_bf16 v[96:111], v[200:203], v[164:167], v[96:111]\n"
"ds_read_b64_tr_b16 v[192:193], v1 offset:49152\n"
"ds_read_b64_tr_b16 v[194:195], v1 offset:50176\n"
"ds_read_b64_tr_b16 v[196:197], v2 offset:49152\n"
"ds_read_b64_tr_b16 v[198:199], v2 offset:50176\n"
"s_waitcnt vmcnt(5) lgkmcnt(8)\n"
"v_mfma_f32_32x32x16_bf16 v[112:127], v[212:215], v[168:171], v[112:127]\n"
"v_mfma_f32_32x32x16_bf16 v[96:111], v[208:211], v[168:171], v[96:111]\n"
"ds_read_b64_tr_b16 v[200:201], v1 offset:51200\n"
"ds_read_b64_tr_b16 v[202:203], v1 offset:52224\n"
"ds_read_b64_tr_b16 v[204:205], v2 offset:51200\n"
"ds_read_b64_tr_b16 v[206:207], v2 offset:52224\n"
"s_waitcnt vmcnt(4) lgkmcnt(8)\n"
"v_mfma_f32_32x32x16_bf16 v[112:127], v[220:223], v[172:175], v[112:127]\n"
"v_mfma_f32_32x32x16_bf16 v[96:111], v[216:219], v[172:175], v[96:111]\n"
"ds_read_b64_tr_b16 v[208:209], v1 offset:53248\n"
"ds_read_b64_tr_b16 v[210:211], v1 offset:54272\n"
"ds_read_b64_tr_b16 v[212:213], v2 offset:53248\n"
"ds_read_b64_tr_b16 v[214:215], v2 offset:54272\n"
"ds_read_b64_tr_b16 v[216:217], v1 offset:55296\n"
"ds_read_b64_tr_b16 v[218:219], v1 offset:56320\n"
"ds_read_b64_tr_b16 v[220:221], v2 offset:55296\n"
"s_waitcnt lgkmcnt(14)\n"
"ds_read_b64_tr_b16 v[222:223], v2 offset:56320\n"
"s_waitcnt vmcnt(3) lgkmcnt(12)\n"
"v_mfma_f32_32x32x16_bf16 v[112:127], v[196:199], v[176:179], v[112:127]\n"
"s_waitcnt vmcnt(2) lgkmcnt(8)\n"
"v_mfma_f32_32x32x16_bf16 v[112:127], v[204:207], v[180:183], v[112:127]\n"
"s_waitcnt vmcnt(1) lgkmcnt(4)\n"
"v_mfma_f32_32x32x16_bf16 v[112:127], v[212:215], v[184:187], v[112:127]\n"
"s_waitcnt vmcnt(0) lgkmcnt(0)\n"
"v_mfma_f32_32x32x16_bf16 v[112:127], v[220:223], v[188:191], v[112:127]\n"
"v_mfma_f32_32x32x16_bf16 v[96:111], v[192:195], v[176:179], v[96:111]\n"
"v_mfma_f32_32x32x16_bf16 v[96:111], v[200:203], v[180:183], v[96:111]\n"
"v_mfma_f32_32x32x16_bf16 v[96:111], v[208:211], v[184:187], v[96:111]\n"
"v_mfma_f32_32x32x16_bf16 v[96:111], v[216:219], v[188:191], v[96:111]\n"
"s_nop 7\n"
"ds_write_b128 v10, v[112:115] offset:0\n"
"ds_write_b128 v10, v[116:119] offset:1024\n"
"ds_write_b128 v10, v[120:123] offset:2048\n"
"ds_write_b128 v10, v[124:127] offset:3072\n"
"ds_write_b128 v11, v[96:99] offset:0\n"
"ds_write_b128 v11, v[100:103] offset:1024\n"
"s_waitcnt lgkmcnt(0)\n"
"s_barrier\n"
"ds_read_b32 v72, v17 offset:0\n"
"ds_read_b32 v73, v17 offset:18432\n"
"ds_read_b32 v74, v17 offset:36864\n"
"ds_read_b32 v75, v17 offset:55296\n"
"ds_read_b128 v[32:35], v18 offset:0\n"
"ds_read_b128 v[36:39], v18 offset:1024\n"
"ds_read_b128 v[40:43], v19 offset:0\n"
"ds_read_b128 v[44:47], v19 offset:1024\n"
"ds_read_b128 v[48:51], v20 offset:0\n"
"ds_read_b128 v[52:55], v20 offset:1024\n"
"ds_read_b128 v[64:67], v22 offset:0\n"
"ds_read_b128 v[68:71], v22 offset:1024\n"
"ds_read_b128 v[56:59], v21 offset:0\n"
"ds_read_b128 v[60:63], v21 offset:1024\n"
"s_waitcnt lgkmcnt(12)\n"
"v_add_f32 v72, v72, v73\n"
"s_waitcnt lgkmcnt(10)\n"
"v_add_f32 v74, v74, v75\n"
"v_add_f32 v72, v72, v74\n"
"v_mul_f32 v72, 0xbfb8aa3b, v72\n"
"v_exp_f32 v72, v72\n"
"s_nop 0\n"
"v_add_f32 v72, 1.0, v72\n"
"v_rcp_f32 v72, v72\n"
"s_waitcnt lgkmcnt(7)\n"
"v_add_f32 v32, v32, v40\n"
"v_add_f32 v33, v33, v41\n"
"v_add_f32 v34, v34, v42\n"
"v_add_f32 v35, v35, v43\n"
"s_waitcnt lgkmcnt(6)\n"
"v_add_f32 v36, v36, v44\n"
"v_add_f32 v37, v37, v45\n"
"v_add_f32 v38, v38, v46\n"
"v_add_f32 v39, v39, v47\n"
"s_waitcnt lgkmcnt(5)\n"
"v_add_f32 v32, v32, v48\n"
"v_add_f32 v33, v33, v49\n"
"v_add_f32 v34, v34, v50\n"
"v_add_f32 v35, v35, v51\n"
"s_waitcnt lgkmcnt(4)\n"
"v_add_f32 v36, v36, v52\n"
"v_add_f32 v37, v37, v53\n"
"v_add_f32 v38, v38, v54\n"
"v_add_f32 v39, v39, v55\n"
"s_waitcnt lgkmcnt(3)\n"
"v_add_f32 v32, v32, v64\n"
"v_add_f32 v33, v33, v65\n"
"v_add_f32 v34, v34, v66\n"
"v_add_f32 v35, v35, v67\n"
"s_waitcnt lgkmcnt(2)\n"
"v_add_f32 v36, v36, v68\n"
"v_add_f32 v37, v37, v69\n"
"v_add_f32 v38, v38, v70\n"
"v_add_f32 v39, v39, v71\n"
"s_waitcnt lgkmcnt(1)\n"
"v_add_f32 v104, v104, v56\n"
"v_add_f32 v105, v105, v57\n"
"v_add_f32 v106, v106, v58\n"
"v_add_f32 v107, v107, v59\n"
"s_waitcnt lgkmcnt(0)\n"
"v_add_f32 v108, v108, v60\n"
"v_add_f32 v109, v109, v61\n"
"v_add_f32 v110, v110, v62\n"
"v_add_f32 v111, v111, v63\n"
"v_sub_f32 v104, v104, v32\n"
"v_sub_f32 v105, v105, v33\n"
"v_sub_f32 v106, v106, v34\n"
"v_sub_f32 v107, v107, v35\n"
"v_sub_f32 v108, v108, v36\n"
"v_sub_f32 v109, v109, v37\n"
"v_sub_f32 v110, v110, v38\n"
"v_sub_f32 v111, v111, v39\n"
"v_fma_f32 v104, v72, v104, v32\n"
"v_fma_f32 v105, v72, v105, v33\n"
"v_fma_f32 v106, v72, v106, v34\n"
"v_fma_f32 v107, v72, v107, v35\n"
"v_fma_f32 v108, v72, v108, v36\n"
"v_fma_f32 v109, v72, v109, v37\n"
"v_fma_f32 v110, v72, v110, v38\n"
"v_fma_f32 v111, v72, v111, v39\n"
"v_cvt_pk_bf16_f32 v104, v104, v105\n"
"v_cvt_pk_bf16_f32 v105, v106, v107\n"
"v_cvt_pk_bf16_f32 v106, v108, v109\n"
"v_cvt_pk_bf16_f32 v107, v110, v111\n"
"s_nop 0\n"
"v_permlane32_swap_b32 v104, v106\n"
"v_permlane32_swap_b32 v105, v107\n"
"buffer_store_dwordx4 v[104:107], v12, s[36:39], s72 offen offset:32\n"
"s_endpgm\n"
"LPAD14:\n"
"s_cbranch_execz LBKB\n"
"LTCB:\n"
"LCB_slow_1:\n"
"s_mov_b32 s50, 131072\n"
"LCB_slow_1l:\n"
"buffer_load_dwordx2 v[8:9], v4, s[32:35], 0 offen sc1\n"
"s_waitcnt vmcnt(0)\n"
"v_cmp_eq_u64 vcc, s[40:41], v[8:9]\n"
"s_nop 1\n"
"s_and_b32 s49, vcc_lo, 0xf\n"
"s_cmp_eq_u32 s49, 0xf\n"
"s_cbranch_scc1 LCB_back_2\n"
"s_sleep 1\n"
"s_sub_u32 s50, s50, 1\n"
"s_cmp_lg_u32 s50, 0\n"
"s_cbranch_scc1 LCB_slow_1l\n"
"s_branch LCB_back_2\n"
"LEND:\n"
".p2align 12\n"
   :: "{s[0:1]}"(__builtin_amdgcn_kernarg_segment_ptr()), "{s2}"(blockIdx.x), "{v0}"(threadIdx.x),
      "s"((uint32_t)(uintptr_t)lds)
   : "memory", "v255", "s101", "vcc");
}
extern "C" void kernel_launch(void* const* d_in, const int* in_sizes, int n_in, void* d_out, int out_size,
                              void* d_ws, size_t ws_size, hipStream_t stream) {
  Args a;
  const void* ptr[9] = {d_in[0], d_in[1], d_in[4], d_in[5], d_in[6], d_in[2], d_ws, d_out, d_in[7]};
  const uint32_t wsb = ws_size < (size_t)33554432 ? (uint32_t)ws_size : (uint32_t)33554432;
  const uint32_t nb[9] = {2048u * 512 * 2, 512u * 1536 * 2, 512u * 1792 * 2, 512u * 1792 * 2, 1792u * 512 * 2,
                          512u * 512 * 2, wsb, 2048u * 512 * 2, 1024u};
  for (int i = 0; i < 9; i++) { a.d[i].p = ptr[i]; a.d[i].n = nb[i]; a.d[i].f = 0x00020000u; }
  fused_k<<<256, 512, 0, stream>>>(a);
}
#define AB_UNSCALED 1
```
